```python
import torch

def _softmax(x, axis=-1):
    return torch.softmax(x, dim=axis)
def _gelu(x):
    return torch.nn.functional.gelu(x, approximate="tanh")
def _silu(x):
    return torch.nn.functional.silu(x)
def _sigmoid(x):
    return torch.sigmoid(x)
def _layernorm(x, axis=-1, eps=1e-5):
    m = x.mean(dim=axis, keepdim=True)
    v = x.var(dim=axis, keepdim=True, unbiased=False)
    return (x - m) / torch.sqrt(v + eps)
def _scatter_add(tgt, idx, src, axis=0):
    return tgt.clone().index_add_(axis, idx, src)
def _segment_sum(src, seg, n):
    out = torch.zeros((n,) + src.shape[1:], dtype=src.dtype)
    return out.index_add_(0, seg, src)
def _pad(x, pw):
    flat = tuple(v for pair in reversed(pw) for v in pair)
    return torch.nn.functional.pad(x, flat)

kernel_name = 'parallel_sum__ssm_conv_s5__geglu_ffn__prompt'

def setup_inputs():

    _w = (lambda a: (lambda t: t.to(torch.bfloat16) if t.is_floating_point() else t)(torch.as_tensor(a)))
    def _A_setup():
        B, S, D = 1, 2048, 640
        P = D
        Lambda_re = -0.5 * (lambda _x: torch.exp(torch.as_tensor(_x)))((lambda *s: torch.randn(*s, dtype=torch.bfloat16) * 0.8)(P))
        Lambda_im = (lambda *s: torch.randn(*s, dtype=torch.bfloat16) * 0.8)(P) * 1.0
        B_re = (lambda *s: torch.randn(*s, dtype=torch.bfloat16) * 0.8)(P, D) * (D ** -0.5)
        B_im = (lambda *s: torch.randn(*s, dtype=torch.bfloat16) * 0.8)(P, D) * (D ** -0.5)
        C_re = (lambda *s: torch.randn(*s, dtype=torch.bfloat16) * 0.8)(D, P) * (P ** -0.5)
        C_im = (lambda *s: torch.randn(*s, dtype=torch.bfloat16) * 0.8)(D, P) * (P ** -0.5)
        log_dt = (lambda *s: torch.randn(*s, dtype=torch.bfloat16) * 0.8)(P) * 0.3 - 2.0
        Dskip = (lambda *s: torch.randn(*s, dtype=torch.bfloat16) * 0.8)(D)
        return {
            "x": _w((lambda *s: torch.randn(*s, dtype=torch.bfloat16) * 0.05)(B, S, D)),
            "Lambda_re": _w(Lambda_re),
            "Lambda_im": _w(Lambda_im),
            "B_re": _w(B_re),
            "B_im": _w(B_im),
            "C_re": _w(C_re),
            "C_im": _w(C_im),
            "log_dt": _w(log_dt),
            "Dskip": _w(Dskip),
        }
    _A_in = _A_setup()

    def _B_setup():
        B, S, D, F = 1, 2048, 640, 1536
        return {
            "x": _w((lambda *s: torch.randn(*s, dtype=torch.bfloat16) * 0.05)(B, S, D)),
            "w_gate": _w((lambda *s: torch.randn(*s, dtype=torch.bfloat16) * 0.05)(D, F)),
            "w_up": _w((lambda *s: torch.randn(*s, dtype=torch.bfloat16) * 0.05)(D, F)),
            "w_down": _w((lambda *s: torch.randn(*s, dtype=torch.bfloat16) * 0.05)(F, D)),
        }
    _B_in = _B_setup()

    return {
        "A_x": _A_in["x"],
        "A_Lambda_re": _A_in["Lambda_re"],
        "A_Lambda_im": _A_in["Lambda_im"],
        "A_B_re": _A_in["B_re"],
        "A_B_im": _A_in["B_im"],
        "A_C_re": _A_in["C_re"],
        "A_C_im": _A_in["C_im"],
        "A_log_dt": _A_in["log_dt"],
        "A_Dskip": _A_in["Dskip"],
        "B_x": _B_in["x"],
        "B_w_gate": _B_in["w_gate"],
        "B_w_up": _B_in["w_up"],
        "B_w_down": _B_in["w_down"],
    }

def reference(A_x, A_Lambda_re, A_Lambda_im, A_B_re, A_B_im, A_C_re, A_C_im, A_log_dt, A_Dskip, B_x, B_w_gate, B_w_up, B_w_down):
    def _A_ref(x, Lambda_re, Lambda_im, B_re, B_im, C_re, C_im, log_dt, Dskip):
        Bsz, L, H = x.shape
        Lam = Lambda_re + 1j * Lambda_im
        Bmat = B_re + 1j * B_im
        Cmat = C_re + 1j * C_im
        dt = torch.exp(log_dt)
        Lam_bar = torch.exp(dt * Lam)
        B_bar = ((Lam_bar - 1.0) / Lam)[:, None] * Bmat
        Bu = torch.einsum("bld,pd->blp", x + 0j, B_bar)
        h = torch.zeros_like(Bu[:, 0, :])
        hs = []
        for t in range(L):
            h = Lam_bar[None, :] * h + Bu[:, t, :]
            hs.append(h)
        hseq = torch.stack(hs, 1)
        y = 2.0 * torch.einsum("blp,dp->bld", hseq, Cmat).real + x * Dskip[None, None, :]
        return y

    def _B_ref(x, w_gate, w_up, w_down):
        return (_gelu(x @ w_gate) * (x @ w_up)) @ w_down

    x = A_x
    xn = _layernorm(x, axis=-1)
    return x + _A_ref(xn, A_Lambda_re, A_Lambda_im, A_B_re, A_B_im, A_C_re, A_C_im, A_log_dt, A_Dskip) + _B_ref(xn, B_w_gate, B_w_up, B_w_down)
```

```cpp
#include <hip/hip_runtime.h>
#include <hip/hip_bf16.h>
typedef __hip_bfloat16 bf16;
constexpr int L=2048, D=640, P=640, F=1536;
__device__ inline float b2f(bf16 v){return __bfloat162float(v);}
__device__ inline float rb(float v){return __bfloat162float(__float2bfloat16(v));}
__global__ void ln_k(const bf16* x, bf16* xn){
  int row=blockIdx.x; int t=threadIdx.x;
  float v[10]; float s=0;
  for(int i=0;i<10;i++){v[i]=b2f(x[row*D+i*64+t]); s+=v[i];}
  for(int o=32;o;o>>=1)s+=__shfl_xor(s,o);
  float mean=s/D; float q=0;
  for(int i=0;i<10;i++){float d=v[i]-mean;q+=d*d;}
  for(int o=32;o;o>>=1)q+=__shfl_xor(q,o);
  float var=rb(q/D); float m=rb(mean);
  float sd=rb(sqrtf(rb(var+1e-5f)));
  for(int i=0;i<10;i++) xn[row*D+i*64+t]=__float2bfloat16(rb(v[i]-m)/sd);
}
__global__ void bu_k(const bf16* xn,const bf16* Bre,const bf16* Bim,float* bur,float* bui){
  int p=blockIdx.x*64+threadIdx.x; int l=blockIdx.y;
  float a=0,b=0;
  for(int d=0;d<D;d++){float xv=b2f(xn[l*D+d]); a+=xv*b2f(Bre[p*D+d]); b+=xv*b2f(Bim[p*D+d]);}
  bur[l*P+p]=a; bui[l*P+p]=b;
}
__global__ void scan_k(const bf16* Lre,const bf16* Lim,const bf16* ldt,float* bur,float* bui){
  int p=blockIdx.x*64+threadIdx.x;
  float dt=rb(expf(b2f(ldt[p]))); float lr=b2f(Lre[p]), li=b2f(Lim[p]);
  float er=expf(dt*lr); float ar=er*cosf(dt*li), ai=er*sinf(dt*li);
  float nr=ar-1.f, ni=ai; float den=lr*lr+li*li;
  float cr=(nr*lr+ni*li)/den, ci=(ni*lr-nr*li)/den;
  float hr=0,hi=0;
  for(int l=0;l<L;l++){float br=bur[l*P+p],bi=bui[l*P+p];
    float ur=cr*br-ci*bi, ui=cr*bi+ci*br;
    float nhr=ar*hr-ai*hi+ur, nhi=ar*hi+ai*hr+ui; hr=nhr;hi=nhi;
    bur[l*P+p]=rb(hr); bui[l*P+p]=rb(hi);}
}
__device__ inline float gelu(float x){ float u=0.7978845608028654f*(x+0.044715f*x*x*x); return 0.5f*x*(1.f+tanhf(u)); }
__global__ void act_k(const bf16* xn,const bf16* wg,const bf16* wu,bf16* act){
  int f=blockIdx.x*64+threadIdx.x; int l=blockIdx.y;
  float a=0,b=0;
  for(int d=0;d<D;d++){float xv=b2f(xn[l*D+d]); a+=xv*b2f(wg[d*F+f]); b+=xv*b2f(wu[d*F+f]);}
  act[l*F+f]=__float2bfloat16(rb(gelu(rb(a)))*rb(b));
}
__global__ void out_k(const bf16* x,const bf16* xn,const float* hr,const float* hi,const bf16* Cre,const bf16* Cim,const bf16* Dk,const bf16* act,const bf16* wd,float* out){
  int d=blockIdx.x*64+threadIdx.x; int l=blockIdx.y;
  float y=0; for(int p=0;p<P;p++) y+=hr[l*P+p]*b2f(Cre[d*P+p])-hi[l*P+p]*b2f(Cim[d*P+p]);
  float o=0; for(int f=0;f<F;f++) o+=b2f(act[l*F+f])*b2f(wd[f*D+d]);
  float A=2.f*y+rb(b2f(xn[l*D+d])*b2f(Dk[d]));
  out[l*D+d]=b2f(x[l*D+d])+A+rb(o);
}
extern "C" void kernel_launch(void* const* d_in, const int* in_sizes, int n_in, void* d_out, int out_size, void* d_ws, size_t ws_size, hipStream_t stream){
  const bf16* x=(const bf16*)d_in[0];
  char* ws=(char*)d_ws;
  bf16* xn=(bf16*)ws; float* bur=(float*)(ws+(4<<20)); float* bui=(float*)(ws+(12<<20)); bf16* act=(bf16*)(ws+(20<<20));
  ln_k<<<L,64,0,stream>>>(x,xn);
  bu_k<<<dim3(P/64,L),64,0,stream>>>(xn,(const bf16*)d_in[3],(const bf16*)d_in[4],bur,bui);
  scan_k<<<P/64,64,0,stream>>>((const bf16*)d_in[1],(const bf16*)d_in[2],(const bf16*)d_in[7],bur,bui);
  act_k<<<dim3(F/64,L),64,0,stream>>>(xn,(const bf16*)d_in[10],(const bf16*)d_in[11],act);
  out_k<<<dim3(D/64,L),64,0,stream>>>(x,xn,bur,bui,(const bf16*)d_in[5],(const bf16*)d_in[6],(const bf16*)d_in[8],act,(const bf16*)d_in[12],(float*)d_out);
}
```

```cpp
#include <hip/hip_runtime.h>
struct KArgs { const void* p[14]; };
__global__ __launch_bounds__(256,1) void fused_main(KArgs args){
  __shared__ __attribute__((aligned(16))) char lds[163840];
  int wid=__builtin_amdgcn_readfirstlane(threadIdx.x>>6);
  asm volatile(
"s_load_dwordx16 s[8:23], %0, 0x0\n"
"s_load_dwordx8 s[24:31], %0, 0x40\n"
"s_load_dwordx4 s[32:35], %0, 0x60\n"
"v_mbcnt_lo_u32_b32 v255, -1, 0\n"
"v_mbcnt_hi_u32_b32 v255, -1, v255\n"
"s_mov_b32 s76, %2\n"
"s_and_b32 s84, %1, 7\n"
"s_lshr_b32 s85, %1, 3\n"
"s_getpc_b64 s[90:91]\n"
"PCREF_%=:\n"
"s_lshl_b32 s86, s85, 2\n"
"s_add_u32 s86, s86, s76\n"
"s_lshl_b32 s86, s86, 9\n"
"v_and_b32 v250, 3, v255\n"
"v_lshlrev_b32 v250, 7, v250\n"
"v_add_u32 v250, s86, v250\n"
"v_min_u32 v250, KEND_%=-PCREF_%=-4, v250\n"
"global_load_dword v250, v250, s[90:91]\n"
"s_lshr_b32 s77, s84, 2\n"
"s_lshl_b32 s77, s77, 3\n"
"s_and_b32 s87, s85, 7\n"
"s_add_u32 s77, s77, s87\n"
"s_and_b32 s94, s84, 3\n"
"s_lshl_b32 s94, s94, 2\n"
"s_lshr_b32 s87, s85, 3\n"
"s_add_u32 s94, s94, s87\n"
"s_lshr_b32 s78, s94, 1\n"
"s_mul_i32 s79, s76, 40960\n"
"s_mov_b32 s87, 0\n"
"s_lshl_b32 s56, s87, 7\n"
"v_lshrrev_b32 v244, 3, v255\n"
"v_lshrrev_b32 v245, 2, v244\n"
"v_and_b32 v246, 3, v244\n"
"v_lshl_add_u32 v245, v245, 4, v246\n"
"s_lshl_b32 s88, s77, 7\n"
"s_lshl_b32 s89, s76, 5\n"
"s_add_u32 s88, s88, s89\n"
"s_mul_i32 s88, s88, 1280\n"
"v_mul_u32_u24 v245, 1280, v245\n"
"v_add_u32 v245, s88, v245\n"
"v_lshrrev_b32 v246, 1, v244\n"
"v_and_b32 v247, 7, v255\n"
"v_xor_b32 v246, v246, v247\n"
"v_lshl_add_u32 v251, v246, 4, v245\n"
"v_xor_b32 v247, 4, v246\n"
"v_lshl_add_u32 v252, v247, 4, v245\n"
"v_add_u32 v252, 4096, v252\n"
"v_add_u32 v253, 8192, v251\n"
"v_add_u32 v254, 8192, v252\n"
"s_mov_b32 s38, 2621440\n"
"s_mov_b32 s39, 0x20000\n"
"s_waitcnt lgkmcnt(0)\n"
"s_mov_b32 s36, s8\n"
"s_and_b32 s37, s9, 0xffff\n"
"s_add_u32 m0, s79, 24576\n"
"s_nop 0\n"
"buffer_load_dwordx4 v251, s[36:39], s56 offen lds\n"
"buffer_load_dwordx4 v252, s[36:39], s56 offen offset:1024 lds\n"
"buffer_load_dwordx4 v253, s[36:39], s56 offen offset:2048 lds\n"
"buffer_load_dwordx4 v254, s[36:39], s56 offen offset:3072 lds\n"
"s_load_dwordx2 s[80:81], s[34:35], 0x0\n"
"s_add_u32 s57, s56, 128\n"
"s_cmp_eq_u32 s57, 1280\n"
"s_cselect_b32 s57, 0, s57\n"
"s_add_u32 s58, s57, 128\n"
"s_cmp_eq_u32 s58, 1280\n"
"s_cselect_b32 s58, 0, s58\n"
"s_add_u32 s59, s58, 128\n"
"s_cmp_eq_u32 s59, 1280\n"
"s_cselect_b32 s59, 0, s59\n"
"s_add_u32 s60, s59, 128\n"
"s_cmp_eq_u32 s60, 1280\n"
"s_cselect_b32 s60, 0, s60\n"
"s_add_u32 s61, s60, 128\n"
"s_cmp_eq_u32 s61, 1280\n"
"s_cselect_b32 s61, 0, s61\n"
"s_add_u32 s62, s61, 128\n"
"s_cmp_eq_u32 s62, 1280\n"
"s_cselect_b32 s62, 0, s62\n"
"s_add_u32 s63, s62, 128\n"
"s_cmp_eq_u32 s63, 1280\n"
"s_cselect_b32 s63, 0, s63\n"
"s_add_u32 s64, s63, 128\n"
"s_cmp_eq_u32 s64, 1280\n"
"s_cselect_b32 s64, 0, s64\n"
"s_add_u32 s65, s64, 128\n"
"s_cmp_eq_u32 s65, 1280\n"
"s_cselect_b32 s65, 0, s65\n"
"s_bitcmp1_b32 s94, 0\n"
"s_cbranch_scc1 TYPEB_%=\n"
"s_lshl_b32 s40, s77, 3\n"
"s_add_u32 s40, s40, s78\n"
"s_lshl_b32 s40, s40, 2\n"
"s_add_u32 s40, s40, s76\n"
"s_mul_i32 s41, s40, 205\n"
"s_lshr_b32 s41, s41, 11\n"
"s_mul_i32 s86, s41, 10\n"
"s_sub_u32 s52, s40, s86\n"
"s_min_u32 s41, s41, 39\n"
"v_lshrrev_b32 v28, 3, v255\n"
"v_and_b32 v29, 7, v255\n"
"v_mul_u32_u24 v80, 1280, v28\n"
"v_lshl_add_u32 v80, v29, 4, v80\n"
"s_mul_i32 s86, s41, 20480\n"
"s_lshl_b32 s88, s52, 7\n"
"s_add_u32 s86, s86, s88\n"
"v_add_u32 v80, s86, v80\n"
"v_add_u32 v81, 10240, v80\n"
"v_lshlrev_b32 v184, 1, v255\n"
"v_add_u32 v184, s88, v184\n"
"global_load_ushort v76, v184, s[10:11]\n"
"global_load_ushort v77, v184, s[12:13]\n"
"s_lshl_b32 s86, s40, 2\n"
"v_lshrrev_b32 v193, 4, v255\n"
"v_add_u32 v193, s86, v193\n"
"global_load_ushort v78, v184, s[22:23]\n"
"v_mul_u32_u24 v184, 1280, v193\n"
"v_and_b32 v156, 15, v255\n"
"v_lshl_add_u32 v184, v156, 4, v184\n"
"buffer_load_dwordx4 v[140:143], v184, s[36:39], 0 offen\n"
"s_lshl_b32 s54, s77, 2\n"
"s_add_u32 s54, s54, s76\n"
"s_min_u32 s86, s54, 39\n"
"s_mul_i32 s87, s78, 40\n"
"s_add_u32 s90, s86, s87\n"
"s_mul_i32 s86, s90, 2560\n"
"v_lshrrev_b32 v4, 5, v255\n"
"v_mul_u32_u24 v4, 1280, v4\n"
"v_and_b32 v148, 15, v255\n"
"v_lshl_add_u32 v4, v148, 4, v4\n"
"v_add_u32 v4, s86, v4\n"
"v_and_b32 v148, 16, v255\n"
"v_cmp_eq_u32 vcc, 0, v148\n"
"v_mov_b32 v108, s14\n"
"v_mov_b32 v109, s15\n"
"v_mov_b32 v148, s16\n"
"buffer_load_dwordx4 v[144:147], v184, s[36:39], 0 offen offset:256\n"
"v_cndmask_b32 v108, v148, v108, vcc\n"
"v_mov_b32 v148, s17\n"
"v_cndmask_b32 v109, v148, v109, vcc\n"
"v_add_co_u32 v108, vcc, v108, v4\n"
"s_nop 1\n"
"v_addc_co_u32 v109, vcc, 0, v109, vcc\n"
"s_lshl_b32 s53, s78, 1\n"
"s_lshr_b32 s87, s77, 2\n"
"s_add_u32 s53, s53, s87\n"
"s_add_u32 s87, s78, 16\n"
"s_cmp_lt_u32 s77, 8\n"
"s_cselect_b32 s53, s53, s87\n"
"s_bitcmp0_b32 s77, 1\n"
"s_cselect_b32 s42, s26, s28\n"
"s_cselect_b32 s43, s27, s29\n"
"s_and_b32 s87, s77, 1\n"
"s_lshl_b32 s88, s87, 2\n"
"s_add_u32 s88, s88, s76\n"
"buffer_load_dwordx4 v[148:151], v184, s[36:39], 0 offen offset:512\n"
"s_mul_i32 s88, s88, 245760\n"
"s_lshl_b32 s53, s53, 7\n"
"s_add_u32 s88, s88, s53\n"
"v_lshrrev_b32 v137, 3, v255\n"
"v_and_b32 v130, 7, v255\n"
"v_mul_u32_u24 v129, 3072, v137\n"
"v_lshl_add_u32 v129, v130, 4, v129\n"
"v_add_u32 v129, s88, v129\n"
"s_cmp_lt_u32 s76, 2\n"
"s_cselect_b32 s44, s26, s28\n"
"s_cselect_b32 s45, s27, s29\n"
"v_lshrrev_b32 v8, 3, v255\n"
"v_and_b32 v185, 7, v255\n"
"s_and_b32 s84, s76, 1\n"
"s_lshl_b32 s85, s84, 3\n"
"s_and_b32 s45, s45, 0xffff\n"
"s_mov_b32 s46, 1966080\n"
"buffer_load_dwordx4 v[152:155], v184, s[36:39], 0 offen offset:768\n"
"s_mov_b32 s47, 0x20000\n"
"s_mul_i32 s66, s56, 1536\n"
"s_mul_i32 s67, s57, 1536\n"
"s_mul_i32 s68, s58, 1536\n"
"s_mul_i32 s69, s59, 1536\n"
"s_mul_i32 s70, s60, 1536\n"
"s_mul_i32 s71, s61, 1536\n"
"s_mul_i32 s72, s62, 1536\n"
"s_mul_i32 s73, s63, 1536\n"
"s_mul_i32 s74, s64, 1536\n"
"s_mul_i32 s75, s65, 1536\n"
"v_add_u32 v36, s85, v8\n"
"v_mul_u32_u24 v36, 3072, v36\n"
"v_and_b32 v135, 2, v8\n"
"s_and_b32 s86, s76, 2\n"
"s_lshl_b32 s86, s86, 1\n"
"v_or_b32 v135, s86, v135\n"
"buffer_load_dwordx4 v[8:11], v184, s[36:39], 0 offen offset:1024\n"
"v_xor_b32 v135, v135, v185\n"
"v_lshl_add_u32 v36, v135, 4, v36\n"
"s_lshl_b32 s86, s78, 8\n"
"v_add_u32 v184, s86, v36\n"
"v_add_u32 v185, 48128, v184\n"
"v_add_u32 v186, 96256, v184\n"
"v_add_u32 v187, 144384, v184\n"
"v_lshlrev_b32 v189, 4, v255\n"
"s_lshl_b32 s86, s86, 5\n"
"s_add_u32 s86, s86, 442368\n"
"v_add_u32 v16, s86, v189\n"
"s_mov_b32 s82, 0xffff0000\n"
"s_mov_b32 s83, 0x3f803f80\n"
"s_mov_b32 s48, s34\n"
"s_and_b32 s49, s35, 0xffff\n"
"s_mov_b32 s50, 0x1400000\n"
"global_load_dwordx4 v[12:15], v[108:109], off\n"
"s_mov_b32 s51, 0x20000\n"
"v_and_b32 v36, 31, v255\n"
"v_lshrrev_b32 v138, 5, v255\n"
"v_bfe_u32 v124, v255, 1, 3\n"
"v_xor_b32 v124, v138, v124\n"
"v_lshlrev_b32 v124, 4, v124\n"
"v_lshl_add_u32 v36, v36, 7, v124\n"
"v_add_u32 v124, s79, v36\n"
"v_xor_b32 v125, 32, v124\n"
"v_xor_b32 v126, 64, v124\n"
"v_xor_b32 v127, 96, v124\n"
"v_bfe_u32 v36, v255, 4, 1\n"
"v_lshl_add_u32 v135, v36, 1, v138\n"
"v_mul_u32_u24 v135, 40960, v135\n"
"v_bfe_u32 v136, v255, 2, 2\n"
"global_load_dwordx4 v[20:23], v[108:109], off offset:256\n"
"v_lshl_add_u32 v135, v136, 7, v135\n"
"v_lshrrev_b32 v136, 1, v136\n"
"v_lshl_add_u32 v136, v36, 1, v136\n"
"v_lshl_add_u32 v135, v136, 5, v135\n"
"v_and_b32 v136, 3, v255\n"
"v_lshl_add_u32 v36, v136, 3, v135\n"
"v_xor_b32 v37, 32, v36\n"
"v_xor_b32 v38, 64, v36\n"
"v_xor_b32 v39, 96, v36\n"
"v_lshlrev_b32 v135, 5, v29\n"
"v_lshl_add_u32 v136, v29, 3, v28\n"
"v_lshlrev_b32 v136, 2, v136\n"
"v_and_b32 v28, 1, v28\n"
"v_lshl_add_u32 v190, v28, 4, v29\n"
"s_lshl_b32 s88, s52, 2\n"
"global_load_dwordx4 v[24:27], v[108:109], off offset:512\n"
"v_lshrrev_b32 v28, 4, v255\n"
"v_add_u32 v28, s88, v28\n"
"v_mul_u32_u24 v29, 205, v28\n"
"v_lshrrev_b32 v29, 10, v29\n"
"v_mul_u32_u24 v29, 6, v29\n"
"v_add_u32 v28, v28, v29\n"
"v_mul_u32_u24 v28, 5120, v28\n"
"v_lshl_add_u32 v190, v190, 4, v28\n"
"s_mul_i32 s87, s41, 205\n"
"s_lshr_b32 s87, s87, 10\n"
"s_mul_i32 s87, s87, 445440\n"
"s_lshl_b32 s86, s41, 10\n"
"s_add_u32 s86, s86, s87\n"
"s_add_u32 s86, s86, 12134400\n"
"s_cmp_lt_u32 s40, 400\n"
"s_cselect_b32 s86, s86, 0x7f000000\n"
"global_load_dwordx4 v[28:31], v[108:109], off offset:768\n"
"v_add_u32 v190, s86, v190\n"
"s_lshl_b32 s91, s40, 7\n"
"s_add_u32 s91, s91, 65536\n"
"s_lshl_b32 s89, s53, 5\n"
"s_and_b32 s87, s77, 1\n"
"s_lshl_b32 s87, s87, 9\n"
"s_add_u32 s89, s89, s87\n"
"s_and_b32 s87, s77, 2\n"
"s_lshl_b32 s87, s87, 7\n"
"s_add_u32 s89, s89, s87\n"
"s_add_u32 s89, s89, 442368\n"
"s_cmp_lt_u32 s77, 12\n"
"s_cselect_b32 s87, s76, 1\n"
"s_cmp_eq_u32 s87, 0\n"
"s_cselect_b32 s89, s89, 0x7f000000\n"
"v_lshrrev_b32 v137, 4, v255\n"
"v_lshlrev_b32 v42, 10, v137\n"
"v_and_b32 v130, 15, v255\n"
"v_lshl_add_u32 v42, v130, 4, v42\n"
"v_add_u32 v42, s89, v42\n"
"v_lshrrev_b32 v137, 5, v255\n"
"v_lshlrev_b32 v40, 10, v137\n"
"v_bfe_u32 v130, v255, 2, 2\n"
"v_lshl_add_u32 v40, v130, 7, v40\n"
"v_bfe_u32 v137, v255, 4, 1\n"
"v_lshl_add_u32 v40, v137, 5, v40\n"
"v_and_b32 v137, 3, v255\n"
"v_lshlrev_b32 v137, 3, v137\n"
"v_add3_u32 v40, v40, v137, s79\n"
"v_lshlrev_b32 v137, 2, v255\n"
"v_add_u32 v137, 22528, v137\n"
"s_waitcnt lgkmcnt(0)\n"
"s_add_u32 s80, s80, 0x9e3779b1\n"
"s_add_u32 s81, s81, 0x85ebca77\n"
"s_add_u32 s16, s80, 1\n"
"s_mov_b32 s17, 0\n"
"s_and_b32 s8, s80, 0xbfffbfff\n"
"s_and_b32 s9, s81, 0xbfffbfff\n"
"v_mov_b32 v130, 0\n"
"v_mov_b32 v131, 0\n"
"v_mov_b32 v132, 0\n"
"v_mov_b32 v133, 0\n"
"v_mov_b32 v121, s80\n"
"v_mov_b32 v123, s81\n"
"v_cmp_eq_u32 vcc, 0, v156\n"
"v_lshlrev_b32 v193, 4, v193\n"
"v_add_u32 v193, 4096, v193\n"
"v_cndmask_b32 v193, -16, v193, vcc\n"
"v_mov_b32 v156, 0\n"
"v_mov_b32 v157, 0\n"
"v_mov_b32 v177, s8\n"
"v_mov_b32 v179, s9\n"
"s_lshl_b32 s86, s90, 5\n"
"s_add_u32 s86, s86, 40960\n"
"s_cmp_lt_u32 s54, 40\n"
"s_cselect_b32 s86, s86, 0x7f000000\n"
"v_bfe_u32 v134, v255, 4, 1\n"
"v_mul_u32_u24 v134, 10240, v134\n"
"v_lshrrev_b32 v4, 5, v255\n"
"v_lshl_add_u32 v134, v4, 4, v134\n"
"v_and_b32 v4, 15, v255\n"
"v_cmp_eq_u32 vcc, 0, v4\n"
"v_add_u32 v134, s86, v134\n"
"s_nop 0\n"
"v_cndmask_b32 v134, -16, v134, vcc\n"
"v_mov_b32 v4, s83\n"
"v_mov_b32 v5, s83\n"
"v_mov_b32 v6, s83\n"
"v_mov_b32 v7, s83\n"
"v_add_u32 v128, s79, v137\n"
"v_add_u32 v41, 81920, v137\n"
"v_mov_b32 v1, s8\n"
"v_mov_b32 v3, s9\n"
"global_load_dwordx4 v[32:35], v[108:109], off offset:1024\n"
"global_load_dwordx4 v[108:111], v80, s[18:19]\n"
"global_load_dwordx4 v[112:115], v80, s[20:21]\n"
"global_load_dwordx4 v[100:103], v81, s[18:19]\n"
"global_load_dwordx4 v[104:107], v81, s[20:21]\n"
"s_waitcnt vmcnt(14)\n"
"v_lshlrev_b32 v78, 16, v78\n"
"v_lshlrev_b32 v76, 16, v76\n"
"v_mul_f32 v78, 0x3fb8aa3b, v78\n"
"v_lshlrev_b32 v77, 16, v77\n"
"v_exp_f32 v78, v78\n"
"s_nop 0\n"
"v_cvt_pk_bf16_f32 v78, v78, v78\n"
"v_lshlrev_b32 v78, 16, v78\n"
"v_mul_f32 v80, v78, v76\n"
"v_mul_f32 v81, v78, v77\n"
"v_mul_f32 v80, 0x3fb8aa3b, v80\n"
"v_mul_f32 v81, 0x3e22f983, v81\n"
"v_exp_f32 v158, v80\n"
"v_fract_f32 v159, v81\n"
"v_cos_f32 v80, v159\n"
"s_add_u32 m0, s79, 12288\n"
"v_sin_f32 v81, v159\n"
"global_load_lds_dwordx4 v129, s[42:43]\n"
"v_add_u32 v129, 23552, v129\n"
"v_mul_f32 v80, v158, v80\n"
"v_mul_f32 v81, v158, v81\n"
"v_add_f32 v80, -1.0, v80\n"
"v_mul_f32 v79, v76, v76\n"
"v_fmac_f32 v79, v77, v77\n"
"v_rcp_f32 v79, v79\n"
"v_mul_f32 v158, v80, v76\n"
"v_mul_f32 v159, v80, v77\n"
"v_fmac_f32 v158, v81, v77\n"
"v_fma_f32 v159, -v81, v76, v159\n"
"v_add_f32 v79, v79, v79\n"
"v_mul_f32 v158, v79, v158\n"
"v_mul_f32 v159, v79, v159\n"
"ds_bpermute_b32 v76, v135, v158\n"
"global_load_lds_dwordx4 v129, s[42:43] offset:1024\n"
"v_add_u32 v129, 23552, v129\n"
"ds_bpermute_b32 v84, v135, v159\n"
"ds_bpermute_b32 v77, v135, v158 offset:4\n"
"ds_bpermute_b32 v85, v135, v159 offset:4\n"
"ds_bpermute_b32 v78, v135, v158 offset:8\n"
"ds_bpermute_b32 v86, v135, v159 offset:8\n"
"global_load_lds_dwordx4 v129, s[42:43] offset:2048\n"
"v_add_u32 v129, 23552, v129\n"
"ds_bpermute_b32 v79, v135, v158 offset:12\n"
"ds_bpermute_b32 v87, v135, v159 offset:12\n"
"ds_bpermute_b32 v80, v135, v158 offset:16\n"
"ds_bpermute_b32 v88, v135, v159 offset:16\n"
"ds_bpermute_b32 v81, v135, v158 offset:20\n"
"global_load_lds_dwordx4 v129, s[42:43] offset:3072\n"
"v_add_u32 v129, 27648, v129\n"
"ds_bpermute_b32 v89, v135, v159 offset:20\n"
"ds_bpermute_b32 v82, v135, v158 offset:24\n"
"ds_bpermute_b32 v90, v135, v159 offset:24\n"
"ds_bpermute_b32 v83, v135, v158 offset:28\n"
"s_add_u32 m0, s79, 16384\n"
"ds_bpermute_b32 v91, v135, v159 offset:28\n"
"global_load_lds_dwordx4 v129, s[42:43]\n"
"v_add_u32 v129, 23552, v129\n"
"s_waitcnt vmcnt(18)\n"
"v_dot2c_f32_bf16 v130, s83, v140\n"
"v_dot2c_f32_bf16 v132, v140, v140\n"
"v_dot2c_f32_bf16 v131, s83, v141\n"
"v_dot2c_f32_bf16 v133, v141, v141\n"
"v_dot2c_f32_bf16 v130, s83, v142\n"
"v_dot2c_f32_bf16 v132, v142, v142\n"
"v_dot2c_f32_bf16 v131, s83, v143\n"
"v_dot2c_f32_bf16 v133, v143, v143\n"
"s_waitcnt vmcnt(17)\n"
"v_dot2c_f32_bf16 v130, s83, v144\n"
"v_dot2c_f32_bf16 v132, v144, v144\n"
"v_dot2c_f32_bf16 v131, s83, v145\n"
"v_dot2c_f32_bf16 v133, v145, v145\n"
"v_dot2c_f32_bf16 v130, s83, v146\n"
"v_dot2c_f32_bf16 v132, v146, v146\n"
"v_dot2c_f32_bf16 v131, s83, v147\n"
"v_dot2c_f32_bf16 v133, v147, v147\n"
"s_waitcnt vmcnt(16)\n"
"v_dot2c_f32_bf16 v130, s83, v148\n"
"v_dot2c_f32_bf16 v132, v148, v148\n"
"v_dot2c_f32_bf16 v131, s83, v149\n"
"v_dot2c_f32_bf16 v133, v149, v149\n"
"global_load_lds_dwordx4 v129, s[42:43] offset:1024\n"
"v_add_u32 v129, 23552, v129\n"
"v_dot2c_f32_bf16 v130, s83, v150\n"
"v_dot2c_f32_bf16 v132, v150, v150\n"
"v_dot2c_f32_bf16 v131, s83, v151\n"
"v_dot2c_f32_bf16 v133, v151, v151\n"
"s_waitcnt vmcnt(16)\n"
"v_dot2c_f32_bf16 v130, s83, v152\n"
"v_dot2c_f32_bf16 v132, v152, v152\n"
"v_dot2c_f32_bf16 v131, s83, v153\n"
"v_dot2c_f32_bf16 v133, v153, v153\n"
"v_dot2c_f32_bf16 v130, s83, v154\n"
"v_dot2c_f32_bf16 v132, v154, v154\n"
"v_dot2c_f32_bf16 v131, s83, v155\n"
"v_dot2c_f32_bf16 v133, v155, v155\n"
"s_waitcnt vmcnt(15)\n"
"v_dot2c_f32_bf16 v130, s83, v8\n"
"v_dot2c_f32_bf16 v132, v8, v8\n"
"v_dot2c_f32_bf16 v131, s83, v9\n"
"v_dot2c_f32_bf16 v133, v9, v9\n"
"global_load_lds_dwordx4 v129, s[42:43] offset:2048\n"
"v_add_u32 v129, 23552, v129\n"
"v_dot2c_f32_bf16 v130, s83, v10\n"
"v_dot2c_f32_bf16 v132, v10, v10\n"
"v_dot2c_f32_bf16 v131, s83, v11\n"
"v_dot2c_f32_bf16 v133, v11, v11\n"
"s_nop 1\n"
"v_add_f32 v130, v130, v131\n"
"s_waitcnt vmcnt(15)\n"
"v_dot2c_f32_bf16 v156, s83, v12\n"
"v_add_f32 v132, v132, v133\n"
"v_dot2c_f32_bf16 v157, s83, v13\n"
"v_dot2c_f32_bf16 v156, s83, v14\n"
"v_add_f32_dpp v130, v130, v130 row_ror:8 row_mask:0xf bank_mask:0xf\n"
"v_dot2c_f32_bf16 v157, s83, v15\n"
"v_add_f32_dpp v132, v132, v132 row_ror:8 row_mask:0xf bank_mask:0xf\n"
"s_waitcnt vmcnt(14)\n"
"v_dot2c_f32_bf16 v156, s83, v20\n"
"v_dot2c_f32_bf16 v157, s83, v21\n"
"v_add_f32_dpp v130, v130, v130 row_ror:4 row_mask:0xf bank_mask:0xf\n"
"global_load_lds_dwordx4 v129, s[42:43] offset:3072\n"
"v_add_u32 v129, 27648, v129\n"
"v_dot2c_f32_bf16 v156, s83, v22\n"
"v_add_f32_dpp v132, v132, v132 row_ror:4 row_mask:0xf bank_mask:0xf\n"
"v_dot2c_f32_bf16 v157, s83, v23\n"
"s_waitcnt vmcnt(14)\n"
"v_dot2c_f32_bf16 v156, s83, v24\n"
"v_add_f32_dpp v130, v130, v130 row_ror:2 row_mask:0xf bank_mask:0xf\n"
"v_dot2c_f32_bf16 v157, s83, v25\n"
"v_add_f32_dpp v132, v132, v132 row_ror:2 row_mask:0xf bank_mask:0xf\n"
"v_dot2c_f32_bf16 v156, s83, v26\n"
"v_dot2c_f32_bf16 v157, s83, v27\n"
"v_add_f32_dpp v130, v130, v130 row_ror:1 row_mask:0xf bank_mask:0xf\n"
"s_waitcnt vmcnt(13)\n"
"v_dot2c_f32_bf16 v156, s83, v28\n"
"v_add_f32_dpp v132, v132, v132 row_ror:1 row_mask:0xf bank_mask:0xf\n"
"v_dot2c_f32_bf16 v157, s83, v29\n"
"v_dot2c_f32_bf16 v156, s83, v30\n"
"s_add_u32 m0, s79, 20480\n"
"v_mul_f32 v130, 0x3acccccd, v130\n"
"global_load_lds_dwordx4 v129, s[42:43]\n"
"v_add_u32 v129, 23552, v129\n"
"v_dot2c_f32_bf16 v157, s83, v31\n"
"v_mul_f32 v132, 0x3acccccd, v132\n"
"s_waitcnt vmcnt(13)\n"
"v_dot2c_f32_bf16 v156, s83, v32\n"
"v_dot2c_f32_bf16 v157, s83, v33\n"
"v_fma_f32 v132, -v130, v130, v132\n"
"v_dot2c_f32_bf16 v156, s83, v34\n"
"v_max_f32 v132, 0, v132\n"
"v_dot2c_f32_bf16 v157, s83, v35\n"
"s_nop 2\n"
"v_add_f32 v156, v156, v157\n"
"v_cvt_pk_bf16_f32 v132, v132, v132\n"
"s_nop 0\n"
"v_add_f32_dpp v156, v156, v156 row_ror:8 row_mask:0xf bank_mask:0xf\n"
"v_lshlrev_b32 v132, 16, v132\n"
"s_nop 0\n"
"v_add_f32_dpp v156, v156, v156 row_ror:4 row_mask:0xf bank_mask:0xf\n"
"s_nop 1\n"
"v_add_f32_dpp v156, v156, v156 row_ror:2 row_mask:0xf bank_mask:0xf\n"
"global_load_lds_dwordx4 v129, s[42:43] offset:1024\n"
"v_add_f32 v132, 0x3727c5ac, v132\n"
"v_add_f32_dpp v156, v156, v156 row_ror:1 row_mask:0xf bank_mask:0xf\n"
"v_cvt_pk_bf16_f32 v132, v132, v132\n"
"v_mul_f32 v156, 0.5, v156\n"
"v_cvt_pk_bf16_f32 v157, v156, v156\n"
"v_lshlrev_b32 v132, 16, v132\n"
"v_lshlrev_b32 v157, 16, v157\n"
"v_sqrt_f32 v132, v132\n"
"v_sub_f32 v157, v156, v157\n"
"v_cvt_pk_bf16_f32 v176, v156, v157\n"
"v_cvt_pk_bf16_f32 v120, -v130, v132\n"
"v_and_b32 v156, s82, v176\n"
"v_mov_b32 v122, v120\n"
"s_add_u32 m0, s79, 0\n"
"v_sub_f32 v157, v157, v156\n"
"buffer_load_dwordx4 v184, s[44:47], s66 offen lds\n"
"v_cvt_pk_bf16_f32 v178, v157, 0\n"
"buffer_store_dwordx4 v[120:123], v193, s[48:51], 0 offen sc1\n"
"buffer_store_dwordx4 v[176:179], v134, s[48:51], 0 offen sc1\n"
"s_waitcnt vmcnt(16)\n"
"v_lshlrev_b32 v144, 16, v108\n"
"v_and_b32 v145, s82, v108\n"
"s_waitcnt vmcnt(15)\n"
"v_lshlrev_b32 v146, 16, v112\n"
"v_and_b32 v147, s82, v112\n"
"s_waitcnt lgkmcnt(13)\n"
"v_pk_mul_f32 v[152:153], v[144:145], v[76:77]\n"
"s_waitcnt lgkmcnt(12)\n"
"v_pk_mul_f32 v[154:155], v[144:145], v[84:85]\n"
"v_pk_fma_f32 v[152:153], v[146:147], v[84:85], v[152:153]\n"
"v_pk_fma_f32 v[154:155], v[146:147], v[76:77], v[154:155] neg_lo:[1,0,0] neg_hi:[1,0,0]\n"
"v_cvt_pk_bf16_f32 v108, v152, v153\n"
"buffer_load_dwordx4 v185, s[44:47], s66 offen offset:1024 lds\n"
"v_cvt_pk_bf16_f32 v112, v154, v155\n"
"v_lshlrev_b32 v144, 16, v109\n"
"v_and_b32 v145, s82, v109\n"
"v_lshlrev_b32 v146, 16, v113\n"
"v_and_b32 v147, s82, v113\n"
"s_waitcnt lgkmcnt(9)\n"
"v_pk_mul_f32 v[152:153], v[144:145], v[78:79]\n"
"s_waitcnt lgkmcnt(8)\n"
"v_pk_mul_f32 v[154:155], v[144:145], v[86:87]\n"
"v_pk_fma_f32 v[152:153], v[146:147], v[86:87], v[152:153]\n"
"v_pk_fma_f32 v[154:155], v[146:147], v[78:79], v[154:155] neg_lo:[1,0,0] neg_hi:[1,0,0]\n"
"v_cvt_pk_bf16_f32 v109, v152, v153\n"
"v_cvt_pk_bf16_f32 v113, v154, v155\n"
"v_lshlrev_b32 v144, 16, v110\n"
"v_and_b32 v145, s82, v110\n"
"v_lshlrev_b32 v146, 16, v114\n"
"v_and_b32 v147, s82, v114\n"
"buffer_load_dwordx4 v186, s[44:47], s66 offen offset:2048 lds\n"
"s_waitcnt lgkmcnt(5)\n"
"v_pk_mul_f32 v[152:153], v[144:145], v[80:81]\n"
"s_waitcnt lgkmcnt(4)\n"
"v_pk_mul_f32 v[154:155], v[144:145], v[88:89]\n"
"v_pk_fma_f32 v[152:153], v[146:147], v[88:89], v[152:153]\n"
"v_pk_fma_f32 v[154:155], v[146:147], v[80:81], v[154:155] neg_lo:[1,0,0] neg_hi:[1,0,0]\n"
"v_cvt_pk_bf16_f32 v110, v152, v153\n"
"v_cvt_pk_bf16_f32 v114, v154, v155\n"
"v_lshlrev_b32 v144, 16, v111\n"
"v_and_b32 v145, s82, v111\n"
"v_lshlrev_b32 v146, 16, v115\n"
"v_and_b32 v147, s82, v115\n"
"s_waitcnt lgkmcnt(1)\n"
"v_pk_mul_f32 v[152:153], v[144:145], v[82:83]\n"
"s_waitcnt lgkmcnt(0)\n"
"v_pk_mul_f32 v[154:155], v[144:145], v[90:91]\n"
"v_pk_fma_f32 v[152:153], v[146:147], v[90:91], v[152:153]\n"
"v_pk_fma_f32 v[154:155], v[146:147], v[82:83], v[154:155] neg_lo:[1,0,0] neg_hi:[1,0,0]\n"
"v_cvt_pk_bf16_f32 v111, v152, v153\n"
"buffer_load_dwordx4 v187, s[44:47], s66 offen offset:3072 lds\n"
"v_cvt_pk_bf16_f32 v115, v154, v155\n"
"ds_bpermute_b32 v140, v136, v108\n"
"ds_bpermute_b32 v141, v136, v109\n"
"ds_bpermute_b32 v142, v136, v110\n"
"ds_bpermute_b32 v143, v136, v111\n"
"s_add_u32 m0, s79, 28672\n"
"ds_bpermute_b32 v144, v136, v112\n"
"buffer_load_dwordx4 v251, s[36:39], s57 offen lds\n"
"ds_bpermute_b32 v145, v136, v113\n"
"ds_bpermute_b32 v146, v136, v114\n"
"ds_bpermute_b32 v147, v136, v115\n"
"s_waitcnt vmcnt(18)\n"
"v_lshlrev_b32 v152, 16, v100\n"
"v_and_b32 v153, s82, v100\n"
"s_waitcnt vmcnt(17)\n"
"v_lshlrev_b32 v154, 16, v104\n"
"v_and_b32 v155, s82, v104\n"
"v_pk_mul_f32 v[108:109], v[152:153], v[76:77]\n"
"v_pk_mul_f32 v[110:111], v[152:153], v[84:85]\n"
"v_pk_fma_f32 v[108:109], v[154:155], v[84:85], v[108:109]\n"
"buffer_load_dwordx4 v252, s[36:39], s57 offen offset:1024 lds\n"
"v_pk_fma_f32 v[110:111], v[154:155], v[76:77], v[110:111] neg_lo:[1,0,0] neg_hi:[1,0,0]\n"
"v_cvt_pk_bf16_f32 v100, v108, v109\n"
"v_cvt_pk_bf16_f32 v104, v110, v111\n"
"v_lshlrev_b32 v152, 16, v101\n"
"v_and_b32 v153, s82, v101\n"
"v_lshlrev_b32 v154, 16, v105\n"
"v_and_b32 v155, s82, v105\n"
"v_pk_mul_f32 v[108:109], v[152:153], v[78:79]\n"
"v_pk_mul_f32 v[110:111], v[152:153], v[86:87]\n"
"v_pk_fma_f32 v[108:109], v[154:155], v[86:87], v[108:109]\n"
"v_pk_fma_f32 v[110:111], v[154:155], v[78:79], v[110:111] neg_lo:[1,0,0] neg_hi:[1,0,0]\n"
"v_cvt_pk_bf16_f32 v101, v108, v109\n"
"v_cvt_pk_bf16_f32 v105, v110, v111\n"
"v_lshlrev_b32 v152, 16, v102\n"
"buffer_load_dwordx4 v253, s[36:39], s57 offen offset:2048 lds\n"
"v_and_b32 v153, s82, v102\n"
"v_lshlrev_b32 v154, 16, v106\n"
"v_and_b32 v155, s82, v106\n"
"v_pk_mul_f32 v[108:109], v[152:153], v[80:81]\n"
"v_pk_mul_f32 v[110:111], v[152:153], v[88:89]\n"
"v_pk_fma_f32 v[108:109], v[154:155], v[88:89], v[108:109]\n"
"v_pk_fma_f32 v[110:111], v[154:155], v[80:81], v[110:111] neg_lo:[1,0,0] neg_hi:[1,0,0]\n"
"v_cvt_pk_bf16_f32 v102, v108, v109\n"
"v_cvt_pk_bf16_f32 v106, v110, v111\n"
"v_lshlrev_b32 v152, 16, v103\n"
"v_and_b32 v153, s82, v103\n"
"v_lshlrev_b32 v154, 16, v107\n"
"v_and_b32 v155, s82, v107\n"
"v_pk_mul_f32 v[108:109], v[152:153], v[82:83]\n"
"v_pk_mul_f32 v[110:111], v[152:153], v[90:91]\n"
"buffer_load_dwordx4 v254, s[36:39], s57 offen offset:3072 lds\n"
"v_pk_fma_f32 v[108:109], v[154:155], v[90:91], v[108:109]\n"
"v_pk_fma_f32 v[110:111], v[154:155], v[82:83], v[110:111] neg_lo:[1,0,0] neg_hi:[1,0,0]\n"
"v_cvt_pk_bf16_f32 v103, v108, v109\n"
"v_cvt_pk_bf16_f32 v107, v110, v111\n"
"ds_bpermute_b32 v148, v136, v100\n"
"ds_bpermute_b32 v149, v136, v101\n"
"ds_bpermute_b32 v150, v136, v102\n"
"s_add_u32 m0, s79, 4096\n"
"ds_bpermute_b32 v151, v136, v103\n"
"buffer_load_dwordx4 v184, s[44:47], s67 offen lds\n"
"ds_bpermute_b32 v152, v136, v104\n"
"ds_bpermute_b32 v153, v136, v105\n"
"ds_bpermute_b32 v154, v136, v106\n"
"ds_bpermute_b32 v155, v136, v107\n"
"buffer_load_dwordx4 v185, s[44:47], s67 offen offset:1024 lds\n"
"buffer_load_dwordx4 v186, s[44:47], s67 offen offset:2048 lds\n"
"buffer_load_dwordx4 v187, s[44:47], s67 offen offset:3072 lds\n"
"s_waitcnt vmcnt(14)\n"
"ds_read_b64_tr_b16 a[156:157], v40 offset:12288\n"
"ds_read_b64_tr_b16 a[158:159], v40 offset:12800\n"
"ds_read_b64_tr_b16 a[76:77], v40 offset:12352\n"
"ds_read_b64_tr_b16 a[78:79], v40 offset:12864\n"
"ds_read_b64_tr_b16 a[136:137], v40 offset:14336\n"
"ds_read_b64_tr_b16 a[138:139], v40 offset:14848\n"
"ds_read_b64_tr_b16 a[144:145], v40 offset:14400\n"
"ds_read_b64_tr_b16 a[146:147], v40 offset:14912\n"
"ds_read_b64_tr_b16 a[152:153], v40 offset:16384\n"
"ds_read_b64_tr_b16 a[154:155], v40 offset:16896\n"
"ds_read_b64_tr_b16 a[124:125], v40 offset:16448\n"
"ds_read_b64_tr_b16 a[126:127], v40 offset:16960\n"
"ds_read_b64_tr_b16 a[112:113], v40 offset:18432\n"
"ds_read_b64_tr_b16 a[114:115], v40 offset:18944\n"
"ds_read_b64_tr_b16 a[128:129], v40 offset:18496\n"
"s_add_u32 m0, s79, 32768\n"
"ds_read_b64_tr_b16 a[130:131], v40 offset:19008\n"
"buffer_load_dwordx4 v251, s[36:39], s58 offen lds\n"
"ds_read_b64_tr_b16 a[132:133], v40 offset:20480\n"
"ds_read_b64_tr_b16 a[134:135], v40 offset:20992\n"
"ds_read_b64_tr_b16 a[68:69], v40 offset:20544\n"
"ds_read_b64_tr_b16 a[70:71], v40 offset:21056\n"
"s_waitcnt lgkmcnt(15)\n"
"v_mfma_f32_32x32x16_bf16 v[76:91], v[4:7], a[156:159], 0\n"
"v_mfma_f32_32x32x16_bf16 v[100:115], v[4:7], a[76:79], 0\n"
"buffer_load_dwordx4 v252, s[36:39], s58 offen offset:1024 lds\n"
"s_waitcnt lgkmcnt(14)\n"
"v_mfma_f32_32x32x16_bf16 v[76:91], v[4:7], a[136:139], v[76:91]\n"
"s_waitcnt lgkmcnt(12)\n"
"v_mfma_f32_32x32x16_bf16 v[100:115], v[4:7], a[144:147], v[100:115]\n"
"buffer_load_dwordx4 v253, s[36:39], s58 offen offset:2048 lds\n"
"s_waitcnt lgkmcnt(10)\n"
"v_mfma_f32_32x32x16_bf16 v[76:91], v[4:7], a[152:155], v[76:91]\n"
"s_waitcnt lgkmcnt(8)\n"
"v_mfma_f32_32x32x16_bf16 v[100:115], v[4:7], a[124:127], v[100:115]\n"
"buffer_load_dwordx4 v254, s[36:39], s58 offen offset:3072 lds\n"
"s_waitcnt lgkmcnt(6)\n"
"v_mfma_f32_32x32x16_bf16 v[76:91], v[4:7], a[112:115], v[76:91]\n"
"s_waitcnt lgkmcnt(4)\n"
"s_add_u32 m0, s79, 8192\n"
"v_mfma_f32_32x32x16_bf16 v[100:115], v[4:7], a[128:131], v[100:115]\n"
"buffer_load_dwordx4 v184, s[44:47], s68 offen lds\n"
"s_waitcnt lgkmcnt(2)\n"
"v_mfma_f32_32x32x16_bf16 v[76:91], v[4:7], a[132:135], v[76:91]\n"
"s_waitcnt lgkmcnt(0)\n"
"v_mfma_f32_32x32x16_bf16 v[100:115], v[4:7], a[68:71], v[100:115]\n"
"buffer_load_dwordx4 v185, s[44:47], s68 offen offset:1024 lds\n"
"v_cmp_gt_u32 vcc, 32, v255\n"
"s_nop 1\n"
"s_nop 8\n"
"v_cndmask_b32 v4, v100, v76, vcc\n"
"ds_write_b32 v128, v4\n"
"s_waitcnt lgkmcnt(0)\n"
"s_barrier\n"
"ds_read_b32 v76, v137\n"
"ds_read_b32 v77, v137 offset:40960\n"
"buffer_load_dwordx4 v186, s[44:47], s68 offen offset:2048 lds\n"
"ds_read_b32 v78, v41\n"
"ds_read_b32 v79, v41 offset:40960\n"
"s_waitcnt lgkmcnt(2)\n"
"v_add_f32 v76, v76, v77\n"
"s_waitcnt lgkmcnt(0)\n"
"v_add_f32 v78, v78, v79\n"
"v_add_f32 v76, v76, v78\n"
"v_cvt_pk_bf16_f32 v77, v76, v76\n"
"v_lshlrev_b32 v77, 16, v77\n"
"v_sub_f32 v77, v76, v77\n"
"v_cvt_pk_bf16_f32 v0, v76, v77\n"
"v_and_b32 v78, s82, v0\n"
"v_sub_f32 v77, v77, v78\n"
"v_cvt_pk_bf16_f32 v2, v77, 0\n"
"buffer_load_dwordx4 v187, s[44:47], s68 offen offset:3072 lds\n"
"buffer_store_dwordx4 v[0:3], v42, s[48:51], 0 offen sc1\n"
"buffer_store_dwordx4 v[140:143], v190, s[48:51], 0 offen sc1\n"
"buffer_store_dwordx4 v[144:147], v190, s[48:51], 0 offen offset:512 sc1\n"
"buffer_store_dwordx4 v[148:151], v190, s[48:51], 0 offen offset:128 sc1\n"
"buffer_store_dwordx4 v[152:155], v190, s[48:51], 0 offen offset:640 sc1\n"
"s_waitcnt vmcnt(21)\n"
"s_barrier\n"
"ds_read_b128 a[156:159], v124 offset:24576\n"
"ds_read_b64_tr_b16 a[128:129], v36\n"
"ds_read_b64_tr_b16 a[130:131], v36 offset:512\n"
"ds_read_b64_tr_b16 a[76:77], v37\n"
"ds_read_b64_tr_b16 a[78:79], v37 offset:512\n"
"ds_read_b64_tr_b16 a[136:137], v38\n"
"ds_read_b64_tr_b16 a[138:139], v38 offset:512\n"
"ds_read_b64_tr_b16 a[144:145], v39\n"
"ds_read_b64_tr_b16 a[146:147], v39 offset:512\n"
"ds_read_b128 a[152:155], v125 offset:24576\n"
"ds_read_b64_tr_b16 a[124:125], v36 offset:1024\n"
"ds_read_b64_tr_b16 a[126:127], v36 offset:1536\n"
"ds_read_b64_tr_b16 a[112:113], v37 offset:1024\n"
"ds_read_b64_tr_b16 a[114:115], v37 offset:1536\n"
"ds_read_b64_tr_b16 a[132:133], v38 offset:1024\n"
"ds_read_b64_tr_b16 a[134:135], v38 offset:1536\n"
"ds_read_b64_tr_b16 a[68:69], v39 offset:1024\n"
"ds_read_b64_tr_b16 a[70:71], v39 offset:1536\n"
"s_waitcnt lgkmcnt(9)\n"
"v_mfma_f32_32x32x16_bf16 v[76:91], a[128:131], a[156:159], 0\n"
"ds_read_b128 a[148:151], v126 offset:24576\n"
"ds_read_b64_tr_b16 a[140:141], v36 offset:2048\n"
"ds_read_b64_tr_b16 a[142:143], v36 offset:2560\n"
"v_and_b32 v156, 3, v255\n"
"v_bfe_u32 v176, v255, 2, 1\n"
"v_mfma_f32_32x32x16_bf16 v[100:115], a[76:79], a[156:159], 0\n"
"ds_read_b64_tr_b16 a[128:129], v37 offset:2048\n"
"ds_read_b64_tr_b16 a[130:131], v37 offset:2560\n"
"v_lshl_add_u32 v156, v176, 4, v156\n"
"v_bfe_u32 v176, v255, 3, 2\n"
"v_lshl_add_u32 v156, v176, 2, v156\n"
"v_lshrrev_b32 v176, 4, v156\n"
"v_mfma_f32_32x32x16_bf16 v[140:155], a[136:139], a[156:159], 0\n"
"ds_read_b64_tr_b16 a[76:77], v38 offset:2048\n"
"s_add_u32 m0, s79, 36864\n"
"ds_read_b64_tr_b16 a[78:79], v38 offset:2560\n"
"buffer_load_dwordx4 v251, s[36:39], s59 offen lds\n"
"v_lshlrev_b32 v176, 10, v176\n"
"v_mfma_f32_32x32x16_bf16 v[0:15], a[144:147], a[156:159], 0\n"
"ds_read_b64_tr_b16 a[136:137], v39 offset:2048\n"
"ds_read_b64_tr_b16 a[138:139], v39 offset:2560\n"
"v_and_b32 v193, 15, v156\n"
"v_lshl_add_u32 v193, v138, 4, v193\n"
"v_lshl_add_u32 v138, v193, 4, v176\n"
"s_lshl_b32 s86, s77, 2\n"
"s_waitcnt lgkmcnt(9)\n"
"v_mfma_f32_32x32x16_bf16 v[76:91], a[124:127], a[152:155], v[76:91]\n"
"ds_read_b128 a[144:147], v127 offset:24576\n"
"ds_read_b64_tr_b16 a[116:117], v36 offset:3072\n"
"ds_read_b64_tr_b16 a[118:119], v36 offset:3584\n"
"buffer_load_dwordx4 v252, s[36:39], s59 offen offset:1024 lds\n"
"v_mfma_f32_32x32x16_bf16 v[100:115], a[112:115], a[152:155], v[100:115]\n"
"ds_read_b64_tr_b16 a[124:125], v37 offset:3072\n"
"ds_read_b64_tr_b16 a[126:127], v37 offset:3584\n"
"s_add_u32 s86, s86, s76\n"
"s_lshl_b32 s86, s86, 9\n"
"s_add_u32 s86, s86, 4096\n"
"v_lshlrev_b32 v176, 4, v156\n"
"v_add_u32 v157, s86, v176\n"
"buffer_load_dwordx4 v253, s[36:39], s59 offen offset:2048 lds\n"
"v_mfma_f32_32x32x16_bf16 v[140:155], a[132:135], a[152:155], v[140:155]\n"
"ds_read_b64_tr_b16 a[112:113], v38 offset:3072\n"
"ds_read_b64_tr_b16 a[114:115], v38 offset:3584\n"
"v_lshlrev_b32 v156, 2, v156\n"
"v_add_u32 v156, s79, v156\n"
"s_lshl_b32 s86, s77, 1\n"
"v_mfma_f32_32x32x16_bf16 v[0:15], a[68:71], a[152:155], v[0:15]\n"
"ds_read_b64_tr_b16 a[132:133], v39 offset:3072\n"
"ds_read_b64_tr_b16 a[134:135], v39 offset:3584\n"
"s_lshr_b32 s87, s76, 1\n"
"s_add_u32 s86, s86, s87\n"
"s_mul_i32 s86, s86, 88\n"
"buffer_load_dwordx4 v254, s[36:39], s59 offen offset:3072 lds\n"
"s_waitcnt lgkmcnt(9)\n"
"v_mfma_f32_32x32x16_bf16 v[76:91], a[140:143], a[148:151], v[76:91]\n"
"s_waitcnt vmcnt(17)\n"
"s_barrier\n"
"s_mul_i32 s87, s78, 11\n"
"v_mfma_f32_32x32x16_bf16 v[100:115], a[128:131], a[148:151], v[100:115]\n"
"ds_read_b128 a[140:143], v124 offset:28672\n"
"ds_read_b64_tr_b16 a[72:73], v36 offset:4096\n"
"ds_read_b64_tr_b16 a[74:75], v36 offset:4608\n"
"s_add_u32 s86, s86, s87\n"
"s_add_u32 m0, s79, 12288\n"
"s_nop 0\n"
"buffer_load_dwordx4 v184, s[44:47], s69 offen lds\n"
"v_mfma_f32_32x32x16_bf16 v[140:155], a[76:79], a[148:151], v[140:155]\n"
"ds_read_b64_tr_b16 a[120:121], v37 offset:4096\n"
"ds_read_b64_tr_b16 a[122:123], v37 offset:4608\n"
"ds_read_b64_tr_b16 a[80:81], v38 offset:4096\n"
"s_lshl_b32 s86, s86, 12\n"
"s_and_b32 s87, s76, 1\n"
"v_mfma_f32_32x32x16_bf16 v[0:15], a[136:139], a[148:151], v[0:15]\n"
"ds_read_b64_tr_b16 a[82:83], v38 offset:4608\n"
"ds_read_b64_tr_b16 a[128:129], v39 offset:4096\n"
"ds_read_b64_tr_b16 a[130:131], v39 offset:4608\n"
"s_lshl_b32 s87, s87, 11\n"
"buffer_load_dwordx4 v185, s[44:47], s69 offen offset:1024 lds\n"
"s_waitcnt lgkmcnt(9)\n"
"v_mfma_f32_32x32x16_bf16 v[76:91], a[116:119], a[144:147], v[76:91]\n"
"ds_read_b128 a[136:139], v125 offset:28672\n"
"ds_read_b64_tr_b16 a[76:77], v36 offset:5120\n"
"ds_read_b64_tr_b16 a[78:79], v36 offset:5632\n"
"s_add_u32 s92, s86, s87\n"
"s_lshr_b32 s27, s94, 3\n"
"v_mfma_f32_32x32x16_bf16 v[100:115], a[124:127], a[144:147], v[100:115]\n"
"ds_read_b64_tr_b16 a[68:69], v37 offset:5120\n"
"ds_read_b64_tr_b16 a[70:71], v37 offset:5632\n"
"s_and_b32 s26, s94, 7\n"
"s_lshl_b32 s89, s76, 1\n"
"buffer_load_dwordx4 v186, s[44:47], s69 offen offset:2048 lds\n"
"v_mfma_f32_32x32x16_bf16 v[140:155], a[112:115], a[144:147], v[140:155]\n"
"ds_read_b64_tr_b16 a[124:125], v38 offset:5120\n"
"ds_read_b64_tr_b16 a[126:127], v38 offset:5632\n"
"s_add_u32 s89, s89, s27\n"
"s_lshl_b32 s86, s26, 3\n"
"s_add_u32 s86, s86, s89\n"
"s_mul_i32 s96, s86, 56320\n"
"s_add_u32 s96, s96, 12124160\n"
"v_mfma_f32_32x32x16_bf16 v[0:15], a[132:135], a[144:147], v[0:15]\n"
"ds_read_b64_tr_b16 a[116:117], v39 offset:5120\n"
"ds_read_b64_tr_b16 a[118:119], v39 offset:5632\n"
"s_lshl_b32 s86, s77, 1\n"
"s_add_u32 s86, s86, s27\n"
"buffer_load_dwordx4 v187, s[44:47], s69 offen offset:3072 lds\n"
"s_waitcnt lgkmcnt(9)\n"
"v_mfma_f32_32x32x16_bf16 v[76:91], a[72:75], a[140:143], v[76:91]\n"
"ds_read_b128 a[132:135], v126 offset:28672\n"
"ds_read_b64_tr_b16 a[112:113], v36 offset:6144\n"
"ds_read_b64_tr_b16 a[114:115], v36 offset:6656\n"
"s_xor_b32 s87, s86, 1\n"
"s_lshl_b32 s86, s86, 3\n"
"s_lshl_b32 s87, s87, 3\n"
"v_mfma_f32_32x32x16_bf16 v[100:115], a[120:123], a[140:143], v[100:115]\n"
"ds_read_b64_tr_b16 a[72:73], v37 offset:6144\n"
"ds_read_b64_tr_b16 a[74:75], v37 offset:6656\n"
"s_add_u32 s86, s86, s89\n"
"s_add_u32 m0, s79, 24576\n"
"s_nop 0\n"
"buffer_load_dwordx4 v251, s[36:39], s60 offen lds\n"
"v_mfma_f32_32x32x16_bf16 v[140:155], a[80:83], a[140:143], v[140:155]\n"
"ds_read_b64_tr_b16 a[120:121], v38 offset:6144\n"
"ds_read_b64_tr_b16 a[122:123], v38 offset:6656\n"
"s_add_u32 s87, s87, s89\n"
"s_mul_i32 s97, s86, 45056\n"
"s_mul_i32 s93, s87, 45056\n"
"s_add_u32 s97, s97, 586752\n"
"s_add_u32 s93, s93, 586752\n"
"v_mfma_f32_32x32x16_bf16 v[0:15], a[128:131], a[140:143], v[0:15]\n"
"ds_read_b64_tr_b16 a[80:81], v39 offset:6144\n"
"ds_read_b64_tr_b16 a[82:83], v39 offset:6656\n"
"s_lshl_b32 s86, s77, 4\n"
"s_add_u32 s87, s86, s94\n"
"s_lshl_b32 s87, s87, 2\n"
"s_add_u32 s87, s87, s76\n"
"s_lshl_b32 s87, s87, 7\n"
"s_waitcnt lgkmcnt(9)\n"
"v_mfma_f32_32x32x16_bf16 v[76:91], a[76:79], a[136:139], v[76:91]\n"
"ds_read_b128 a[128:131], v127 offset:28672\n"
"ds_read_b64_tr_b16 a[104:105], v36 offset:7168\n"
"ds_read_b64_tr_b16 a[106:107], v36 offset:7680\n"
"s_add_u32 s28, s87, 131072\n"
"s_lshl_b32 s87, s89, 1\n"
"v_mfma_f32_32x32x16_bf16 v[100:115], a[68:71], a[136:139], v[100:115]\n"
"ds_read_b64_tr_b16 a[76:77], v37 offset:7168\n"
"ds_read_b64_tr_b16 a[78:79], v37 offset:7680\n"
"s_add_u32 s86, s86, s87\n"
"s_lshl_b32 s86, s86, 9\n"
"s_add_u32 s86, s86, 131072\n"
"v_lshrrev_b32 v176, 3, v255\n"
"v_lshlrev_b32 v176, 7, v176\n"
"v_mfma_f32_32x32x16_bf16 v[140:155], a[124:127], a[136:139], v[140:155]\n"
"ds_read_b64_tr_b16 a[68:69], v38 offset:7168\n"
"ds_read_b64_tr_b16 a[70:71], v38 offset:7680\n"
"v_add_u32 v190, s86, v176\n"
"v_lshrrev_b32 v176, 3, v255\n"
"v_bfe_u32 v193, v255, 2, 1\n"
"v_min_u32 v177, 5, v176\n"
"s_lshl_b32 s86, s89, 2\n"
"v_mfma_f32_32x32x16_bf16 v[0:15], a[116:119], a[136:139], v[0:15]\n"
"ds_read_b64_tr_b16 a[92:93], v39 offset:7168\n"
"ds_read_b64_tr_b16 a[94:95], v39 offset:7680\n"
"s_add_u32 s87, s87, 28\n"
"s_sub_u32 s87, s87, s86\n"
"v_lshrrev_b32 v42, 2, v177\n"
"v_mul_u32_u24 v42, s87, v42\n"
"s_waitcnt lgkmcnt(9)\n"
"v_mfma_f32_32x32x16_bf16 v[76:91], a[112:115], a[132:135], v[76:91]\n"
"s_waitcnt vmcnt(14)\n"
"s_barrier\n"
"v_add3_u32 v177, v177, v42, s86\n"
"v_mul_u32_u24 v177, 10, v177\n"
"v_mfma_f32_32x32x16_bf16 v[100:115], a[72:75], a[132:135], v[100:115]\n"
"ds_read_b128 a[124:127], v124 offset:32768\n"
"ds_read_b64_tr_b16 a[112:113], v36 offset:8192\n"
"ds_read_b64_tr_b16 a[114:115], v36 offset:8704\n"
"s_mul_i32 s86, s26, 5\n"
"s_lshr_b32 s86, s86, 2\n"
"v_add3_u32 v177, v177, v193, s86\n"
"v_mfma_f32_32x32x16_bf16 v[140:155], a[120:123], a[132:135], v[140:155]\n"
"ds_read_b64_tr_b16 a[88:89], v37 offset:8192\n"
"ds_read_b64_tr_b16 a[90:91], v37 offset:8704\n"
"ds_read_b64_tr_b16 a[72:73], v38 offset:8192\n"
"buffer_load_dwordx4 v252, s[36:39], s60 offen offset:1024 lds\n"
"v_lshlrev_b32 v177, 7, v177\n"
"v_mfma_f32_32x32x16_bf16 v[0:15], a[80:83], a[132:135], v[0:15]\n"
"ds_read_b64_tr_b16 a[74:75], v38 offset:8704\n"
"ds_read_b64_tr_b16 a[96:97], v39 offset:8192\n"
"ds_read_b64_tr_b16 a[98:99], v39 offset:8704\n"
"v_add_u32 v42, 65600, v177\n"
"v_min_u32 v176, 4, v176\n"
"v_mul_u32_u24 v176, 10, v176\n"
"s_waitcnt lgkmcnt(9)\n"
"v_mfma_f32_32x32x16_bf16 v[76:91], a[104:107], a[128:131], v[76:91]\n"
"ds_read_b128 a[120:123], v125 offset:32768\n"
"ds_read_b64_tr_b16 a[108:109], v36 offset:9216\n"
"ds_read_b64_tr_b16 a[110:111], v36 offset:9728\n"
"buffer_load_dwordx4 v253, s[36:39], s60 offen offset:2048 lds\n"
"v_mfma_f32_32x32x16_bf16 v[100:115], a[76:79], a[128:131], v[100:115]\n"
"ds_read_b64_tr_b16 a[100:101], v37 offset:9216\n"
"ds_read_b64_tr_b16 a[102:103], v37 offset:9728\n"
"s_mul_i32 s86, s26, 50\n"
"s_mul_i32 s87, s89, 5\n"
"buffer_load_dwordx4 v254, s[36:39], s60 offen offset:3072 lds\n"
"v_mfma_f32_32x32x16_bf16 v[140:155], a[68:71], a[128:131], v[140:155]\n"
"ds_read_b64_tr_b16 a[84:85], v38 offset:9216\n"
"ds_read_b64_tr_b16 a[86:87], v38 offset:9728\n"
"s_lshr_b32 s87, s87, 2\n"
"s_add_u32 s86, s86, s87\n"
"v_add3_u32 v176, v176, v193, s86\n"
"v_lshlrev_b32 v176, 7, v176\n"
"v_mfma_f32_32x32x16_bf16 v[0:15], a[92:95], a[128:131], v[0:15]\n"
"ds_read_b64_tr_b16 a[40:41], v39 offset:9216\n"
"s_add_u32 m0, s79, 16384\n"
"ds_read_b64_tr_b16 a[42:43], v39 offset:9728\n"
"buffer_load_dwordx4 v184, s[44:47], s70 offen lds\n"
"v_add_u32 v193, 65536, v176\n"
"s_waitcnt lgkmcnt(9)\n"
"v_mfma_f32_32x32x16_bf16 v[76:91], a[112:115], a[124:127], v[76:91]\n"
"ds_read_b128 a[116:119], v126 offset:32768\n"
"ds_read_b64_tr_b16 a[60:61], v36 offset:10240\n"
"ds_read_b64_tr_b16 a[62:63], v36 offset:10752\n"
"v_mfma_f32_32x32x16_bf16 v[100:115], a[88:91], a[124:127], v[100:115]\n"
"ds_read_b64_tr_b16 a[64:65], v37 offset:10240\n"
"ds_read_b64_tr_b16 a[66:67], v37 offset:10752\n"
"buffer_load_dwordx4 v185, s[44:47], s70 offen offset:1024 lds\n"
"v_mfma_f32_32x32x16_bf16 v[140:155], a[72:75], a[124:127], v[140:155]\n"
"ds_read_b64_tr_b16 a[88:89], v38 offset:10240\n"
"ds_read_b64_tr_b16 a[90:91], v38 offset:10752\n"
"v_mfma_f32_32x32x16_bf16 v[0:15], a[96:99], a[124:127], v[0:15]\n"
"ds_read_b64_tr_b16 a[80:81], v39 offset:10240\n"
"ds_read_b64_tr_b16 a[82:83], v39 offset:10752\n"
"buffer_load_dwordx4 v186, s[44:47], s70 offen offset:2048 lds\n"
"s_waitcnt lgkmcnt(9)\n"
"v_mfma_f32_32x32x16_bf16 v[76:91], a[108:111], a[120:123], v[76:91]\n"
"ds_read_b128 a[112:115], v127 offset:32768\n"
"ds_read_b64_tr_b16 a[104:105], v36 offset:11264\n"
"ds_read_b64_tr_b16 a[106:107], v36 offset:11776\n"
"v_mfma_f32_32x32x16_bf16 v[100:115], a[100:103], a[120:123], v[100:115]\n"
"ds_read_b64_tr_b16 a[68:69], v37 offset:11264\n"
"ds_read_b64_tr_b16 a[70:71], v37 offset:11776\n"
"v_mfma_f32_32x32x16_bf16 v[140:155], a[84:87], a[120:123], v[140:155]\n"
"ds_read_b64_tr_b16 a[76:77], v38 offset:11264\n"
"ds_read_b64_tr_b16 a[78:79], v38 offset:11776\n"
"buffer_load_dwordx4 v187, s[44:47], s70 offen offset:3072 lds\n"
"v_mfma_f32_32x32x16_bf16 v[0:15], a[40:43], a[120:123], v[0:15]\n"
"ds_read_b64_tr_b16 a[92:93], v39 offset:11264\n"
"s_add_u32 m0, s79, 28672\n"
"ds_read_b64_tr_b16 a[94:95], v39 offset:11776\n"
"buffer_load_dwordx4 v251, s[36:39], s61 offen lds\n"
"buffer_load_dwordx4 v252, s[36:39], s61 offen offset:1024 lds\n"
"buffer_load_dwordx4 v253, s[36:39], s61 offen offset:2048 lds\n"
"buffer_load_dwordx4 v254, s[36:39], s61 offen offset:3072 lds\n"
"s_add_u32 m0, s79, 20480\n"
"s_nop 0\n"
"buffer_load_dwordx4 v184, s[44:47], s71 offen lds\n"
"s_waitcnt lgkmcnt(9)\n"
"v_mfma_f32_32x32x16_bf16 v[76:91], a[60:63], a[116:119], v[76:91]\n"
"s_waitcnt vmcnt(13)\n"
"s_barrier\n"
"buffer_load_dwordx4 v185, s[44:47], s71 offen offset:1024 lds\n"
"v_mfma_f32_32x32x16_bf16 v[100:115], a[64:67], a[116:119], v[100:115]\n"
"ds_read_b128 a[72:75], v124 offset:36864\n"
"ds_read_b64_tr_b16 a[60:61], v36 offset:12288\n"
"ds_read_b64_tr_b16 a[62:63], v36 offset:12800\n"
"v_mov_b32 v176, s80\n"
"v_mov_b32 v177, s81\n"
"s_mov_b64 exec, 1\n"
"buffer_store_dwordx2 v[176:177], off, s[48:51], s91 sc1\n"
"s_mov_b64 exec, -1\n"
"v_mfma_f32_32x32x16_bf16 v[140:155], a[88:91], a[116:119], v[140:155]\n"
"ds_read_b64_tr_b16 a[84:85], v37 offset:12288\n"
"ds_read_b64_tr_b16 a[86:87], v37 offset:12800\n"
"ds_read_b64_tr_b16 a[64:65], v38 offset:12288\n"
"v_mfma_f32_32x32x16_bf16 v[0:15], a[80:83], a[116:119], v[0:15]\n"
"ds_read_b64_tr_b16 a[66:67], v38 offset:12800\n"
"ds_read_b64_tr_b16 a[88:89], v39 offset:12288\n"
"ds_read_b64_tr_b16 a[90:91], v39 offset:12800\n"
"buffer_load_dwordx4 v186, s[44:47], s71 offen offset:2048 lds\n"
"s_waitcnt lgkmcnt(9)\n"
"v_mfma_f32_32x32x16_bf16 v[76:91], a[104:107], a[112:115], v[76:91]\n"
"ds_read_b128 a[80:83], v125 offset:36864\n"
"ds_read_b64_tr_b16 a[8:9], v36 offset:13312\n"
"ds_read_b64_tr_b16 a[10:11], v36 offset:13824\n"
"v_mfma_f32_32x32x16_bf16 v[100:115], a[68:71], a[112:115], v[100:115]\n"
"ds_read_b64_tr_b16 a[104:105], v37 offset:13312\n"
"ds_read_b64_tr_b16 a[106:107], v37 offset:13824\n"
"v_mfma_f32_32x32x16_bf16 v[140:155], a[76:79], a[112:115], v[140:155]\n"
"ds_read_b64_tr_b16 a[100:101], v38 offset:13312\n"
"ds_read_b64_tr_b16 a[102:103], v38 offset:13824\n"
"buffer_load_dwordx4 v187, s[44:47], s71 offen offset:3072 lds\n"
"v_mfma_f32_32x32x16_bf16 v[0:15], a[92:95], a[112:115], v[0:15]\n"
"ds_read_b64_tr_b16 a[96:97], v39 offset:13312\n"
"ds_read_b64_tr_b16 a[98:99], v39 offset:13824\n"
"s_waitcnt lgkmcnt(9)\n"
"v_mfma_f32_32x32x16_bf16 v[76:91], a[60:63], a[72:75], v[76:91]\n"
"ds_read_b128 a[68:71], v126 offset:36864\n"
"ds_read_b64_tr_b16 a[108:109], v36 offset:14336\n"
"s_add_u32 m0, s79, 32768\n"
"ds_read_b64_tr_b16 a[110:111], v36 offset:14848\n"
"buffer_load_dwordx4 v251, s[36:39], s62 offen lds\n"
"v_mfma_f32_32x32x16_bf16 v[100:115], a[84:87], a[72:75], v[100:115]\n"
"ds_read_b64_tr_b16 a[52:53], v37 offset:14336\n"
"ds_read_b64_tr_b16 a[54:55], v37 offset:14848\n"
"v_mfma_f32_32x32x16_bf16 v[140:155], a[64:67], a[72:75], v[140:155]\n"
"ds_read_b64_tr_b16 a[84:85], v38 offset:14336\n"
"ds_read_b64_tr_b16 a[86:87], v38 offset:14848\n"
"buffer_load_dwordx4 v252, s[36:39], s62 offen offset:1024 lds\n"
"v_mfma_f32_32x32x16_bf16 v[0:15], a[88:91], a[72:75], v[0:15]\n"
"ds_read_b64_tr_b16 a[92:93], v39 offset:14336\n"
"ds_read_b64_tr_b16 a[94:95], v39 offset:14848\n"
"s_waitcnt lgkmcnt(9)\n"
"v_mfma_f32_32x32x16_bf16 v[76:91], a[8:11], a[80:83], v[76:91]\n"
"ds_read_b128 a[76:79], v127 offset:36864\n"
"ds_read_b64_tr_b16 a[40:41], v36 offset:15360\n"
"ds_read_b64_tr_b16 a[42:43], v36 offset:15872\n"
"buffer_load_dwordx4 v253, s[36:39], s62 offen offset:2048 lds\n"
"v_mfma_f32_32x32x16_bf16 v[100:115], a[104:107], a[80:83], v[100:115]\n"
"ds_read_b64_tr_b16 a[60:61], v37 offset:15360\n"
"ds_read_b64_tr_b16 a[62:63], v37 offset:15872\n"
"v_mfma_f32_32x32x16_bf16 v[140:155], a[100:103], a[80:83], v[140:155]\n"
"ds_read_b64_tr_b16 a[64:65], v38 offset:15360\n"
"ds_read_b64_tr_b16 a[66:67], v38 offset:15872\n"
"v_mfma_f32_32x32x16_bf16 v[0:15], a[96:99], a[80:83], v[0:15]\n"
"ds_read_b64_tr_b16 a[88:89], v39 offset:15360\n"
"ds_read_b64_tr_b16 a[90:91], v39 offset:15872\n"
"buffer_load_dwordx4 v254, s[36:39], s62 offen offset:3072 lds\n"
"s_waitcnt lgkmcnt(9)\n"
"v_mfma_f32_32x32x16_bf16 v[76:91], a[108:111], a[68:71], v[76:91]\n"
"s_waitcnt vmcnt(13)\n"
"s_barrier\n"
"s_add_u32 m0, s79, 0\n"
"s_nop 0\n"
"buffer_load_dwordx4 v184, s[44:47], s72 offen lds\n"
"v_mfma_f32_32x32x16_bf16 v[100:115], a[52:55], a[68:71], v[100:115]\n"
"ds_read_b128 a[104:107], v124 offset:24576\n"
"ds_read_b64_tr_b16 a[28:29], v36 offset:16384\n"
"ds_read_b64_tr_b16 a[30:31], v36 offset:16896\n"
"v_mfma_f32_32x32x16_bf16 v[140:155], a[84:87], a[68:71], v[140:155]\n"
"ds_read_b64_tr_b16 a[100:101], v37 offset:16384\n"
"ds_read_b64_tr_b16 a[102:103], v37 offset:16896\n"
"ds_read_b64_tr_b16 a[84:85], v38 offset:16384\n"
"buffer_load_dwordx4 v185, s[44:47], s72 offen offset:1024 lds\n"
"v_mfma_f32_32x32x16_bf16 v[0:15], a[92:95], a[68:71], v[0:15]\n"
"ds_read_b64_tr_b16 a[86:87], v38 offset:16896\n"
"ds_read_b64_tr_b16 a[96:97], v39 offset:16384\n"
"ds_read_b64_tr_b16 a[98:99], v39 offset:16896\n"
"s_waitcnt lgkmcnt(9)\n"
"v_mfma_f32_32x32x16_bf16 v[76:91], a[40:43], a[76:79], v[76:91]\n"
"ds_read_b128 a[92:95], v125 offset:24576\n"
"ds_read_b64_tr_b16 a[8:9], v36 offset:17408\n"
"ds_read_b64_tr_b16 a[10:11], v36 offset:17920\n"
"buffer_load_dwordx4 v186, s[44:47], s72 offen offset:2048 lds\n"
"v_mfma_f32_32x32x16_bf16 v[100:115], a[60:63], a[76:79], v[100:115]\n"
"ds_read_b64_tr_b16 a[108:109], v37 offset:17408\n"
"ds_read_b64_tr_b16 a[110:111], v37 offset:17920\n"
"v_mfma_f32_32x32x16_bf16 v[140:155], a[64:67], a[76:79], v[140:155]\n"
"ds_read_b64_tr_b16 a[52:53], v38 offset:17408\n"
"ds_read_b64_tr_b16 a[54:55], v38 offset:17920\n"
"buffer_load_dwordx4 v187, s[44:47], s72 offen offset:3072 lds\n"
"v_mfma_f32_32x32x16_bf16 v[0:15], a[88:91], a[76:79], v[0:15]\n"
"ds_read_b64_tr_b16 a[20:21], v39 offset:17408\n"
"ds_read_b64_tr_b16 a[22:23], v39 offset:17920\n"
"s_waitcnt lgkmcnt(9)\n"
"v_mfma_f32_32x32x16_bf16 v[76:91], a[28:31], a[104:107], v[76:91]\n"
"ds_read_b128 a[88:91], v126 offset:24576\n"
"ds_read_b64_tr_b16 a[24:25], v36 offset:18432\n"
"s_add_u32 m0, s79, 36864\n"
"ds_read_b64_tr_b16 a[26:27], v36 offset:18944\n"
"buffer_load_dwordx4 v251, s[36:39], s63 offen lds\n"
"v_mfma_f32_32x32x16_bf16 v[100:115], a[100:103], a[104:107], v[100:115]\n"
"ds_read_b64_tr_b16 a[28:29], v37 offset:18432\n"
"ds_read_b64_tr_b16 a[30:31], v37 offset:18944\n"
"v_mfma_f32_32x32x16_bf16 v[140:155], a[84:87], a[104:107], v[140:155]\n"
"ds_read_b64_tr_b16 a[16:17], v38 offset:18432\n"
"ds_read_b64_tr_b16 a[18:19], v38 offset:18944\n"
"buffer_load_dwordx4 v252, s[36:39], s63 offen offset:1024 lds\n"
"v_mfma_f32_32x32x16_bf16 v[0:15], a[96:99], a[104:107], v[0:15]\n"
"ds_read_b64_tr_b16 a[100:101], v39 offset:18432\n"
"ds_read_b64_tr_b16 a[102:103], v39 offset:18944\n"
"s_waitcnt lgkmcnt(9)\n"
"v_mfma_f32_32x32x16_bf16 v[76:91], a[8:11], a[92:95], v[76:91]\n"
"ds_read_b128 a[96:99], v127 offset:24576\n"
"ds_read_b64_tr_b16 a[40:41], v36 offset:19456\n"
"ds_read_b64_tr_b16 a[42:43], v36 offset:19968\n"
"buffer_load_dwordx4 v253, s[36:39], s63 offen offset:2048 lds\n"
"v_mfma_f32_32x32x16_bf16 v[100:115], a[108:111], a[92:95], v[100:115]\n"
"ds_read_b64_tr_b16 a[84:85], v37 offset:19456\n"
"ds_read_b64_tr_b16 a[86:87], v37 offset:19968\n"
"v_mfma_f32_32x32x16_bf16 v[140:155], a[52:55], a[92:95], v[140:155]\n"
"ds_read_b64_tr_b16 a[60:61], v38 offset:19456\n"
"ds_read_b64_tr_b16 a[62:63], v38 offset:19968\n"
"v_mfma_f32_32x32x16_bf16 v[0:15], a[20:23], a[92:95], v[0:15]\n"
"ds_read_b64_tr_b16 a[64:65], v39 offset:19456\n"
"ds_read_b64_tr_b16 a[66:67], v39 offset:19968\n"
"buffer_load_dwordx4 v254, s[36:39], s63 offen offset:3072 lds\n"
"s_add_u32 m0, s79, 4096\n"
"s_nop 0\n"
"buffer_load_dwordx4 v184, s[44:47], s73 offen lds\n"
"s_waitcnt lgkmcnt(9)\n"
"v_mfma_f32_32x32x16_bf16 v[76:91], a[24:27], a[88:91], v[76:91]\n"
"s_waitcnt vmcnt(13)\n"
"s_barrier\n"
"buffer_load_dwordx4 v185, s[44:47], s73 offen offset:1024 lds\n"
"v_mfma_f32_32x32x16_bf16 v[100:115], a[28:31], a[88:91], v[100:115]\n"
"ds_read_b128 a[108:111], v124 offset:28672\n"
"ds_read_b64_tr_b16 a[8:9], v36 offset:20480\n"
"ds_read_b64_tr_b16 a[10:11], v36 offset:20992\n"
"v_mfma_f32_32x32x16_bf16 v[140:155], a[16:19], a[88:91], v[140:155]\n"
"ds_read_b64_tr_b16 a[52:53], v37 offset:20480\n"
"ds_read_b64_tr_b16 a[54:55], v37 offset:20992\n"
"ds_read_b64_tr_b16 a[16:17], v38 offset:20480\n"
"buffer_load_dwordx4 v186, s[44:47], s73 offen offset:2048 lds\n"
"v_mfma_f32_32x32x16_bf16 v[0:15], a[100:103], a[88:91], v[0:15]\n"
"ds_read_b64_tr_b16 a[18:19], v38 offset:20992\n"
"ds_read_b64_tr_b16 a[24:25], v39 offset:20480\n"
"ds_read_b64_tr_b16 a[26:27], v39 offset:20992\n"
"s_waitcnt lgkmcnt(9)\n"
"v_mfma_f32_32x32x16_bf16 v[76:91], a[40:43], a[96:99], v[76:91]\n"
"ds_read_b128 a[100:103], v125 offset:28672\n"
"ds_read_b64_tr_b16 a[20:21], v36 offset:21504\n"
"ds_read_b64_tr_b16 a[22:23], v36 offset:22016\n"
"buffer_load_dwordx4 v187, s[44:47], s73 offen offset:3072 lds\n"
"v_mfma_f32_32x32x16_bf16 v[100:115], a[84:87], a[96:99], v[100:115]\n"
"ds_read_b64_tr_b16 a[168:169], v37 offset:21504\n"
"ds_read_b64_tr_b16 a[170:171], v37 offset:22016\n"
"v_mfma_f32_32x32x16_bf16 v[140:155], a[60:63], a[96:99], v[140:155]\n"
"ds_read_b64_tr_b16 a[28:29], v38 offset:21504\n"
"s_add_u32 m0, s79, 24576\n"
"ds_read_b64_tr_b16 a[30:31], v38 offset:22016\n"
"buffer_load_dwordx4 v251, s[36:39], s64 offen lds\n"
"v_mfma_f32_32x32x16_bf16 v[0:15], a[64:67], a[96:99], v[0:15]\n"
"ds_read_b64_tr_b16 a[60:61], v39 offset:21504\n"
"ds_read_b64_tr_b16 a[62:63], v39 offset:22016\n"
"s_waitcnt lgkmcnt(9)\n"
"v_mfma_f32_32x32x16_bf16 v[76:91], a[8:11], a[108:111], v[76:91]\n"
"ds_read_b128 a[84:87], v126 offset:28672\n"
"ds_read_b64_tr_b16 a[4:5], v36 offset:22528\n"
"ds_read_b64_tr_b16 a[6:7], v36 offset:23040\n"
"buffer_load_dwordx4 v252, s[36:39], s64 offen offset:1024 lds\n"
"v_mfma_f32_32x32x16_bf16 v[100:115], a[52:55], a[108:111], v[100:115]\n"
"ds_read_b64_tr_b16 a[192:193], v37 offset:22528\n"
"ds_read_b64_tr_b16 a[194:195], v37 offset:23040\n"
"v_mfma_f32_32x32x16_bf16 v[140:155], a[16:19], a[108:111], v[140:155]\n"
"ds_read_b64_tr_b16 a[8:9], v38 offset:22528\n"
"ds_read_b64_tr_b16 a[10:11], v38 offset:23040\n"
"buffer_load_dwordx4 v253, s[36:39], s64 offen offset:2048 lds\n"
"v_mfma_f32_32x32x16_bf16 v[0:15], a[24:27], a[108:111], v[0:15]\n"
"ds_read_b64_tr_b16 a[52:53], v39 offset:22528\n"
"ds_read_b64_tr_b16 a[54:55], v39 offset:23040\n"
"s_waitcnt lgkmcnt(9)\n"
"v_mfma_f32_32x32x16_bf16 v[76:91], a[20:23], a[100:103], v[76:91]\n"
"ds_read_b128 a[64:67], v127 offset:28672\n"
"ds_read_b64_tr_b16 a[24:25], v36 offset:23552\n"
"ds_read_b64_tr_b16 a[26:27], v36 offset:24064\n"
"v_mfma_f32_32x32x16_bf16 v[100:115], a[168:171], a[100:103], v[100:115]\n"
"ds_read_b64_tr_b16 a[40:41], v37 offset:23552\n"
"ds_read_b64_tr_b16 a[42:43], v37 offset:24064\n"
"buffer_load_dwordx4 v254, s[36:39], s64 offen offset:3072 lds\n"
"v_mfma_f32_32x32x16_bf16 v[140:155], a[28:31], a[100:103], v[140:155]\n"
"ds_read_b64_tr_b16 a[16:17], v38 offset:23552\n"
"ds_read_b64_tr_b16 a[18:19], v38 offset:24064\n"
"v_mfma_f32_32x32x16_bf16 v[0:15], a[60:63], a[100:103], v[0:15]\n"
"ds_read_b64_tr_b16 a[28:29], v39 offset:23552\n"
"s_add_u32 m0, s79, 8192\n"
"ds_read_b64_tr_b16 a[30:31], v39 offset:24064\n"
"buffer_load_dwordx4 v184, s[44:47], s74 offen lds\n"
"s_waitcnt lgkmcnt(9)\n"
"v_mfma_f32_32x32x16_bf16 v[76:91], a[4:7], a[84:87], v[76:91]\n"
"s_waitcnt vmcnt(13)\n"
"s_barrier\n"
"buffer_load_dwordx4 v[176:179], v157, s[48:51], 0 offen sc1\n"
"v_mfma_f32_32x32x16_bf16 v[100:115], a[192:195], a[84:87], v[100:115]\n"
"ds_read_b128 a[60:63], v124 offset:32768\n"
"ds_read_b64_tr_b16 a[56:57], v36\n"
"ds_read_b64_tr_b16 a[58:59], v36 offset:512\n"
"v_mfma_f32_32x32x16_bf16 v[140:155], a[8:11], a[84:87], v[140:155]\n"
"ds_read_b64_tr_b16 a[32:33], v37\n"
"ds_read_b64_tr_b16 a[34:35], v37 offset:512\n"
"ds_read_b64_tr_b16 a[8:9], v38\n"
"buffer_load_dwordx4 v185, s[44:47], s74 offen offset:1024 lds\n"
"v_mfma_f32_32x32x16_bf16 v[0:15], a[52:55], a[84:87], v[0:15]\n"
"ds_read_b64_tr_b16 a[10:11], v38 offset:512\n"
"ds_read_b64_tr_b16 a[20:21], v39\n"
"ds_read_b64_tr_b16 a[22:23], v39 offset:512\n"
"s_waitcnt lgkmcnt(9)\n"
"v_mfma_f32_32x32x16_bf16 v[76:91], a[24:27], a[64:67], v[76:91]\n"
"ds_read_b128 a[52:55], v125 offset:32768\n"
"ds_read_b64_tr_b16 a[188:189], v36 offset:1024\n"
"ds_read_b64_tr_b16 a[190:191], v36 offset:1536\n"
"buffer_load_dwordx4 v186, s[44:47], s74 offen offset:2048 lds\n"
"v_mfma_f32_32x32x16_bf16 v[100:115], a[40:43], a[64:67], v[100:115]\n"
"ds_read_b64_tr_b16 a[168:169], v37 offset:1024\n"
"ds_read_b64_tr_b16 a[170:171], v37 offset:1536\n"
"v_mfma_f32_32x32x16_bf16 v[140:155], a[16:19], a[64:67], v[140:155]\n"
"ds_read_b64_tr_b16 a[4:5], v38 offset:1024\n"
"ds_read_b64_tr_b16 a[6:7], v38 offset:1536\n"
"buffer_load_dwordx4 v187, s[44:47], s74 offen offset:3072 lds\n"
"v_mfma_f32_32x32x16_bf16 v[0:15], a[28:31], a[64:67], v[0:15]\n"
"ds_read_b64_tr_b16 a[12:13], v39 offset:1024\n"
"ds_read_b64_tr_b16 a[14:15], v39 offset:1536\n"
"s_waitcnt lgkmcnt(9)\n"
"v_mfma_f32_32x32x16_bf16 v[76:91], a[56:59], a[60:63], v[76:91]\n"
"ds_read_b128 a[40:43], v126 offset:32768\n"
"ds_read_b64_tr_b16 a[44:45], v36 offset:2048\n"
"s_add_u32 m0, s79, 28672\n"
"ds_read_b64_tr_b16 a[46:47], v36 offset:2560\n"
"buffer_load_dwordx4 v251, s[36:39], s65 offen lds\n"
"v_mfma_f32_32x32x16_bf16 v[100:115], a[32:35], a[60:63], v[100:115]\n"
"ds_read_b64_tr_b16 a[192:193], v37 offset:2048\n"
"ds_read_b64_tr_b16 a[194:195], v37 offset:2560\n"
"v_mfma_f32_32x32x16_bf16 v[140:155], a[8:11], a[60:63], v[140:155]\n"
"ds_read_b64_tr_b16 a[16:17], v38 offset:2048\n"
"ds_read_b64_tr_b16 a[18:19], v38 offset:2560\n"
"buffer_load_dwordx4 v252, s[36:39], s65 offen offset:1024 lds\n"
"v_mfma_f32_32x32x16_bf16 v[0:15], a[20:23], a[60:63], v[0:15]\n"
"ds_read_b64_tr_b16 a[24:25], v39 offset:2048\n"
"ds_read_b64_tr_b16 a[26:27], v39 offset:2560\n"
"s_waitcnt lgkmcnt(9)\n"
"v_mfma_f32_32x32x16_bf16 v[76:91], a[188:191], a[52:55], v[76:91]\n"
"ds_read_b128 a[28:31], v127 offset:32768\n"
"ds_read_b64_tr_b16 a[8:9], v36 offset:3072\n"
"ds_read_b64_tr_b16 a[10:11], v36 offset:3584\n"
"buffer_load_dwordx4 v253, s[36:39], s65 offen offset:2048 lds\n"
"v_mfma_f32_32x32x16_bf16 v[100:115], a[168:171], a[52:55], v[100:115]\n"
"ds_read_b64_tr_b16 a[20:21], v37 offset:3072\n"
"ds_read_b64_tr_b16 a[22:23], v37 offset:3584\n"
"v_mfma_f32_32x32x16_bf16 v[140:155], a[4:7], a[52:55], v[140:155]\n"
"ds_read_b64_tr_b16 a[168:169], v38 offset:3072\n"
"ds_read_b64_tr_b16 a[170:171], v38 offset:3584\n"
"v_mfma_f32_32x32x16_bf16 v[0:15], a[12:15], a[52:55], v[0:15]\n"
"ds_read_b64_tr_b16 a[4:5], v39 offset:3072\n"
"ds_read_b64_tr_b16 a[6:7], v39 offset:3584\n"
"buffer_load_dwordx4 v254, s[36:39], s65 offen offset:3072 lds\n"
"s_waitcnt lgkmcnt(9)\n"
"v_mfma_f32_32x32x16_bf16 v[76:91], a[44:47], a[40:43], v[76:91]\n"
"s_waitcnt vmcnt(13)\n"
"s_barrier\n"
"s_add_u32 m0, s79, 12288\n"
"s_nop 0\n"
"buffer_load_dwordx4 v184, s[44:47], s75 offen lds\n"
"v_add_u32 v184, 128, v184\n"
"v_mfma_f32_32x32x16_bf16 v[100:115], a[192:195], a[40:43], v[100:115]\n"
"ds_read_b128 a[32:35], v124 offset:36864\n"
"ds_read_b64_tr_b16 a[188:189], v36 offset:4096\n"
"ds_read_b64_tr_b16 a[190:191], v36 offset:4608\n"
"buffer_load_dwordx4 v185, s[44:47], s75 offen offset:1024 lds\n"
"v_add_u32 v185, 128, v185\n"
"v_mfma_f32_32x32x16_bf16 v[140:155], a[16:19], a[40:43], v[140:155]\n"
"ds_read_b64_tr_b16 a[56:57], v37 offset:4096\n"
"ds_read_b64_tr_b16 a[58:59], v37 offset:4608\n"
"ds_read_b64_tr_b16 a[12:13], v38 offset:4096\n"
"v_mfma_f32_32x32x16_bf16 v[0:15], a[24:27], a[40:43], v[0:15]\n"
"ds_read_b64_tr_b16 a[14:15], v38 offset:4608\n"
"ds_read_b64_tr_b16 a[192:193], v39 offset:4096\n"
"ds_read_b64_tr_b16 a[194:195], v39 offset:4608\n"
"buffer_load_dwordx4 v[20:23], v16, s[48:51], 0 offen sc1\n"
"s_waitcnt lgkmcnt(9)\n"
"v_mfma_f32_32x32x16_bf16 v[76:91], a[8:11], a[28:31], v[76:91]\n"
"ds_read_b128 a[16:19], v125 offset:36864\n"
"ds_read_b64_tr_b16 a[44:45], v36 offset:5120\n"
"ds_read_b64_tr_b16 a[46:47], v36 offset:5632\n"
"v_mfma_f32_32x32x16_bf16 v[100:115], a[20:23], a[28:31], v[100:115]\n"
"ds_read_b64_tr_b16 a[24:25], v37 offset:5120\n"
"ds_read_b64_tr_b16 a[26:27], v37 offset:5632\n"
"v_mfma_f32_32x32x16_bf16 v[140:155], a[168:171], a[28:31], v[140:155]\n"
"ds_read_b64_tr_b16 a[20:21], v38 offset:5120\n"
"ds_read_b64_tr_b16 a[22:23], v38 offset:5632\n"
"buffer_load_dwordx4 v186, s[44:47], s75 offen offset:2048 lds\n"
"v_add_u32 v186, 128, v186\n"
"v_mfma_f32_32x32x16_bf16 v[0:15], a[4:7], a[28:31], v[0:15]\n"
"ds_read_b64_tr_b16 a[176:177], v39 offset:5120\n"
"ds_read_b64_tr_b16 a[178:179], v39 offset:5632\n"
"buffer_load_dwordx4 v[24:27], v16, s[48:51], 0 offen offset:1024 sc1\n"
"s_waitcnt lgkmcnt(9)\n"
"v_mfma_f32_32x32x16_bf16 v[76:91], a[188:191], a[32:35], v[76:91]\n"
"ds_read_b128 a[4:7], v126 offset:36864\n"
"ds_read_b64_tr_b16 a[48:49], v36 offset:6144\n"
"ds_read_b64_tr_b16 a[50:51], v36 offset:6656\n"
"v_mfma_f32_32x32x16_bf16 v[100:115], a[56:59], a[32:35], v[100:115]\n"
"ds_read_b64_tr_b16 a[188:189], v37 offset:6144\n"
"ds_read_b64_tr_b16 a[190:191], v37 offset:6656\n"
"v_mfma_f32_32x32x16_bf16 v[140:155], a[12:15], a[32:35], v[140:155]\n"
"ds_read_b64_tr_b16 a[168:169], v38 offset:6144\n"
"ds_read_b64_tr_b16 a[170:171], v38 offset:6656\n"
"buffer_load_dwordx4 v187, s[44:47], s75 offen offset:3072 lds\n"
"v_add_u32 v187, 128, v187\n"
"v_mfma_f32_32x32x16_bf16 v[0:15], a[192:195], a[32:35], v[0:15]\n"
"ds_read_b64_tr_b16 a[12:13], v39 offset:6144\n"
"ds_read_b64_tr_b16 a[14:15], v39 offset:6656\n"
"buffer_load_dwordx4 v[28:31], v16, s[48:51], 0 offen offset:2048 sc1\n"
"s_waitcnt lgkmcnt(9)\n"
"v_mfma_f32_32x32x16_bf16 v[76:91], a[44:47], a[16:19], v[76:91]\n"
"ds_read_b128 a[8:11], v127 offset:36864\n"
"ds_read_b64_tr_b16 a[56:57], v36 offset:7168\n"
"ds_read_b64_tr_b16 a[58:59], v36 offset:7680\n"
"v_mfma_f32_32x32x16_bf16 v[100:115], a[24:27], a[16:19], v[100:115]\n"
"ds_read_b64_tr_b16 a[192:193], v37 offset:7168\n"
"ds_read_b64_tr_b16 a[194:195], v37 offset:7680\n"
"v_mfma_f32_32x32x16_bf16 v[140:155], a[20:23], a[16:19], v[140:155]\n"
"ds_read_b64_tr_b16 a[44:45], v38 offset:7168\n"
"ds_read_b64_tr_b16 a[46:47], v38 offset:7680\n"
"buffer_load_dwordx4 v[120:123], v16, s[48:51], 0 offen offset:3072 sc1\n"
"v_mfma_f32_32x32x16_bf16 v[0:15], a[176:179], a[16:19], v[0:15]\n"
"ds_read_b64_tr_b16 a[0:1], v39 offset:7168\n"
"s_add_u32 m0, s79, 16384\n"
"ds_read_b64_tr_b16 a[2:3], v39 offset:7680\n"
"buffer_load_dwordx4 v184, s[44:47], s66 offen lds\n"
"s_waitcnt lgkmcnt(9)\n"
"v_mfma_f32_32x32x16_bf16 v[76:91], a[48:51], a[4:7], v[76:91]\n"
"s_waitcnt vmcnt(13)\n"
"s_barrier\n"
"buffer_load_dwordx4 v185, s[44:47], s66 offen offset:1024 lds\n"
"v_mfma_f32_32x32x16_bf16 v[100:115], a[188:191], a[4:7], v[100:115]\n"
"ds_read_b128 a[20:23], v124 offset:24576\n"
"ds_read_b64_tr_b16 a[48:49], v36 offset:8192\n"
"ds_read_b64_tr_b16 a[50:51], v36 offset:8704\n"
"v_mfma_f32_32x32x16_bf16 v[140:155], a[168:171], a[4:7], v[140:155]\n"
"ds_read_b64_tr_b16 a[36:37], v37 offset:8192\n"
"ds_read_b64_tr_b16 a[38:39], v37 offset:8704\n"
"ds_read_b64_tr_b16 a[168:169], v38 offset:8192\n"
"buffer_load_dwordx4 v186, s[44:47], s66 offen offset:2048 lds\n"
"v_mfma_f32_32x32x16_bf16 v[0:15], a[12:15], a[4:7], v[0:15]\n"
"ds_read_b64_tr_b16 a[170:171], v38 offset:8704\n"
"ds_read_b64_tr_b16 a[24:25], v39 offset:8192\n"
"ds_read_b64_tr_b16 a[26:27], v39 offset:8704\n"
"s_waitcnt lgkmcnt(9)\n"
"v_mfma_f32_32x32x16_bf16 v[76:91], a[56:59], a[8:11], v[76:91]\n"
"ds_read_b128 a[12:15], v125 offset:24576\n"
"ds_read_b64_tr_b16 a[176:177], v36 offset:9216\n"
"ds_read_b64_tr_b16 a[178:179], v36 offset:9728\n"
"buffer_load_dwordx4 v187, s[44:47], s66 offen offset:3072 lds\n"
"v_mfma_f32_32x32x16_bf16 v[100:115], a[192:195], a[8:11], v[100:115]\n"
"ds_read_b64_tr_b16 a[188:189], v37 offset:9216\n"
"ds_read_b64_tr_b16 a[190:191], v37 offset:9728\n"
"v_mfma_f32_32x32x16_bf16 v[140:155], a[44:47], a[8:11], v[140:155]\n"
"ds_read_b64_tr_b16 a[192:193], v38 offset:9216\n"
"s_add_u32 m0, s79, 20480\n"
"ds_read_b64_tr_b16 a[194:195], v38 offset:9728\n"
"buffer_load_dwordx4 v184, s[44:47], s67 offen lds\n"
"v_mfma_f32_32x32x16_bf16 v[0:15], a[0:3], a[8:11], v[0:15]\n"
"ds_read_b64_tr_b16 a[44:45], v39 offset:9216\n"
"ds_read_b64_tr_b16 a[46:47], v39 offset:9728\n"
"s_waitcnt lgkmcnt(9)\n"
"v_mfma_f32_32x32x16_bf16 v[76:91], a[48:51], a[20:23], v[76:91]\n"
"ds_read_b128 a[0:3], v126 offset:24576\n"
"ds_read_b64_tr_b16 a[56:57], v36 offset:10240\n"
"ds_read_b64_tr_b16 a[58:59], v36 offset:10752\n"
"buffer_load_dwordx4 v185, s[44:47], s67 offen offset:1024 lds\n"
"v_mfma_f32_32x32x16_bf16 v[100:115], a[36:39], a[20:23], v[100:115]\n"
"ds_read_b64_tr_b16 a[48:49], v37 offset:10240\n"
"ds_read_b64_tr_b16 a[50:51], v37 offset:10752\n"
"v_xor_b32 v41, s80, v177\n"
"v_bitop3_b32 v41, v179, s81, v41 bitop3:0xbe\n"
"v_cmp_ne_u32 vcc, 0, v41\n"
"s_nop 1\n"
"s_cmp_lg_u64 vcc, 0\n"
"s_cbranch_scc1 SLOWAS_%=\n"
"BACKAS_%=:\n"
"v_mfma_f32_32x32x16_bf16 v[140:155], a[168:171], a[20:23], v[140:155]\n"
"ds_read_b64_tr_b16 a[36:37], v38 offset:10240\n"
"ds_read_b64_tr_b16 a[38:39], v38 offset:10752\n"
"buffer_load_dwordx4 v186, s[44:47], s67 offen offset:2048 lds\n"
"v_and_b32 v41, s82, v176\n"
"v_mfma_f32_32x32x16_bf16 v[0:15], a[24:27], a[20:23], v[0:15]\n"
"ds_read_b64_tr_b16 a[168:169], v39 offset:10240\n"
"ds_read_b64_tr_b16 a[170:171], v39 offset:10752\n"
"v_lshlrev_b32 v128, 16, v176\n"
"v_rcp_f32 v137, v41\n"
"ds_write_b32 v156, v176 offset:40704\n"
"s_waitcnt lgkmcnt(10)\n"
"v_mfma_f32_32x32x16_bf16 v[76:91], a[176:179], a[12:15], v[76:91]\n"
"ds_read_b128 a[24:27], v127 offset:24576\n"
"ds_read_b64_tr_b16 a[180:181], v36 offset:11264\n"
"ds_read_b64_tr_b16 a[182:183], v36 offset:11776\n"
"buffer_load_dwordx4 v187, s[44:47], s67 offen offset:3072 lds\n"
"v_mfma_f32_32x32x16_bf16 v[100:115], a[188:191], a[12:15], v[100:115]\n"
"ds_read_b64_tr_b16 a[176:177], v37 offset:11264\n"
"ds_read_b64_tr_b16 a[178:179], v37 offset:11776\n"
"v_lshrrev_b32 v178, 16, v128\n"
"v_mul_f32 v156, v41, v41\n"
"v_or_b32 v176, v128, v178\n"
"v_mfma_f32_32x32x16_bf16 v[140:155], a[192:195], a[12:15], v[140:155]\n"
"ds_read_b64_tr_b16 a[184:185], v38 offset:11264\n"
"s_add_u32 m0, s79, 0\n"
"ds_read_b64_tr_b16 a[186:187], v38 offset:11776\n"
"buffer_load_dwordx4 v184, s[44:47], s68 offen lds\n"
"v_mov_b32 v177, 0\n"
"v_mfma_f32_32x32x16_bf16 v[0:15], a[44:47], a[12:15], v[0:15]\n"
"ds_read_b64_tr_b16 a[188:189], v39 offset:11264\n"
"ds_read_b64_tr_b16 a[190:191], v39 offset:11776\n"
"v_mov_b32 v179, 0\n"
"v_mul_f32 v41, v137, v137\n"
"v_mul_f32 v158, 0xc0135761, v137\n"
"v_mul_f32 v41, v41, v158\n"
"v_mul_f32 v136, 0x3d372713, v41\n"
"buffer_load_dwordx4 v185, s[44:47], s68 offen offset:1024 lds\n"
"buffer_load_dwordx4 v186, s[44:47], s68 offen offset:2048 lds\n"
"s_waitcnt lgkmcnt(10)\n"
"v_mfma_f32_32x32x16_bf16 v[76:91], a[56:59], a[0:3], v[76:91]\n"
"s_waitcnt vmcnt(13)\n"
"s_barrier\n"
"buffer_load_dwordx4 v187, s[44:47], s68 offen offset:3072 lds\n"
"v_mfma_f32_32x32x16_bf16 v[100:115], a[48:51], a[0:3], v[100:115]\n"
"ds_read_b128 a[56:59], v124 offset:28672\n"
"ds_read_b64_tr_b16 a[160:161], v36 offset:12288\n"
"ds_read_b64_tr_b16 a[162:163], v36 offset:12800\n"
"v_mfma_f32_32x32x16_bf16 v[140:155], a[36:39], a[0:3], v[140:155]\n"
"ds_read_b64_tr_b16 a[172:173], v37 offset:12288\n"
"ds_read_b64_tr_b16 a[174:175], v37 offset:12800\n"
"s_add_u32 m0, s79, 4096\n"
"ds_read_b64_tr_b16 a[36:37], v38 offset:12288\n"
"buffer_load_dwordx4 v184, s[44:47], s69 offen lds\n"
"v_mfma_f32_32x32x16_bf16 v[0:15], a[168:171], a[0:3], v[0:15]\n"
"ds_read_b64_tr_b16 a[38:39], v38 offset:12800\n"
"ds_read_b64_tr_b16 a[192:193], v39 offset:12288\n"
"ds_read_b64_tr_b16 a[194:195], v39 offset:12800\n"
"s_waitcnt lgkmcnt(9)\n"
"v_mfma_f32_32x32x16_bf16 v[76:91], a[180:183], a[24:27], v[76:91]\n"
"ds_read_b128 a[48:51], v125 offset:28672\n"
"ds_read_b64_tr_b16 a[168:169], v36 offset:13312\n"
"ds_read_b64_tr_b16 a[170:171], v36 offset:13824\n"
"buffer_load_dwordx4 v185, s[44:47], s69 offen offset:1024 lds\n"
"v_mfma_f32_32x32x16_bf16 v[100:115], a[176:179], a[24:27], v[100:115]\n"
"ds_read_b64_tr_b16 a[180:181], v37 offset:13312\n"
"ds_read_b64_tr_b16 a[182:183], v37 offset:13824\n"
"v_mfma_f32_32x32x16_bf16 v[140:155], a[184:187], a[24:27], v[140:155]\n"
"ds_read_b64_tr_b16 a[176:177], v38 offset:13312\n"
"ds_read_b64_tr_b16 a[178:179], v38 offset:13824\n"
"buffer_load_dwordx4 v186, s[44:47], s69 offen offset:2048 lds\n"
"v_xor_b32 v128, s8, v21\n"
"v_bitop3_b32 v128, v23, s9, v128 bitop3:0xbe\n"
"v_mfma_f32_32x32x16_bf16 v[0:15], a[188:191], a[24:27], v[0:15]\n"
"ds_read_b64_tr_b16 a[184:185], v39 offset:13312\n"
"ds_read_b64_tr_b16 a[186:187], v39 offset:13824\n"
"v_bitop3_b32 v128, v25, s8, v128 bitop3:0xbe\n"
"v_bitop3_b32 v128, v27, s9, v128 bitop3:0xbe\n"
"s_waitcnt vmcnt(16)\n"
"v_bitop3_b32 v128, v29, s8, v128 bitop3:0xbe\n"
"v_bitop3_b32 v128, v31, s9, v128 bitop3:0xbe\n"
"s_waitcnt lgkmcnt(9)\n"
"v_mfma_f32_32x32x16_bf16 v[76:91], a[160:163], a[56:59], v[76:91]\n"
"ds_read_b128 a[44:47], v126 offset:28672\n"
"ds_read_b64_tr_b16 a[188:189], v36 offset:14336\n"
"ds_read_b64_tr_b16 a[190:191], v36 offset:14848\n"
"buffer_load_dwordx2 v[40:41], v193, s[48:51], 0 offen sc1\n"
"v_mfma_f32_32x32x16_bf16 v[100:115], a[172:175], a[56:59], v[100:115]\n"
"ds_read_b64_tr_b16 a[160:161], v37 offset:14336\n"
"ds_read_b64_tr_b16 a[162:163], v37 offset:14848\n"
"s_waitcnt vmcnt(16)\n"
"v_bitop3_b32 v128, v121, s8, v128 bitop3:0xbe\n"
"v_bitop3_b32 v128, v123, s9, v128 bitop3:0xbe\n"
"v_cmp_ne_u32 vcc, 0, v128\n"
"s_nop 1\n"
"s_cmp_lg_u64 vcc, 0\n"
"s_cbranch_scc1 SLOWAX0_%=\n"
"BACKAX0_%=:\n"
"buffer_load_dwordx4 v187, s[44:47], s69 offen offset:3072 lds\n"
"v_mfma_f32_32x32x16_bf16 v[140:155], a[36:39], a[56:59], v[140:155]\n"
"ds_read_b64_tr_b16 a[220:221], v38 offset:14336\n"
"ds_read_b64_tr_b16 a[222:223], v38 offset:14848\n"
"v_mfma_f32_32x32x16_bf16 v[0:15], a[192:195], a[56:59], v[0:15]\n"
"ds_read_b64_tr_b16 a[204:205], v39 offset:14336\n"
"ds_read_b64_tr_b16 a[206:207], v39 offset:14848\n"
"s_waitcnt lgkmcnt(9)\n"
"v_mfma_f32_32x32x16_bf16 v[76:91], a[168:171], a[48:51], v[76:91]\n"
"ds_read_b128 a[36:39], v127 offset:28672\n"
"ds_read_b64_tr_b16 a[216:217], v36 offset:15360\n"
"ds_read_b64_tr_b16 a[218:219], v36 offset:15872\n"
"v_mfma_f32_32x32x16_bf16 v[100:115], a[180:183], a[48:51], v[100:115]\n"
"ds_read_b64_tr_b16 a[208:209], v37 offset:15360\n"
"ds_read_b64_tr_b16 a[210:211], v37 offset:15872\n"
"v_mfma_f32_32x32x16_bf16 v[140:155], a[176:179], a[48:51], v[140:155]\n"
"ds_read_b64_tr_b16 a[212:213], v38 offset:15360\n"
"ds_read_b64_tr_b16 a[214:215], v38 offset:15872\n"
"v_mfma_f32_32x32x16_bf16 v[0:15], a[184:187], a[48:51], v[0:15]\n"
"ds_read_b64_tr_b16 a[200:201], v39 offset:15360\n"
"ds_read_b64_tr_b16 a[202:203], v39 offset:15872\n"
"s_waitcnt lgkmcnt(9)\n"
"v_mfma_f32_32x32x16_bf16 v[76:91], a[188:191], a[44:47], v[76:91]\n"
"s_waitcnt vmcnt(13)\n"
"s_barrier\n"
"v_mfma_f32_32x32x16_bf16 v[100:115], a[160:163], a[44:47], v[100:115]\n"
"ds_read_b64_tr_b16 a[196:197], v36 offset:16384\n"
"ds_read_b64_tr_b16 a[198:199], v36 offset:16896\n"
"s_add_u32 m0, s79, 8192\n"
"ds_read_b64_tr_b16 a[164:165], v37 offset:16384\n"
"buffer_load_dwordx4 v184, s[44:47], s70 offen lds\n"
"v_mfma_f32_32x32x16_bf16 v[140:155], a[220:223], a[44:47], v[140:155]\n"
"ds_read_b64_tr_b16 a[166:167], v37 offset:16896\n"
"ds_read_b64_tr_b16 a[172:173], v38 offset:16384\n"
"ds_read_b64_tr_b16 a[174:175], v38 offset:16896\n"
"buffer_load_dwordx4 v185, s[44:47], s70 offen offset:1024 lds\n"
"v_mfma_f32_32x32x16_bf16 v[0:15], a[204:207], a[44:47], v[0:15]\n"
"ds_read_b64_tr_b16 a[168:169], v39 offset:16384\n"
"ds_read_b64_tr_b16 a[170:171], v39 offset:16896\n"
"s_waitcnt lgkmcnt(8)\n"
"v_mfma_f32_32x32x16_bf16 v[76:91], a[216:219], a[36:39], v[76:91]\n"
"ds_read_b64_tr_b16 a[192:193], v36 offset:17408\n"
"ds_read_b64_tr_b16 a[194:195], v36 offset:17920\n"
"buffer_load_dwordx4 v186, s[44:47], s70 offen offset:2048 lds\n"
"v_mfma_f32_32x32x16_bf16 v[100:115], a[208:211], a[36:39], v[100:115]\n"
"ds_read_b64_tr_b16 a[176:177], v37 offset:17408\n"
"ds_read_b64_tr_b16 a[178:179], v37 offset:17920\n"
"v_mfma_f32_32x32x16_bf16 v[140:155], a[212:215], a[36:39], v[140:155]\n"
"ds_read_b64_tr_b16 a[180:181], v38 offset:17408\n"
"ds_read_b64_tr_b16 a[182:183], v38 offset:17920\n"
"buffer_load_dwordx4 v187, s[44:47], s70 offen offset:3072 lds\n"
"v_mfma_f32_32x32x16_bf16 v[0:15], a[200:203], a[36:39], v[0:15]\n"
"ds_read_b64_tr_b16 a[188:189], v39 offset:17408\n"
"ds_read_b64_tr_b16 a[190:191], v39 offset:17920\n"
"v_mfma_f32_32x32x16_bf16 v[76:91], v[20:23], v[176:179], v[76:91]\n"
"v_mfma_f32_32x32x16_bf16 v[100:115], v[24:27], v[176:179], v[100:115]\n"
"v_mfma_f32_32x32x16_bf16 v[140:155], v[28:31], v[176:179], v[140:155]\n"
"v_mfma_f32_32x32x16_bf16 v[0:15], v[120:123], v[176:179], v[0:15]\n"
"s_waitcnt lgkmcnt(8)\n"
"v_mfma_f32_32x32x16_bf16 v[20:35], a[196:199], a[156:159], 0\n"
"ds_read_b64_tr_b16 a[184:185], v36 offset:18432\n"
"ds_read_b64_tr_b16 a[186:187], v36 offset:18944\n"
"v_mfma_f32_32x32x16_bf16 v[120:135], a[164:167], a[156:159], 0\n"
"ds_read_b64_tr_b16 a[160:161], v37 offset:18432\n"
"ds_read_b64_tr_b16 a[162:163], v37 offset:18944\n"
"s_nop 1\n"
"v_mul_f32 v180, v76, v76\n"
"v_mul_f32 v181, v77, v77\n"
"v_mul_f32 v182, v78, v78\n"
"v_mul_f32 v183, v79, v79\n"
"v_fma_f32 v180, v180, v136, v158\n"
"v_fma_f32 v181, v181, v136, v158\n"
"v_fma_f32 v182, v182, v136, v158\n"
"v_fma_f32 v183, v183, v136, v158\n"
"v_mfma_f32_32x32x16_bf16 v[160:175], a[172:175], a[156:159], 0\n"
"ds_read_b64_tr_b16 a[164:165], v38 offset:18432\n"
"ds_read_b64_tr_b16 a[166:167], v38 offset:18944\n"
"v_mul_f32 v180, v180, v76\n"
"v_mul_f32 v181, v181, v77\n"
"v_mul_f32 v182, v182, v78\n"
"v_mul_f32 v183, v183, v79\n"
"v_exp_f32 v180, v180\n"
"v_mul_f32 v84, v76, v84\n"
"v_exp_f32 v181, v181\n"
"v_mfma_f32_32x32x16_bf16 v[60:75], a[168:171], a[156:159], 0\n"
"ds_read_b64_tr_b16 a[172:173], v39 offset:18432\n"
"ds_read_b64_tr_b16 a[174:175], v39 offset:18944\n"
"v_mul_f32 v85, v77, v85\n"
"v_exp_f32 v182, v182\n"
"v_mul_f32 v86, v78, v86\n"
"v_exp_f32 v183, v183\n"
"v_mul_f32 v87, v79, v87\n"
"v_fma_f32 v180, v180, v156, v156\n"
"v_fma_f32 v181, v181, v156, v156\n"
"s_waitcnt lgkmcnt(8)\n"
"v_mfma_f32_32x32x16_bf16 v[20:35], a[192:195], a[152:155], v[20:35]\n"
"ds_read_b64_tr_b16 a[168:169], v36 offset:19456\n"
"ds_read_b64_tr_b16 a[170:171], v36 offset:19968\n"
"v_fma_f32 v182, v182, v156, v156\n"
"v_fma_f32 v183, v183, v156, v156\n"
"v_rcp_f32 v180, v180\n"
"v_rcp_f32 v181, v181\n"
"v_rcp_f32 v182, v182\n"
"v_mfma_f32_32x32x16_bf16 v[120:135], a[176:179], a[152:155], v[120:135]\n"
"ds_read_b64_tr_b16 a[156:157], v37 offset:19456\n"
"ds_read_b64_tr_b16 a[158:159], v37 offset:19968\n"
"v_rcp_f32 v183, v183\n"
"v_mul_f32 v84, v84, v180\n"
"v_mul_f32 v85, v85, v181\n"
"v_mul_f32 v86, v86, v182\n"
"v_mul_f32 v87, v87, v183\n"
"v_mul_f32 v180, v80, v80\n"
"v_mul_f32 v181, v81, v81\n"
"v_mul_f32 v182, v82, v82\n"
"v_mfma_f32_32x32x16_bf16 v[160:175], a[180:183], a[152:155], v[160:175]\n"
"ds_read_b64_tr_b16 a[176:177], v38 offset:19456\n"
"ds_read_b64_tr_b16 a[178:179], v38 offset:19968\n"
"v_mul_f32 v183, v83, v83\n"
"v_fma_f32 v180, v180, v136, v158\n"
"v_fma_f32 v181, v181, v136, v158\n"
"v_fma_f32 v182, v182, v136, v158\n"
"v_fma_f32 v183, v183, v136, v158\n"
"v_mul_f32 v180, v180, v80\n"
"v_mul_f32 v181, v181, v81\n"
"v_mul_f32 v182, v182, v82\n"
"v_mfma_f32_32x32x16_bf16 v[60:75], a[188:191], a[152:155], v[60:75]\n"
"ds_read_b64_tr_b16 a[180:181], v39 offset:19456\n"
"ds_read_b64_tr_b16 a[182:183], v39 offset:19968\n"
"v_mul_f32 v183, v183, v83\n"
"v_exp_f32 v180, v180\n"
"v_mul_f32 v88, v80, v88\n"
"v_exp_f32 v181, v181\n"
"v_mul_f32 v89, v81, v89\n"
"v_exp_f32 v182, v182\n"
"s_waitcnt lgkmcnt(8)\n"
"v_mfma_f32_32x32x16_bf16 v[20:35], a[184:187], a[148:151], v[20:35]\n"
"s_waitcnt vmcnt(13)\n"
"s_barrier\n"
"v_mul_f32 v90, v82, v90\n"
"v_exp_f32 v183, v183\n"
"v_mul_f32 v91, v83, v91\n"
"s_waitcnt vmcnt(5)\n"
"v_xor_b32 v252, s80, v40\n"
"v_bitop3_b32 v252, v41, s81, v252 bitop3:0xbe\n"
"v_cmp_ne_u32 vcc, 0, v252\n"
"s_nop 1\n"
"s_cmp_lg_u64 vcc, 0\n"
"s_cbranch_scc1 SLOWAJC_%=\n"
"BACKAJC_%=:\n"
"s_add_u32 m0, s79, 28672\n"
"s_add_u32 s95, s96, 10240\n"
"buffer_load_dwordx4 v189, s[48:51], s95 offen sc1 lds\n"
"v_mfma_f32_32x32x16_bf16 v[120:135], a[160:163], a[148:151], v[120:135]\n"
"ds_read_b64_tr_b16 a[152:153], v36 offset:20480\n"
"ds_read_b64_tr_b16 a[154:155], v36 offset:20992\n"
"ds_read_b64_tr_b16 a[160:161], v37 offset:20480\n"
"s_add_u32 m0, s79, 12288\n"
"v_fma_f32 v180, v180, v156, v156\n"
"buffer_load_dwordx4 v184, s[44:47], s71 offen lds\n"
"v_fma_f32 v181, v181, v156, v156\n"
"v_fma_f32 v182, v182, v156, v156\n"
"v_mfma_f32_32x32x16_bf16 v[160:175], a[164:167], a[148:151], v[160:175]\n"
"ds_read_b64_tr_b16 a[162:163], v37 offset:20992\n"
"ds_read_b64_tr_b16 a[184:185], v38 offset:20480\n"
"ds_read_b64_tr_b16 a[186:187], v38 offset:20992\n"
"v_fma_f32 v183, v183, v156, v156\n"
"v_rcp_f32 v180, v180\n"
"v_rcp_f32 v181, v181\n"
"v_rcp_f32 v182, v182\n"
"buffer_load_dwordx4 v185, s[44:47], s71 offen offset:1024 lds\n"
"v_mfma_f32_32x32x16_bf16 v[60:75], a[172:175], a[148:151], v[60:75]\n"
"ds_read_b64_tr_b16 a[164:165], v39 offset:20480\n"
"ds_read_b64_tr_b16 a[166:167], v39 offset:20992\n"
"v_rcp_f32 v183, v183\n"
"v_mul_f32 v88, v88, v180\n"
"v_mul_f32 v89, v89, v181\n"
"v_mul_f32 v90, v90, v182\n"
"v_mul_f32 v91, v91, v183\n"
"v_cvt_pk_bf16_f32 v180, v84, v85\n"
"v_cvt_pk_bf16_f32 v181, v86, v87\n"
"v_cvt_pk_bf16_f32 v182, v88, v89\n"
"s_waitcnt lgkmcnt(8)\n"
"v_mfma_f32_32x32x16_bf16 v[20:35], a[168:171], a[144:147], v[20:35]\n"
"ds_read_b64_tr_b16 a[148:149], v36 offset:21504\n"
"ds_read_b64_tr_b16 a[150:151], v36 offset:22016\n"
"buffer_load_dwordx4 v186, s[44:47], s71 offen offset:2048 lds\n"
"v_cvt_pk_bf16_f32 v183, v90, v91\n"
"v_mul_f32 v76, v100, v100\n"
"v_mul_f32 v77, v101, v101\n"
"v_mul_f32 v78, v102, v102\n"
"v_mul_f32 v79, v103, v103\n"
"v_mfma_f32_32x32x16_bf16 v[120:135], a[156:159], a[144:147], v[120:135]\n"
"ds_read_b64_tr_b16 a[168:169], v37 offset:21504\n"
"ds_read_b64_tr_b16 a[170:171], v37 offset:22016\n"
"v_fma_f32 v76, v76, v136, v158\n"
"v_fma_f32 v77, v77, v136, v158\n"
"buffer_load_dwordx4 v187, s[44:47], s71 offen offset:3072 lds\n"
"v_fma_f32 v78, v78, v136, v158\n"
"v_fma_f32 v79, v79, v136, v158\n"
"v_mul_f32 v76, v76, v100\n"
"v_mfma_f32_32x32x16_bf16 v[160:175], a[176:179], a[144:147], v[160:175]\n"
"ds_read_b64_tr_b16 a[156:157], v38 offset:21504\n"
"ds_read_b64_tr_b16 a[158:159], v38 offset:22016\n"
"v_mul_f32 v77, v77, v101\n"
"v_mul_f32 v78, v78, v102\n"
"v_mul_f32 v79, v79, v103\n"
"v_exp_f32 v76, v76\n"
"v_mul_f32 v108, v100, v108\n"
"s_add_u32 m0, s79, 28672\n"
"v_exp_f32 v77, v77\n"
"buffer_load_dwordx4 v189, s[48:51], s95 offen offset:1024 sc1 lds\n"
"v_mfma_f32_32x32x16_bf16 v[60:75], a[180:183], a[144:147], v[60:75]\n"
"ds_read_b64_tr_b16 a[176:177], v39 offset:21504\n"
"ds_read_b64_tr_b16 a[178:179], v39 offset:22016\n"
"v_mul_f32 v109, v101, v109\n"
"v_exp_f32 v78, v78\n"
"v_mul_f32 v110, v102, v110\n"
"v_exp_f32 v79, v79\n"
"v_mul_f32 v111, v103, v111\n"
"v_fma_f32 v76, v76, v156, v156\n"
"v_fma_f32 v77, v77, v156, v156\n"
"s_waitcnt lgkmcnt(8)\n"
"v_mfma_f32_32x32x16_bf16 v[20:35], a[152:155], a[140:143], v[20:35]\n"
"ds_read_b64_tr_b16 a[172:173], v36 offset:22528\n"
"ds_read_b64_tr_b16 a[174:175], v36 offset:23040\n"
"buffer_load_dwordx4 v189, s[48:51], s95 offen offset:2048 sc1 lds\n"
"v_fma_f32 v78, v78, v156, v156\n"
"v_fma_f32 v79, v79, v156, v156\n"
"v_rcp_f32 v76, v76\n"
"v_mfma_f32_32x32x16_bf16 v[120:135], a[160:163], a[140:143], v[120:135]\n"
"ds_read_b64_tr_b16 a[152:153], v37 offset:22528\n"
"ds_read_b64_tr_b16 a[154:155], v37 offset:23040\n"
"v_rcp_f32 v77, v77\n"
"v_rcp_f32 v78, v78\n"
"buffer_load_dwordx4 v189, s[48:51], s95 offen offset:3072 sc1 lds\n"
"v_rcp_f32 v79, v79\n"
"v_mfma_f32_32x32x16_bf16 v[160:175], a[184:187], a[140:143], v[160:175]\n"
"ds_read_b64_tr_b16 a[144:145], v38 offset:22528\n"
"ds_read_b64_tr_b16 a[146:147], v38 offset:23040\n"
"v_mul_f32 v108, v108, v76\n"
"v_mul_f32 v109, v109, v77\n"
"v_mul_f32 v110, v110, v78\n"
"v_mul_f32 v111, v111, v79\n"
"v_mul_f32 v76, v104, v104\n"
"v_mul_f32 v77, v105, v105\n"
"v_mul_f32 v78, v106, v106\n"
"v_mul_f32 v79, v107, v107\n"
"s_add_u32 m0, s79, 32768\n"
"s_add_u32 s95, s96, 14336\n"
"buffer_load_dwordx4 v189, s[48:51], s95 offen sc1 lds\n"
"v_mfma_f32_32x32x16_bf16 v[60:75], a[164:167], a[140:143], v[60:75]\n"
"ds_read_b64_tr_b16 a[184:185], v39 offset:22528\n"
"ds_read_b64_tr_b16 a[186:187], v39 offset:23040\n"
"v_fma_f32 v76, v76, v136, v158\n"
"v_fma_f32 v77, v77, v136, v158\n"
"v_fma_f32 v78, v78, v136, v158\n"
"v_fma_f32 v79, v79, v136, v158\n"
"v_mul_f32 v76, v76, v104\n"
"v_mul_f32 v77, v77, v105\n"
"v_mul_f32 v78, v78, v106\n"
"v_mul_f32 v79, v79, v107\n"
"s_waitcnt lgkmcnt(8)\n"
"v_mfma_f32_32x32x16_bf16 v[20:35], a[148:151], a[136:139], v[20:35]\n"
"ds_read_b64_tr_b16 a[164:165], v36 offset:23552\n"
"ds_read_b64_tr_b16 a[166:167], v36 offset:24064\n"
"buffer_load_dwordx4 v189, s[48:51], s95 offen offset:1024 sc1 lds\n"
"v_exp_f32 v76, v76\n"
"v_mul_f32 v112, v104, v112\n"
"v_exp_f32 v77, v77\n"
"v_mfma_f32_32x32x16_bf16 v[120:135], a[168:171], a[136:139], v[120:135]\n"
"ds_read_b64_tr_b16 a[180:181], v37 offset:23552\n"
"ds_read_b64_tr_b16 a[182:183], v37 offset:24064\n"
"v_mul_f32 v113, v105, v113\n"
"v_exp_f32 v78, v78\n"
"v_mul_f32 v114, v106, v114\n"
"buffer_load_dwordx4 v189, s[48:51], s95 offen offset:2048 sc1 lds\n"
"v_exp_f32 v79, v79\n"
"v_mfma_f32_32x32x16_bf16 v[160:175], a[156:159], a[136:139], v[160:175]\n"
"ds_read_b64_tr_b16 a[148:149], v38 offset:23552\n"
"ds_read_b64_tr_b16 a[150:151], v38 offset:24064\n"
"v_mul_f32 v115, v107, v115\n"
"v_fma_f32 v76, v76, v156, v156\n"
"v_fma_f32 v77, v77, v156, v156\n"
"v_fma_f32 v78, v78, v156, v156\n"
"v_fma_f32 v79, v79, v156, v156\n"
"v_rcp_f32 v76, v76\n"
"v_mfma_f32_32x32x16_bf16 v[60:75], a[176:179], a[136:139], v[60:75]\n"
"ds_read_b64_tr_b16 a[160:161], v39 offset:23552\n"
"ds_read_b64_tr_b16 a[162:163], v39 offset:24064\n"
"buffer_load_dwordx4 a[176:179], v189, s[48:51], s95 offen offset:3072 sc1\n"
"v_rcp_f32 v77, v77\n"
"v_rcp_f32 v78, v78\n"
"v_rcp_f32 v79, v79\n"
"s_waitcnt lgkmcnt(8)\n"
"v_mfma_f32_32x32x16_bf16 v[20:35], a[172:175], a[132:135], v[20:35]\n"
"s_barrier\n"
"s_add_u32 s95, s96, 18432\n"
"buffer_load_dwordx4 a[188:191], v189, s[48:51], s95 offen sc1\n"
"v_mul_f32 v112, v112, v76\n"
"v_mul_f32 v113, v113, v77\n"
"v_mul_f32 v114, v114, v78\n"
"v_mfma_f32_32x32x16_bf16 v[120:135], a[152:155], a[132:135], v[120:135]\n"
"ds_read_b64_tr_b16 a[172:173], v36\n"
"ds_read_b64_tr_b16 a[174:175], v36 offset:512\n"
"ds_read_b64_tr_b16 a[156:157], v37\n"
"v_mul_f32 v115, v115, v79\n"
"v_cvt_pk_bf16_f32 v76, v108, v109\n"
"v_cvt_pk_bf16_f32 v77, v110, v111\n"
"v_cvt_pk_bf16_f32 v78, v112, v113\n"
"v_cvt_pk_bf16_f32 v79, v114, v115\n"
"s_add_u32 m0, s79, 16384\n"
"v_mul_f32 v100, v140, v140\n"
"buffer_load_dwordx4 v184, s[44:47], s72 offen lds\n"
"v_mfma_f32_32x32x16_bf16 v[160:175], a[144:147], a[132:135], v[160:175]\n"
"ds_read_b64_tr_b16 a[158:159], v37 offset:512\n"
"ds_read_b64_tr_b16 a[136:137], v38\n"
"ds_read_b64_tr_b16 a[138:139], v38 offset:512\n"
"v_mul_f32 v101, v141, v141\n"
"v_mul_f32 v102, v142, v142\n"
"v_mul_f32 v103, v143, v143\n"
"v_fma_f32 v100, v100, v136, v158\n"
"v_fma_f32 v101, v101, v136, v158\n"
"v_fma_f32 v102, v102, v136, v158\n"
"v_fma_f32 v103, v103, v136, v158\n"
"v_mfma_f32_32x32x16_bf16 v[60:75], a[184:187], a[132:135], v[60:75]\n"
"ds_read_b64_tr_b16 a[168:169], v39\n"
"ds_read_b64_tr_b16 a[170:171], v39 offset:512\n"
"buffer_load_dwordx4 v185, s[44:47], s72 offen offset:1024 lds\n"
"v_mul_f32 v100, v100, v140\n"
"v_mul_f32 v101, v101, v141\n"
"v_mul_f32 v102, v102, v142\n"
"v_mul_f32 v103, v103, v143\n"
"v_exp_f32 v100, v100\n"
"s_waitcnt lgkmcnt(8)\n"
"v_mfma_f32_32x32x16_bf16 v[20:35], a[164:167], a[128:131], v[20:35]\n"
"ds_read_b64_tr_b16 a[140:141], v36 offset:1024\n"
"ds_read_b64_tr_b16 a[142:143], v36 offset:1536\n"
"v_mul_f32 v148, v140, v148\n"
"buffer_load_dwordx4 v186, s[44:47], s72 offen offset:2048 lds\n"
"v_exp_f32 v101, v101\n"
"v_mul_f32 v149, v141, v149\n"
"v_mfma_f32_32x32x16_bf16 v[120:135], a[180:183], a[128:131], v[120:135]\n"
"ds_read_b64_tr_b16 a[144:145], v37 offset:1024\n"
"ds_read_b64_tr_b16 a[146:147], v37 offset:1536\n"
"v_exp_f32 v102, v102\n"
"v_mul_f32 v150, v142, v150\n"
"v_exp_f32 v103, v103\n"
"v_mul_f32 v151, v143, v151\n"
"v_fma_f32 v100, v100, v156, v156\n"
"v_fma_f32 v101, v101, v156, v156\n"
"buffer_load_dwordx4 v187, s[44:47], s72 offen offset:3072 lds\n"
"v_mfma_f32_32x32x16_bf16 v[160:175], a[148:151], a[128:131], v[160:175]\n"
"ds_read_b64_tr_b16 a[164:165], v38 offset:1024\n"
"ds_read_b64_tr_b16 a[166:167], v38 offset:1536\n"
"v_fma_f32 v102, v102, v156, v156\n"
"v_fma_f32 v103, v103, v156, v156\n"
"v_rcp_f32 v100, v100\n"
"v_rcp_f32 v101, v101\n"
"v_rcp_f32 v102, v102\n"
"v_mfma_f32_32x32x16_bf16 v[60:75], a[160:163], a[128:131], v[60:75]\n"
"ds_read_b64_tr_b16 a[132:133], v39 offset:1024\n"
"ds_read_b64_tr_b16 a[134:135], v39 offset:1536\n"
"buffer_load_dwordx4 a[160:163], v189, s[48:51], s95 offen offset:1024 sc1\n"
"v_rcp_f32 v103, v103\n"
"v_mul_f32 v148, v148, v100\n"
"v_mul_f32 v149, v149, v101\n"
"v_mul_f32 v150, v150, v102\n"
"v_mul_f32 v151, v151, v103\n"
"s_waitcnt lgkmcnt(8)\n"
"v_mfma_f32_32x32x16_bf16 v[20:35], a[172:175], a[124:127], v[20:35]\n"
"ds_read_b64_tr_b16 a[128:129], v36 offset:2048\n"
"ds_read_b64_tr_b16 a[130:131], v36 offset:2560\n"
"v_mul_f32 v100, v144, v144\n"
"v_mul_f32 v101, v145, v145\n"
"v_mul_f32 v102, v146, v146\n"
"buffer_load_dwordx4 a[172:175], v189, s[48:51], s95 offen offset:2048 sc1\n"
"v_mul_f32 v103, v147, v147\n"
"v_fma_f32 v100, v100, v136, v158\n"
"v_mfma_f32_32x32x16_bf16 v[120:135], a[156:159], a[124:127], v[120:135]\n"
"ds_read_b64_tr_b16 a[148:149], v37 offset:2048\n"
"ds_read_b64_tr_b16 a[150:151], v37 offset:2560\n"
"v_fma_f32 v101, v101, v136, v158\n"
"v_fma_f32 v102, v102, v136, v158\n"
"v_fma_f32 v103, v103, v136, v158\n"
"v_mul_f32 v100, v100, v144\n"
"v_mul_f32 v101, v101, v145\n"
"v_mul_f32 v102, v102, v146\n"
"v_mul_f32 v103, v103, v147\n"
"v_exp_f32 v100, v100\n"
"buffer_load_dwordx4 a[180:183], v189, s[48:51], s95 offen offset:3072 sc1\n"
"v_mfma_f32_32x32x16_bf16 v[160:175], a[136:139], a[124:127], v[160:175]\n"
"ds_read_b64_tr_b16 a[156:157], v38 offset:2048\n"
"ds_read_b64_tr_b16 a[158:159], v38 offset:2560\n"
"v_mul_f32 v152, v144, v152\n"
"v_exp_f32 v101, v101\n"
"v_mul_f32 v153, v145, v153\n"
"v_exp_f32 v102, v102\n"
"v_mul_f32 v154, v146, v154\n"
"v_exp_f32 v103, v103\n"
"v_mfma_f32_32x32x16_bf16 v[60:75], a[168:171], a[124:127], v[60:75]\n"
"ds_read_b64_tr_b16 a[136:137], v39 offset:2048\n"
"ds_read_b64_tr_b16 a[138:139], v39 offset:2560\n"
"s_add_u32 s95, s96, 22528\n"
"buffer_load_dwordx4 a[184:187], v189, s[48:51], s95 offen sc1\n"
"v_mul_f32 v155, v147, v155\n"
"v_fma_f32 v100, v100, v156, v156\n"
"v_fma_f32 v101, v101, v156, v156\n"
"v_fma_f32 v102, v102, v156, v156\n"
"v_fma_f32 v103, v103, v156, v156\n"
"s_waitcnt lgkmcnt(8)\n"
"v_mfma_f32_32x32x16_bf16 v[20:35], a[140:143], a[120:123], v[20:35]\n"
"ds_read_b64_tr_b16 a[168:169], v36 offset:3072\n"
"ds_read_b64_tr_b16 a[170:171], v36 offset:3584\n"
"v_rcp_f32 v100, v100\n"
"buffer_load_dwordx4 a[152:155], v189, s[48:51], s95 offen offset:1024 sc1\n"
"v_rcp_f32 v101, v101\n"
"v_mfma_f32_32x32x16_bf16 v[120:135], a[144:147], a[120:123], v[120:135]\n"
"ds_read_b64_tr_b16 a[140:141], v37 offset:3072\n"
"ds_read_b64_tr_b16 a[142:143], v37 offset:3584\n"
"v_rcp_f32 v102, v102\n"
"v_rcp_f32 v103, v103\n"
"v_mul_f32 v152, v152, v100\n"
"v_mul_f32 v153, v153, v101\n"
"v_mul_f32 v154, v154, v102\n"
"v_mul_f32 v155, v155, v103\n"
"v_cvt_pk_bf16_f32 v100, v148, v149\n"
"buffer_load_dwordx4 a[144:147], v189, s[48:51], s95 offen offset:2048 sc1\n"
"v_mfma_f32_32x32x16_bf16 v[160:175], a[164:167], a[120:123], v[160:175]\n"
"ds_read_b64_tr_b16 a[124:125], v38 offset:3072\n"
"ds_read_b64_tr_b16 a[126:127], v38 offset:3584\n"
"v_cvt_pk_bf16_f32 v101, v150, v151\n"
"v_cvt_pk_bf16_f32 v102, v152, v153\n"
"v_cvt_pk_bf16_f32 v103, v154, v155\n"
"v_mul_f32 v140, v0, v0\n"
"v_mul_f32 v141, v1, v1\n"
"v_mul_f32 v142, v2, v2\n"
"v_mul_f32 v143, v3, v3\n"
"v_fma_f32 v140, v140, v136, v158\n"
"v_mfma_f32_32x32x16_bf16 v[60:75], a[132:135], a[120:123], v[60:75]\n"
"ds_read_b64_tr_b16 a[164:165], v39 offset:3072\n"
"ds_read_b64_tr_b16 a[166:167], v39 offset:3584\n"
"buffer_load_dwordx4 a[192:195], v189, s[48:51], s95 offen offset:3072 sc1\n"
"v_fma_f32 v141, v141, v136, v158\n"
"v_fma_f32 v142, v142, v136, v158\n"
"v_fma_f32 v143, v143, v136, v158\n"
"v_mul_f32 v140, v140, v0\n"
"v_mul_f32 v141, v141, v1\n"
"v_mul_f32 v142, v142, v2\n"
"s_waitcnt lgkmcnt(8)\n"
"v_mfma_f32_32x32x16_bf16 v[20:35], a[128:131], a[116:119], v[20:35]\n"
"s_waitcnt vmcnt(28)\n"
"s_barrier\n"
"s_add_u32 s95, s96, 26624\n"
"buffer_load_dwordx4 a[196:199], v189, s[48:51], s95 offen sc1\n"
"v_mul_f32 v143, v143, v3\n"
"v_exp_f32 v140, v140\n"
"v_mfma_f32_32x32x16_bf16 v[120:135], a[148:151], a[116:119], v[120:135]\n"
"ds_read_b64_tr_b16 a[132:133], v36 offset:4096\n"
"ds_read_b64_tr_b16 a[134:135], v36 offset:4608\n"
"ds_read_b64_tr_b16 a[128:129], v37 offset:4096\n"
"v_mul_f32 v8, v0, v8\n"
"v_exp_f32 v141, v141\n"
"v_mul_f32 v9, v1, v9\n"
"v_exp_f32 v142, v142\n"
"s_add_u32 m0, s79, 20480\n"
"v_mul_f32 v10, v2, v10\n"
"buffer_load_dwordx4 v184, s[44:47], s73 offen lds\n"
"v_mfma_f32_32x32x16_bf16 v[160:175], a[156:159], a[116:119], v[160:175]\n"
"ds_read_b64_tr_b16 a[130:131], v37 offset:4608\n"
"ds_read_b64_tr_b16 a[120:121], v38 offset:4096\n"
"ds_read_b64_tr_b16 a[122:123], v38 offset:4608\n"
"v_exp_f32 v143, v143\n"
"v_mul_f32 v11, v3, v11\n"
"v_fma_f32 v140, v140, v156, v156\n"
"v_fma_f32 v141, v141, v156, v156\n"
"v_fma_f32 v142, v142, v156, v156\n"
"v_fma_f32 v143, v143, v156, v156\n"
"v_mfma_f32_32x32x16_bf16 v[60:75], a[136:139], a[116:119], v[60:75]\n"
"ds_read_b64_tr_b16 a[148:149], v39 offset:4096\n"
"ds_read_b64_tr_b16 a[150:151], v39 offset:4608\n"
"buffer_load_dwordx4 v185, s[44:47], s73 offen offset:1024 lds\n"
"v_rcp_f32 v140, v140\n"
"v_rcp_f32 v141, v141\n"
"v_rcp_f32 v142, v142\n"
"s_waitcnt lgkmcnt(8)\n"
"v_mfma_f32_32x32x16_bf16 v[20:35], a[168:171], a[112:115], v[20:35]\n"
"ds_read_b64_tr_b16 a[116:117], v36 offset:5120\n"
"ds_read_b64_tr_b16 a[118:119], v36 offset:5632\n"
"v_rcp_f32 v143, v143\n"
"buffer_load_dwordx4 v186, s[44:47], s73 offen offset:2048 lds\n"
"v_mul_f32 v8, v8, v140\n"
"v_mul_f32 v9, v9, v141\n"
"v_mul_f32 v10, v10, v142\n"
"v_mfma_f32_32x32x16_bf16 v[120:135], a[140:143], a[112:115], v[120:135]\n"
"ds_read_b64_tr_b16 a[168:169], v37 offset:5120\n"
"ds_read_b64_tr_b16 a[170:171], v37 offset:5632\n"
"v_mul_f32 v11, v11, v143\n"
"v_mul_f32 v140, v4, v4\n"
"v_mul_f32 v141, v5, v5\n"
"v_mul_f32 v142, v6, v6\n"
"v_mul_f32 v143, v7, v7\n"
"v_fma_f32 v140, v140, v136, v158\n"
"v_fma_f32 v141, v141, v136, v158\n"
"buffer_load_dwordx4 v187, s[44:47], s73 offen offset:3072 lds\n"
"v_mfma_f32_32x32x16_bf16 v[160:175], a[124:127], a[112:115], v[160:175]\n"
"ds_read_b64_tr_b16 a[140:141], v38 offset:5120\n"
"ds_read_b64_tr_b16 a[142:143], v38 offset:5632\n"
"v_fma_f32 v142, v142, v136, v158\n"
"v_fma_f32 v143, v143, v136, v158\n"
"v_mul_f32 v140, v140, v4\n"
"v_mul_f32 v141, v141, v5\n"
"v_mul_f32 v142, v142, v6\n"
"v_mul_f32 v143, v143, v7\n"
"v_exp_f32 v140, v140\n"
"v_mul_f32 v12, v4, v12\n"
"v_mfma_f32_32x32x16_bf16 v[60:75], a[164:167], a[112:115], v[60:75]\n"
"ds_read_b64_tr_b16 a[124:125], v39 offset:5120\n"
"ds_read_b64_tr_b16 a[126:127], v39 offset:5632\n"
"buffer_load_dwordx4 a[164:167], v189, s[48:51], s95 offen offset:1024 sc1\n"
"v_exp_f32 v141, v141\n"
"v_mul_f32 v13, v5, v13\n"
"v_exp_f32 v142, v142\n"
"v_mul_f32 v14, v6, v14\n"
"s_waitcnt lgkmcnt(8)\n"
"v_mfma_f32_32x32x16_bf16 v[20:35], a[132:135], a[72:75], v[20:35]\n"
"ds_read_b64_tr_b16 a[112:113], v36 offset:6144\n"
"ds_read_b64_tr_b16 a[114:115], v36 offset:6656\n"
"v_exp_f32 v143, v143\n"
"buffer_load_dwordx2 v[104:105], v42, s[48:51], 0 offen sc1\n"
"v_mul_f32 v15, v7, v15\n"
"v_fma_f32 v140, v140, v156, v156\n"
"v_fma_f32 v141, v141, v156, v156\n"
"v_mfma_f32_32x32x16_bf16 v[120:135], a[128:131], a[72:75], v[120:135]\n"
"ds_read_b64_tr_b16 a[132:133], v37 offset:6144\n"
"ds_read_b64_tr_b16 a[134:135], v37 offset:6656\n"
"v_fma_f32 v142, v142, v156, v156\n"
"v_fma_f32 v143, v143, v156, v156\n"
"v_rcp_f32 v140, v140\n"
"v_rcp_f32 v141, v141\n"
"v_rcp_f32 v142, v142\n"
"buffer_load_dwordx4 a[136:139], v189, s[48:51], s95 offen offset:2048 sc1\n"
"v_mfma_f32_32x32x16_bf16 v[160:175], a[120:123], a[72:75], v[160:175]\n"
"ds_read_b64_tr_b16 a[128:129], v38 offset:6144\n"
"ds_read_b64_tr_b16 a[130:131], v38 offset:6656\n"
"v_rcp_f32 v143, v143\n"
"v_mul_f32 v12, v12, v140\n"
"v_mul_f32 v13, v13, v141\n"
"v_mul_f32 v14, v14, v142\n"
"v_mul_f32 v15, v15, v143\n"
"v_cvt_pk_bf16_f32 v140, v8, v9\n"
"v_cvt_pk_bf16_f32 v141, v10, v11\n"
"v_cvt_pk_bf16_f32 v142, v12, v13\n"
"v_mfma_f32_32x32x16_bf16 v[60:75], a[148:151], a[72:75], v[60:75]\n"
"ds_read_b64_tr_b16 a[120:121], v39 offset:6144\n"
"ds_read_b64_tr_b16 a[122:123], v39 offset:6656\n"
"buffer_load_dwordx4 a[156:159], v189, s[48:51], s95 offen offset:3072 sc1\n"
"v_cvt_pk_bf16_f32 v143, v14, v15\n"
"s_add_u32 s14, s92, 589824\n"
"buffer_store_dwordx4 v[180:183], v138, s[48:51], s14 offen sc1\n"
"buffer_store_dwordx4 v[76:79], v138, s[48:51], s14 offen offset:512 sc1\n"
"s_nop 1\n"
"s_add_u32 s14, s92, 593920\n"
"buffer_store_dwordx4 v[100:103], v138, s[48:51], s14 offen sc1\n"
"buffer_store_dwordx4 v[140:143], v138, s[48:51], s14 offen offset:512 sc1\n"
"s_waitcnt lgkmcnt(8)\n"
"v_mfma_f32_32x32x16_bf16 v[20:35], a[116:119], a[80:83], v[20:35]\n"
"ds_read_b64_tr_b16 a[72:73], v36 offset:7168\n"
"ds_read_b64_tr_b16 a[74:75], v36 offset:7680\n"
"v_mfma_f32_32x32x16_bf16 v[120:135], a[168:171], a[80:83], v[120:135]\n"
"ds_read_b64_tr_b16 a[116:117], v37 offset:7168\n"
"ds_read_b64_tr_b16 a[118:119], v37 offset:7680\n"
"v_mfma_f32_32x32x16_bf16 v[160:175], a[140:143], a[80:83], v[160:175]\n"
"ds_read_b64_tr_b16 a[168:169], v38 offset:7168\n"
"ds_read_b64_tr_b16 a[170:171], v38 offset:7680\n"
"v_mfma_f32_32x32x16_bf16 v[60:75], a[124:127], a[80:83], v[60:75]\n"
"ds_read_b64_tr_b16 a[140:141], v39 offset:7168\n"
"ds_read_b64_tr_b16 a[142:143], v39 offset:7680\n"
"s_waitcnt lgkmcnt(8)\n"
"v_mfma_f32_32x32x16_bf16 v[20:35], a[112:115], a[68:71], v[20:35]\n"
"s_waitcnt vmcnt(37)\n"
"s_barrier\n"
"s_add_u32 s95, s96, 30720\n"
"buffer_load_dwordx4 a[200:203], v189, s[48:51], s95 offen sc1\n"
"v_mfma_f32_32x32x16_bf16 v[120:135], a[132:135], a[68:71], v[120:135]\n"
"ds_read_b64_tr_b16 a[148:149], v36 offset:8192\n"
"ds_read_b64_tr_b16 a[150:151], v36 offset:8704\n"
"ds_read_b64_tr_b16 a[80:81], v37 offset:8192\n"
"v_mfma_f32_32x32x16_bf16 v[160:175], a[128:131], a[68:71], v[160:175]\n"
"ds_read_b64_tr_b16 a[82:83], v37 offset:8704\n"
"ds_read_b64_tr_b16 a[112:113], v38 offset:8192\n"
"s_add_u32 m0, s79, 0\n"
"ds_read_b64_tr_b16 a[114:115], v38 offset:8704\n"
"buffer_load_dwordx4 v184, s[44:47], s74 offen lds\n"
"v_mfma_f32_32x32x16_bf16 v[60:75], a[120:123], a[68:71], v[60:75]\n"
"ds_read_b64_tr_b16 a[124:125], v39 offset:8192\n"
"ds_read_b64_tr_b16 a[126:127], v39 offset:8704\n"
"s_waitcnt lgkmcnt(8)\n"
"v_mfma_f32_32x32x16_bf16 v[20:35], a[72:75], a[76:79], v[20:35]\n"
"ds_read_b64_tr_b16 a[128:129], v36 offset:9216\n"
"ds_read_b64_tr_b16 a[130:131], v36 offset:9728\n"
"buffer_load_dwordx4 v185, s[44:47], s74 offen offset:1024 lds\n"
"v_mfma_f32_32x32x16_bf16 v[120:135], a[116:119], a[76:79], v[120:135]\n"
"ds_read_b64_tr_b16 a[132:133], v37 offset:9216\n"
"ds_read_b64_tr_b16 a[134:135], v37 offset:9728\n"
"v_mfma_f32_32x32x16_bf16 v[160:175], a[168:171], a[76:79], v[160:175]\n"
"ds_read_b64_tr_b16 a[68:69], v38 offset:9216\n"
"ds_read_b64_tr_b16 a[70:71], v38 offset:9728\n"
"buffer_load_dwordx4 v186, s[44:47], s74 offen offset:2048 lds\n"
"v_mfma_f32_32x32x16_bf16 v[60:75], a[140:143], a[76:79], v[60:75]\n"
"ds_read_b64_tr_b16 a[116:117], v39 offset:9216\n"
"ds_read_b64_tr_b16 a[118:119], v39 offset:9728\n"
"s_waitcnt lgkmcnt(8)\n"
"v_mfma_f32_32x32x16_bf16 v[20:35], a[148:151], a[104:107], v[20:35]\n"
"ds_read_b64_tr_b16 a[76:77], v36 offset:10240\n"
"ds_read_b64_tr_b16 a[78:79], v36 offset:10752\n"
"buffer_load_dwordx4 v187, s[44:47], s74 offen offset:3072 lds\n"
"v_mfma_f32_32x32x16_bf16 v[120:135], a[80:83], a[104:107], v[120:135]\n"
"ds_read_b64_tr_b16 a[72:73], v37 offset:10240\n"
"ds_read_b64_tr_b16 a[74:75], v37 offset:10752\n"
"s_mov_b32 s90, 4096\n"
"v_mfma_f32_32x32x16_bf16 v[160:175], a[112:115], a[104:107], v[160:175]\n"
"ds_read_b64_tr_b16 a[140:141], v38 offset:10240\n"
"ds_read_b64_tr_b16 a[142:143], v38 offset:10752\n"
"v_mfma_f32_32x32x16_bf16 v[60:75], a[124:127], a[104:107], v[60:75]\n"
"ds_read_b64_tr_b16 a[120:121], v39 offset:10240\n"
"ds_read_b64_tr_b16 a[122:123], v39 offset:10752\n"
"buffer_load_dwordx4 a[124:127], v189, s[48:51], s95 offen offset:1024 sc1\n"
"s_waitcnt lgkmcnt(8)\n"
"v_mfma_f32_32x32x16_bf16 v[20:35], a[128:131], a[92:95], v[20:35]\n"
"ds_read_b64_tr_b16 a[104:105], v36 offset:11264\n"
"ds_read_b64_tr_b16 a[106:107], v36 offset:11776\n"
"buffer_load_dwordx4 v[140:143], v16, s[48:51], s90 offen sc1\n"
"v_mfma_f32_32x32x16_bf16 v[120:135], a[132:135], a[92:95], v[120:135]\n"
"ds_read_b64_tr_b16 a[168:169], v37 offset:11264\n"
"ds_read_b64_tr_b16 a[170:171], v37 offset:11776\n"
"v_mfma_f32_32x32x16_bf16 v[160:175], a[68:71], a[92:95], v[160:175]\n"
"ds_read_b64_tr_b16 a[112:113], v38 offset:11264\n"
"ds_read_b64_tr_b16 a[114:115], v38 offset:11776\n"
"v_mfma_f32_32x32x16_bf16 v[60:75], a[116:119], a[92:95], v[60:75]\n"
"ds_read_b64_tr_b16 a[128:129], v39 offset:11264\n"
"ds_read_b64_tr_b16 a[130:131], v39 offset:11776\n"
"buffer_load_dwordx4 a[80:83], v189, s[48:51], s95 offen offset:2048 sc1\n"
"s_waitcnt lgkmcnt(8)\n"
"v_mfma_f32_32x32x16_bf16 v[20:35], a[76:79], a[88:91], v[20:35]\n"
"s_waitcnt vmcnt(40)\n"
"s_barrier\n"
"v_mfma_f32_32x32x16_bf16 v[120:135], a[72:75], a[88:91], v[120:135]\n"
"ds_read_b64_tr_b16 a[116:117], v36 offset:12288\n"
"ds_read_b64_tr_b16 a[118:119], v36 offset:12800\n"
"s_add_u32 m0, s79, 4096\n"
"ds_read_b64_tr_b16 a[132:133], v37 offset:12288\n"
"buffer_load_dwordx4 v184, s[44:47], s75 offen lds\n"
"v_mfma_f32_32x32x16_bf16 v[160:175], a[140:143], a[88:91], v[160:175]\n"
"ds_read_b64_tr_b16 a[134:135], v37 offset:12800\n"
"ds_read_b64_tr_b16 a[76:77], v38 offset:12288\n"
"ds_read_b64_tr_b16 a[78:79], v38 offset:12800\n"
"buffer_load_dwordx4 v[144:147], v16, s[48:51], s90 offen offset:1024 sc1\n"
"v_mfma_f32_32x32x16_bf16 v[60:75], a[120:123], a[88:91], v[60:75]\n"
"ds_read_b64_tr_b16 a[68:69], v39 offset:12288\n"
"ds_read_b64_tr_b16 a[70:71], v39 offset:12800\n"
"s_waitcnt lgkmcnt(8)\n"
"v_mfma_f32_32x32x16_bf16 v[20:35], a[104:107], a[96:99], v[20:35]\n"
"ds_read_b64_tr_b16 a[88:89], v36 offset:13312\n"
"ds_read_b64_tr_b16 a[90:91], v36 offset:13824\n"
"v_mfma_f32_32x32x16_bf16 v[120:135], a[168:171], a[96:99], v[120:135]\n"
"ds_read_b64_tr_b16 a[104:105], v37 offset:13312\n"
"ds_read_b64_tr_b16 a[106:107], v37 offset:13824\n"
"buffer_load_dwordx4 v[148:151], v16, s[48:51], s90 offen offset:2048 sc1\n"
"v_mfma_f32_32x32x16_bf16 v[160:175], a[112:115], a[96:99], v[160:175]\n"
"ds_read_b64_tr_b16 a[168:169], v38 offset:13312\n"
"ds_read_b64_tr_b16 a[170:171], v38 offset:13824\n"
"v_mfma_f32_32x32x16_bf16 v[60:75], a[128:131], a[96:99], v[60:75]\n"
"ds_read_b64_tr_b16 a[92:93], v39 offset:13312\n"
"ds_read_b64_tr_b16 a[94:95], v39 offset:13824\n"
"s_waitcnt lgkmcnt(8)\n"
"v_mfma_f32_32x32x16_bf16 v[20:35], a[116:119], a[108:111], v[20:35]\n"
"ds_read_b64_tr_b16 a[96:97], v36 offset:14336\n"
"ds_read_b64_tr_b16 a[98:99], v36 offset:14848\n"
"buffer_load_dwordx4 v185, s[44:47], s75 offen offset:1024 lds\n"
"v_mfma_f32_32x32x16_bf16 v[120:135], a[132:135], a[108:111], v[120:135]\n"
"ds_read_b64_tr_b16 a[112:113], v37 offset:14336\n"
"ds_read_b64_tr_b16 a[114:115], v37 offset:14848\n"
"buffer_load_dwordx4 v[152:155], v16, s[48:51], s90 offen offset:3072 sc1\n"
"v_mfma_f32_32x32x16_bf16 v[160:175], a[76:79], a[108:111], v[160:175]\n"
"ds_read_b64_tr_b16 a[120:121], v38 offset:14336\n"
"ds_read_b64_tr_b16 a[122:123], v38 offset:14848\n"
"v_mfma_f32_32x32x16_bf16 v[60:75], a[68:71], a[108:111], v[60:75]\n"
"ds_read_b64_tr_b16 a[132:133], v39 offset:14336\n"
"ds_read_b64_tr_b16 a[134:135], v39 offset:14848\n"
"s_waitcnt lgkmcnt(8)\n"
"v_mfma_f32_32x32x16_bf16 v[20:35], a[88:91], a[100:103], v[20:35]\n"
"ds_read_b64_tr_b16 a[128:129], v36 offset:15360\n"
"ds_read_b64_tr_b16 a[130:131], v36 offset:15872\n"
"v_mfma_f32_32x32x16_bf16 v[120:135], a[104:107], a[100:103], v[120:135]\n"
"ds_read_b64_tr_b16 a[88:89], v37 offset:15360\n"
"ds_read_b64_tr_b16 a[90:91], v37 offset:15872\n"
"buffer_load_dwordx4 v186, s[44:47], s75 offen offset:2048 lds\n"
"v_mfma_f32_32x32x16_bf16 v[160:175], a[168:171], a[100:103], v[160:175]\n"
"ds_read_b64_tr_b16 a[104:105], v38 offset:15360\n"
"ds_read_b64_tr_b16 a[106:107], v38 offset:15872\n"
"s_waitcnt vmcnt(14)\n"
"v_mov_b32 v180, s16\n"
"v_mov_b32 v181, s81\n"
"s_mov_b64 exec, 1\n"
"buffer_store_dwordx2 v[180:181], off, s[48:51], s28 sc1\n"
"s_mov_b64 exec, -1\n"
"v_mfma_f32_32x32x16_bf16 v[60:75], a[92:95], a[100:103], v[60:75]\n"
"ds_read_b64_tr_b16 a[68:69], v39 offset:15360\n"
"ds_read_b64_tr_b16 a[70:71], v39 offset:15872\n"
"s_waitcnt lgkmcnt(8)\n"
"v_mfma_f32_32x32x16_bf16 v[20:35], a[96:99], a[84:87], v[20:35]\n"
"s_barrier\n"
"v_mfma_f32_32x32x16_bf16 v[120:135], a[112:115], a[84:87], v[120:135]\n"
"ds_read_b64_tr_b16 a[76:77], v36 offset:16384\n"
"ds_read_b64_tr_b16 a[78:79], v36 offset:16896\n"
"ds_read_b64_tr_b16 a[92:93], v37 offset:16384\n"
"buffer_load_dwordx4 v187, s[44:47], s75 offen offset:3072 lds\n"
"v_mfma_f32_32x32x16_bf16 v[160:175], a[120:123], a[84:87], v[160:175]\n"
"ds_read_b64_tr_b16 a[94:95], v37 offset:16896\n"
"ds_read_b64_tr_b16 a[108:109], v38 offset:16384\n"
"ds_read_b64_tr_b16 a[110:111], v38 offset:16896\n"
"s_add_u32 s86, s76, 0\n"
"s_and_b32 s86, s86, 3\n"
"s_add_u32 s86, s86, 3\n"
"v_mfma_f32_32x32x16_bf16 v[60:75], a[132:135], a[84:87], v[60:75]\n"
"ds_read_b64_tr_b16 a[96:97], v39 offset:16384\n"
"ds_read_b64_tr_b16 a[98:99], v39 offset:16896\n"
"buffer_load_dwordx4 a[132:135], v189, s[48:51], s95 offen offset:3072 sc1\n"
"s_lshl_b32 s86, s86, 10\n"
"s_waitcnt lgkmcnt(8)\n"
"v_mfma_f32_32x32x16_bf16 v[20:35], a[128:131], a[64:67], v[20:35]\n"
"ds_read_b64_tr_b16 a[84:85], v36 offset:17408\n"
"ds_read_b64_tr_b16 a[86:87], v36 offset:17920\n"
"v_add_u32 v193, s86, v189\n"
"s_add_u32 s86, s76, 1\n"
"s_and_b32 s86, s86, 3\n"
"v_mfma_f32_32x32x16_bf16 v[120:135], a[88:91], a[64:67], v[120:135]\n"
"ds_read_b64_tr_b16 a[100:101], v37 offset:17408\n"
"ds_read_b64_tr_b16 a[102:103], v37 offset:17920\n"
"s_add_u32 s95, s96, 34816\n"
"buffer_load_dwordx4 a[128:131], v189, s[48:51], s95 offen sc1\n"
"v_xor_b32 v180, s80, v104\n"
"v_bitop3_b32 v180, v105, s81, v180 bitop3:0xbe\n"
"v_cmp_ne_u32 vcc, 0, v180\n"
"s_nop 1\n"
"s_cmp_lg_u64 vcc, 0\n"
"s_cbranch_scc1 SLOWAJW_%=\n"
"BACKAJW_%=:\n"
"v_mfma_f32_32x32x16_bf16 v[160:175], a[104:107], a[64:67], v[160:175]\n"
"ds_read_b64_tr_b16 a[88:89], v38 offset:17408\n"
"ds_read_b64_tr_b16 a[90:91], v38 offset:17920\n"
"s_add_u32 s86, s86, 2\n"
"s_lshl_b32 s86, s86, 10\n"
"v_add_u32 v196, s86, v189\n"
"s_add_u32 s86, s76, 2\n"
"s_add_u32 s95, s96, 0\n"
"buffer_load_dwordx4 a[120:123], v189, s[48:51], s95 offen sc1\n"
"v_mfma_f32_32x32x16_bf16 v[60:75], a[68:71], a[64:67], v[60:75]\n"
"ds_read_b64_tr_b16 a[104:105], v39 offset:17408\n"
"ds_read_b64_tr_b16 a[106:107], v39 offset:17920\n"
"s_and_b32 s86, s86, 3\n"
"s_add_u32 s86, s86, 1\n"
"s_lshl_b32 s86, s86, 10\n"
"v_add_u32 v195, s86, v189\n"
"s_waitcnt lgkmcnt(8)\n"
"v_mfma_f32_32x32x16_bf16 v[20:35], a[76:79], a[60:63], v[20:35]\n"
"ds_read_b64_tr_b16 a[64:65], v36 offset:18432\n"
"ds_read_b64_tr_b16 a[66:67], v36 offset:18944\n"
"s_add_u32 s86, s76, 3\n"
"s_and_b32 s86, s86, 3\n"
"buffer_load_dwordx4 a[68:71], v189, s[48:51], s95 offen offset:1024 sc1\n"
"v_mfma_f32_32x32x16_bf16 v[120:135], a[92:95], a[60:63], v[120:135]\n"
"ds_read_b64_tr_b16 a[76:77], v37 offset:18432\n"
"ds_read_b64_tr_b16 a[78:79], v37 offset:18944\n"
"s_add_u32 s86, s86, 0\n"
"s_lshl_b32 s86, s86, 10\n"
"v_add_u32 v194, s86, v189\n"
"s_lshl_b32 s86, s77, 1\n"
"v_mfma_f32_32x32x16_bf16 v[160:175], a[108:111], a[60:63], v[160:175]\n"
"ds_read_b64_tr_b16 a[92:93], v38 offset:18432\n"
"ds_read_b64_tr_b16 a[94:95], v38 offset:18944\n"
"s_add_u32 s86, s86, s27\n"
"s_lshl_b32 s86, s86, 2\n"
"buffer_load_dwordx4 a[112:115], v189, s[48:51], s95 offen offset:2048 sc1\n"
"v_mfma_f32_32x32x16_bf16 v[60:75], a[96:99], a[60:63], v[60:75]\n"
"ds_read_b64_tr_b16 a[108:109], v39 offset:18432\n"
"ds_read_b64_tr_b16 a[110:111], v39 offset:18944\n"
"s_add_u32 s86, s86, s76\n"
"s_mul_i32 s56, s86, 20480\n"
"s_mul_i32 s57, s26, 160\n"
"s_lshl_b32 s86, s27, 1\n"
"s_waitcnt lgkmcnt(8)\n"
"v_mfma_f32_32x32x16_bf16 v[20:35], a[84:87], a[52:55], v[20:35]\n"
"ds_read_b64_tr_b16 a[60:61], v36 offset:19456\n"
"ds_read_b64_tr_b16 a[62:63], v36 offset:19968\n"
"s_lshr_b32 s87, s76, 1\n"
"s_add_u32 s86, s86, s87\n"
"buffer_load_dwordx4 a[96:99], v189, s[48:51], s95 offen offset:3072 sc1\n"
"v_mfma_f32_32x32x16_bf16 v[120:135], a[100:103], a[52:55], v[120:135]\n"
"ds_read_b64_tr_b16 a[84:85], v37 offset:19456\n"
"ds_read_b64_tr_b16 a[86:87], v37 offset:19968\n"
"s_mul_i32 s86, s86, 40960\n"
"s_and_b32 s87, s76, 1\n"
"s_lshl_b32 s87, s87, 6\n"
"s_add_u32 s59, s86, s87\n"
"v_mfma_f32_32x32x16_bf16 v[160:175], a[88:91], a[52:55], v[160:175]\n"
"ds_read_b64_tr_b16 a[100:101], v38 offset:19456\n"
"ds_read_b64_tr_b16 a[102:103], v38 offset:19968\n"
"v_lshrrev_b32 v180, 4, v255\n"
"v_and_b32 v182, 15, v255\n"
"s_add_u32 s95, s96, 4096\n"
"buffer_load_dwordx4 a[168:171], v189, s[48:51], s95 offen sc1\n"
"v_mfma_f32_32x32x16_bf16 v[60:75], a[104:107], a[52:55], v[60:75]\n"
"ds_read_b64_tr_b16 a[88:89], v39 offset:19456\n"
"ds_read_b64_tr_b16 a[90:91], v39 offset:19968\n"
"v_mul_u32_u24 v181, 5120, v180\n"
"v_add_u32 v181, s56, v181\n"
"v_lshlrev_b32 v104, 1, v182\n"
"v_add_u32 v104, s57, v104\n"
"s_waitcnt lgkmcnt(8)\n"
"v_mfma_f32_32x32x16_bf16 v[20:35], a[64:67], a[40:43], v[20:35]\n"
"s_barrier\n"
"buffer_load_dwordx4 a[116:119], v189, s[48:51], s95 offen offset:1024 sc1\n"
"v_mfma_f32_32x32x16_bf16 v[120:135], a[76:79], a[40:43], v[120:135]\n"
"ds_read_b64_tr_b16 a[104:105], v36 offset:20480\n"
"ds_read_b64_tr_b16 a[106:107], v36 offset:20992\n"
"ds_read_b64_tr_b16 a[76:77], v37 offset:20480\n"
"v_add_u32 v76, v181, v104\n"
"v_lshlrev_b32 v252, 1, v76\n"
"v_lshlrev_b32 v105, 4, v180\n"
"v_mfma_f32_32x32x16_bf16 v[160:175], a[92:95], a[40:43], v[160:175]\n"
"ds_read_b64_tr_b16 a[78:79], v37 offset:20992\n"
"ds_read_b64_tr_b16 a[64:65], v38 offset:20480\n"
"ds_read_b64_tr_b16 a[66:67], v38 offset:20992\n"
"v_add_u32 v105, s59, v105\n"
"buffer_load_dwordx4 a[140:143], v189, s[48:51], s95 offen offset:2048 sc1\n"
"v_mfma_f32_32x32x16_bf16 v[60:75], a[108:111], a[40:43], v[60:75]\n"
"ds_read_b64_tr_b16 a[52:53], v39 offset:20480\n"
"ds_read_b64_tr_b16 a[54:55], v39 offset:20992\n"
"ds_read_b128 v[40:43], v105 offset:40704\n"
"global_load_ushort v184, v104, s[24:25]\n"
"s_waitcnt lgkmcnt(9)\n"
"v_mfma_f32_32x32x16_bf16 v[20:35], a[60:63], a[28:31], v[20:35]\n"
"ds_read_b64_tr_b16 a[108:109], v36 offset:21504\n"
"ds_read_b64_tr_b16 a[110:111], v36 offset:22016\n"
"global_load_ushort v185, v104, s[24:25] offset:32\n"
"v_mfma_f32_32x32x16_bf16 v[120:135], a[84:87], a[28:31], v[120:135]\n"
"ds_read_b64_tr_b16 a[92:93], v37 offset:21504\n"
"ds_read_b64_tr_b16 a[94:95], v37 offset:22016\n"
"global_load_ushort v186, v104, s[24:25] offset:64\n"
"v_mfma_f32_32x32x16_bf16 v[160:175], a[100:103], a[28:31], v[160:175]\n"
"ds_read_b64_tr_b16 a[84:85], v38 offset:21504\n"
"ds_read_b64_tr_b16 a[86:87], v38 offset:22016\n"
"global_load_ushort v187, v104, s[24:25] offset:96\n"
"v_mfma_f32_32x32x16_bf16 v[60:75], a[88:91], a[28:31], v[60:75]\n"
"ds_read_b64_tr_b16 a[100:101], v39 offset:21504\n"
"ds_read_b64_tr_b16 a[102:103], v39 offset:22016\n"
"buffer_load_dwordx4 a[72:75], v189, s[48:51], s95 offen offset:3072 sc1\n"
"s_waitcnt lgkmcnt(9)\n"
"v_mfma_f32_32x32x16_bf16 v[20:35], a[104:107], a[32:35], v[20:35]\n"
"ds_read_b64_tr_b16 a[28:29], v36 offset:22528\n"
"ds_read_b64_tr_b16 a[30:31], v36 offset:23040\n"
"global_load_ushort v188, v104, s[24:25] offset:128\n"
"s_waitcnt vmcnt(24)\n"
"v_xor_b32 v104, s8, v141\n"
"v_mfma_f32_32x32x16_bf16 v[120:135], a[76:79], a[32:35], v[120:135]\n"
"ds_read_b64_tr_b16 a[88:89], v37 offset:22528\n"
"ds_read_b64_tr_b16 a[90:91], v37 offset:23040\n"
"v_bitop3_b32 v104, v143, s9, v104 bitop3:0xbe\n"
"s_waitcnt vmcnt(21)\n"
"v_bitop3_b32 v104, v145, s8, v104 bitop3:0xbe\n"
"v_bitop3_b32 v104, v147, s9, v104 bitop3:0xbe\n"
"v_mfma_f32_32x32x16_bf16 v[160:175], a[64:67], a[32:35], v[160:175]\n"
"ds_read_b64_tr_b16 a[60:61], v38 offset:22528\n"
"ds_read_b64_tr_b16 a[62:63], v38 offset:23040\n"
"s_add_u32 s95, s96, 8192\n"
"buffer_load_dwordx4 a[64:67], v189, s[48:51], s95 offen sc1\n"
"s_waitcnt vmcnt(21)\n"
"v_bitop3_b32 v104, v149, s8, v104 bitop3:0xbe\n"
"v_mfma_f32_32x32x16_bf16 v[60:75], a[52:55], a[32:35], v[60:75]\n"
"ds_read_b64_tr_b16 a[40:41], v39 offset:22528\n"
"ds_read_b64_tr_b16 a[42:43], v39 offset:23040\n"
"v_bitop3_b32 v104, v151, s9, v104 bitop3:0xbe\n"
"s_waitcnt vmcnt(19)\n"
"v_bitop3_b32 v104, v153, s8, v104 bitop3:0xbe\n"
"v_bitop3_b32 v104, v155, s9, v104 bitop3:0xbe\n"
"s_waitcnt lgkmcnt(8)\n"
"v_mfma_f32_32x32x16_bf16 v[20:35], a[108:111], a[16:19], v[20:35]\n"
"ds_read_b64_tr_b16 a[32:33], v36 offset:23552\n"
"ds_read_b64_tr_b16 a[34:35], v36 offset:24064\n"
"buffer_load_dwordx4 a[52:55], v189, s[48:51], s95 offen offset:1024 sc1\n"
"v_mfma_f32_32x32x16_bf16 v[120:135], a[92:95], a[16:19], v[120:135]\n"
"ds_read_b64_tr_b16 a[108:109], v37 offset:23552\n"
"ds_read_b64_tr_b16 a[110:111], v37 offset:24064\n"
"v_cmp_ne_u32 vcc, 0, v104\n"
"s_nop 1\n"
"s_cmp_lg_u64 vcc, 0\n"
"s_cbranch_scc1 SLOWAX1_%=\n"
"BACKAX1_%=:\n"
"v_mfma_f32_32x32x16_bf16 v[160:175], a[84:87], a[16:19], v[160:175]\n"
"ds_read_b64_tr_b16 a[92:93], v38 offset:23552\n"
"ds_read_b64_tr_b16 a[94:95], v38 offset:24064\n"
"s_add_u32 s95, s96, 35840\n"
"buffer_load_dwordx4 a[76:79], v189, s[48:51], s95 offen sc1\n"
"v_mfma_f32_32x32x16_bf16 v[60:75], a[100:103], a[16:19], v[60:75]\n"
"ds_read_b64_tr_b16 a[84:85], v39 offset:23552\n"
"ds_read_b64_tr_b16 a[86:87], v39 offset:24064\n"
"buffer_load_ushort v0, v76, s[36:39], 0 offen\n"
"s_waitcnt lgkmcnt(8)\n"
"v_mfma_f32_32x32x16_bf16 v[20:35], a[28:31], a[4:7], v[20:35]\n"
"s_barrier\n"
"v_mfma_f32_32x32x16_bf16 v[120:135], a[88:91], a[4:7], v[120:135]\n"
"ds_read_b64_tr_b16 a[100:101], v36\n"
"ds_read_b64_tr_b16 a[102:103], v36 offset:512\n"
"ds_read_b64_tr_b16 a[16:17], v37\n"
"buffer_load_dwordx4 a[104:107], v189, s[48:51], s95 offen offset:1024 sc1\n"
"v_mfma_f32_32x32x16_bf16 v[160:175], a[60:63], a[4:7], v[160:175]\n"
"ds_read_b64_tr_b16 a[18:19], v37 offset:512\n"
"ds_read_b64_tr_b16 a[28:29], v38\n"
"ds_read_b64_tr_b16 a[30:31], v38 offset:512\n"
"buffer_load_ushort v4, v76, s[36:39], 0 offen offset:32\n"
"v_mfma_f32_32x32x16_bf16 v[60:75], a[40:43], a[4:7], v[60:75]\n"
"ds_read_b64_tr_b16 a[60:61], v39\n"
"ds_read_b64_tr_b16 a[62:63], v39 offset:512\n"
"buffer_load_ushort v8, v76, s[36:39], 0 offen offset:64\n"
"s_waitcnt lgkmcnt(8)\n"
"v_mfma_f32_32x32x16_bf16 v[20:35], a[32:35], a[8:11], v[20:35]\n"
"ds_read_b64_tr_b16 a[4:5], v36 offset:1024\n"
"ds_read_b64_tr_b16 a[6:7], v36 offset:1536\n"
"buffer_load_ushort v12, v76, s[36:39], 0 offen offset:96\n"
"v_mfma_f32_32x32x16_bf16 v[120:135], a[108:111], a[8:11], v[120:135]\n"
"ds_read_b64_tr_b16 a[40:41], v37 offset:1024\n"
"ds_read_b64_tr_b16 a[42:43], v37 offset:1536\n"
"buffer_load_dwordx4 a[148:151], v189, s[48:51], s95 offen offset:2048 sc1\n"
"v_mfma_f32_32x32x16_bf16 v[160:175], a[92:95], a[8:11], v[160:175]\n"
"ds_read_b64_tr_b16 a[88:89], v38 offset:1024\n"
"ds_read_b64_tr_b16 a[90:91], v38 offset:1536\n"
"buffer_load_ushort v16, v76, s[36:39], 0 offen offset:128\n"
"v_mfma_f32_32x32x16_bf16 v[60:75], a[84:87], a[8:11], v[60:75]\n"
"ds_read_b64_tr_b16 a[92:93], v39 offset:1024\n"
"ds_read_b64_tr_b16 a[94:95], v39 offset:1536\n"
"buffer_load_ushort v1, v76, s[36:39], 0 offen offset:1280\n"
"s_waitcnt lgkmcnt(8)\n"
"v_mfma_f32_32x32x16_bf16 v[20:35], a[100:103], a[20:23], v[20:35]\n"
"ds_read_b64_tr_b16 a[84:85], v36 offset:2048\n"
"ds_read_b64_tr_b16 a[86:87], v36 offset:2560\n"
"buffer_load_ushort v5, v76, s[36:39], 0 offen offset:1312\n"
"v_mfma_f32_32x32x16_bf16 v[120:135], a[16:19], a[20:23], v[120:135]\n"
"ds_read_b64_tr_b16 a[100:101], v37 offset:2048\n"
"ds_read_b64_tr_b16 a[102:103], v37 offset:2560\n"
"buffer_load_dwordx2 v[104:105], v190, s[48:51], 0 offen sc1\n"
"v_mfma_f32_32x32x16_bf16 v[160:175], a[28:31], a[20:23], v[160:175]\n"
"ds_read_b64_tr_b16 a[108:109], v38 offset:2048\n"
"ds_read_b64_tr_b16 a[110:111], v38 offset:2560\n"
"buffer_load_ushort v9, v76, s[36:39], 0 offen offset:1344\n"
"v_mfma_f32_32x32x16_bf16 v[60:75], a[60:63], a[20:23], v[60:75]\n"
"ds_read_b64_tr_b16 a[32:33], v39 offset:2048\n"
"ds_read_b64_tr_b16 a[34:35], v39 offset:2560\n"
"buffer_load_ushort v13, v76, s[36:39], 0 offen offset:1376\n"
"s_waitcnt lgkmcnt(8)\n"
"v_mfma_f32_32x32x16_bf16 v[20:35], a[4:7], a[12:15], v[20:35]\n"
"ds_read_b64_tr_b16 a[20:21], v36 offset:3072\n"
"ds_read_b64_tr_b16 a[22:23], v36 offset:3584\n"
"buffer_load_ushort v17, v76, s[36:39], 0 offen offset:1408\n"
"v_mfma_f32_32x32x16_bf16 v[120:135], a[40:43], a[12:15], v[120:135]\n"
"ds_read_b64_tr_b16 a[8:9], v37 offset:3072\n"
"ds_read_b64_tr_b16 a[10:11], v37 offset:3584\n"
"buffer_load_dwordx4 a[40:43], v189, s[48:51], s95 offen offset:3072 sc1\n"
"v_mfma_f32_32x32x16_bf16 v[160:175], a[88:91], a[12:15], v[160:175]\n"
"ds_read_b64_tr_b16 a[4:5], v38 offset:3072\n"
"ds_read_b64_tr_b16 a[6:7], v38 offset:3584\n"
"buffer_load_ushort v2, v76, s[36:39], 0 offen offset:2560\n"
"v_mfma_f32_32x32x16_bf16 v[60:75], a[92:95], a[12:15], v[60:75]\n"
"ds_read_b64_tr_b16 a[16:17], v39 offset:3072\n"
"ds_read_b64_tr_b16 a[18:19], v39 offset:3584\n"
"buffer_load_ushort v6, v76, s[36:39], 0 offen offset:2592\n"
"s_waitcnt lgkmcnt(8)\n"
"v_mfma_f32_32x32x16_bf16 v[20:35], a[84:87], a[0:3], v[20:35]\n"
"s_waitcnt vmcnt(34)\n"
"s_barrier\n"
"s_add_u32 s95, s96, 39936\n"
"buffer_load_dwordx4 a[88:91], v189, s[48:51], s95 offen sc1\n"
"v_mfma_f32_32x32x16_bf16 v[120:135], a[100:103], a[0:3], v[120:135]\n"
"ds_read_b64_tr_b16 a[92:93], v36 offset:4096\n"
"ds_read_b64_tr_b16 a[94:95], v36 offset:4608\n"
"ds_read_b64_tr_b16 a[12:13], v37 offset:4096\n"
"buffer_load_ushort v10, v76, s[36:39], 0 offen offset:2624\n"
"v_mfma_f32_32x32x16_bf16 v[160:175], a[108:111], a[0:3], v[160:175]\n"
"ds_read_b64_tr_b16 a[14:15], v37 offset:4608\n"
"ds_read_b64_tr_b16 a[100:101], v38 offset:4096\n"
"ds_read_b64_tr_b16 a[102:103], v38 offset:4608\n"
"buffer_load_ushort v14, v76, s[36:39], 0 offen offset:2656\n"
"v_mfma_f32_32x32x16_bf16 v[60:75], a[32:35], a[0:3], v[60:75]\n"
"ds_read_b64_tr_b16 a[84:85], v39 offset:4096\n"
"ds_read_b64_tr_b16 a[86:87], v39 offset:4608\n"
"buffer_load_ushort v18, v76, s[36:39], 0 offen offset:2688\n"
"s_waitcnt lgkmcnt(8)\n"
"v_mfma_f32_32x32x16_bf16 v[20:35], a[20:23], a[24:27], v[20:35]\n"
"ds_read_b64_tr_b16 a[28:29], v36 offset:5120\n"
"ds_read_b64_tr_b16 a[30:31], v36 offset:5632\n"
"buffer_load_dwordx4 a[60:63], v189, s[48:51], s95 offen offset:1024 sc1\n"
"v_mfma_f32_32x32x16_bf16 v[120:135], a[8:11], a[24:27], v[120:135]\n"
"ds_read_b64_tr_b16 a[108:109], v37 offset:5120\n"
"ds_read_b64_tr_b16 a[110:111], v37 offset:5632\n"
"buffer_load_ushort v3, v76, s[36:39], 0 offen offset:3840\n"
"v_mfma_f32_32x32x16_bf16 v[160:175], a[4:7], a[24:27], v[160:175]\n"
"ds_read_b64_tr_b16 a[20:21], v38 offset:5120\n"
"ds_read_b64_tr_b16 a[22:23], v38 offset:5632\n"
"buffer_load_ushort v7, v76, s[36:39], 0 offen offset:3872\n"
"v_mfma_f32_32x32x16_bf16 v[60:75], a[16:19], a[24:27], v[60:75]\n"
"ds_read_b64_tr_b16 a[32:33], v39 offset:5120\n"
"ds_read_b64_tr_b16 a[34:35], v39 offset:5632\n"
"buffer_load_ushort v11, v76, s[36:39], 0 offen offset:3904\n"
"s_waitcnt lgkmcnt(8)\n"
"v_mfma_f32_32x32x16_bf16 v[20:35], a[92:95], a[56:59], v[20:35]\n"
"ds_read_b64_tr_b16 a[4:5], v36 offset:6144\n"
"ds_read_b64_tr_b16 a[6:7], v36 offset:6656\n"
"buffer_load_dwordx4 a[92:95], v189, s[48:51], s95 offen offset:2048 sc1\n"
"v_mfma_f32_32x32x16_bf16 v[120:135], a[12:15], a[56:59], v[120:135]\n"
"ds_read_b64_tr_b16 a[16:17], v37 offset:6144\n"
"ds_read_b64_tr_b16 a[18:19], v37 offset:6656\n"
"buffer_load_ushort v15, v76, s[36:39], 0 offen offset:3936\n"
"v_mfma_f32_32x32x16_bf16 v[160:175], a[100:103], a[56:59], v[160:175]\n"
"ds_read_b64_tr_b16 a[12:13], v38 offset:6144\n"
"ds_read_b64_tr_b16 a[14:15], v38 offset:6656\n"
"buffer_load_ushort v19, v76, s[36:39], 0 offen offset:3968\n"
"v_mfma_f32_32x32x16_bf16 v[60:75], a[84:87], a[56:59], v[60:75]\n"
"ds_read_b64_tr_b16 a[8:9], v39 offset:6144\n"
"ds_read_b64_tr_b16 a[10:11], v39 offset:6656\n"
"v_add_u32 v253, s79, v189\n"
"v_lshrrev_b32 v76, 5, v255\n"
"v_bfe_u32 v77, v255, 1, 1\n"
"s_waitcnt lgkmcnt(8)\n"
"v_mfma_f32_32x32x16_bf16 v[20:35], a[28:31], a[48:51], v[20:35]\n"
"ds_read_b64_tr_b16 a[0:1], v36 offset:7168\n"
"ds_read_b64_tr_b16 a[2:3], v36 offset:7680\n"
"buffer_load_dwordx4 a[84:87], v189, s[48:51], s95 offen offset:3072 sc1\n"
"v_mfma_f32_32x32x16_bf16 v[120:135], a[108:111], a[48:51], v[120:135]\n"
"ds_read_b64_tr_b16 a[56:57], v37 offset:7168\n"
"ds_read_b64_tr_b16 a[58:59], v37 offset:7680\n"
"v_lshl_add_u32 v76, v76, 1, v77\n"
"v_bfe_u32 v77, v255, 4, 1\n"
"v_lshl_add_u32 v76, v76, 1, v77\n"
"v_mfma_f32_32x32x16_bf16 v[160:175], a[20:23], a[48:51], v[160:175]\n"
"ds_read_b64_tr_b16 a[24:25], v38 offset:7168\n"
"ds_read_b64_tr_b16 a[26:27], v38 offset:7680\n"
"s_add_u32 s95, s96, 44032\n"
"buffer_load_dwordx4 a[108:111], v189, s[48:51], s95 offen sc1\n"
"v_bfe_u32 v77, v255, 2, 2\n"
"v_mfma_f32_32x32x16_bf16 v[60:75], a[32:35], a[48:51], v[60:75]\n"
"ds_read_b64_tr_b16 a[20:21], v39 offset:7168\n"
"ds_read_b64_tr_b16 a[22:23], v39 offset:7680\n"
"v_lshl_add_u32 v76, v76, 3, v77\n"
"v_and_b32 v77, 1, v255\n"
"v_lshl_add_u32 v76, v76, 1, v77\n"
"s_waitcnt lgkmcnt(8)\n"
"v_mfma_f32_32x32x16_bf16 v[20:35], a[4:7], a[44:47], v[20:35]\n"
"v_lshlrev_b32 v76, 3, v76\n"
"buffer_load_dwordx4 a[100:103], v189, s[48:51], s95 offen offset:1024 sc1\n"
"v_add_u32 v197, s79, v76\n"
"v_mfma_f32_32x32x16_bf16 v[120:135], a[16:19], a[44:47], v[120:135]\n"
"s_mov_b32 s44, s32\n"
"s_and_b32 s45, s33, 0xffff\n"
"s_mov_b32 s46, 0x500000\n"
"s_mov_b32 s47, 0x20000\n"
"s_mov_b32 s88, 5120\n"
"s_sub_u32 s86, s76, 1\n"
"v_mfma_f32_32x32x16_bf16 v[160:175], a[12:15], a[44:47], v[160:175]\n"
"s_and_b32 s86, s86, 3\n"
"s_mul_i32 s86, s86, 40960\n"
"v_add_u32 v192, s86, v189\n"
"s_sub_u32 s86, s76, 2\n"
"buffer_load_dwordx4 a[212:215], v189, s[48:51], s95 offen offset:2048 sc1\n"
"v_mfma_f32_32x32x16_bf16 v[60:75], a[8:11], a[44:47], v[60:75]\n"
"s_and_b32 s86, s86, 3\n"
"s_mul_i32 s86, s86, 40960\n"
"v_add_u32 v191, s86, v189\n"
"s_sub_u32 s86, s76, 3\n"
"s_and_b32 s86, s86, 3\n"
"s_mul_i32 s86, s86, 40960\n"
"s_waitcnt lgkmcnt(0)\n"
"v_mfma_f32_32x32x16_bf16 v[20:35], a[0:3], a[36:39], v[20:35]\n"
"v_add_u32 v254, s86, v189\n"
"s_lshl_b32 s86, s77, 1\n"
"s_add_u32 s86, s86, s27\n"
"s_lshl_b32 s86, s86, 3\n"
"s_add_u32 s86, s86, s26\n"
"buffer_load_dwordx4 a[208:211], v189, s[48:51], s95 offen offset:3072 sc1\n"
"v_mfma_f32_32x32x16_bf16 v[120:135], a[56:59], a[36:39], v[120:135]\n"
"s_lshl_b32 s86, s86, 2\n"
"s_add_u32 s86, s86, s76\n"
"s_xor_b32 s87, s86, 32\n"
"s_mul_i32 s60, s86, 5120\n"
"s_mul_i32 s61, s87, 5120\n"
"s_add_u32 s60, s60, 15728640\n"
"v_mfma_f32_32x32x16_bf16 v[160:175], a[24:27], a[36:39], v[160:175]\n"
"s_add_u32 s61, s61, 15728640\n"
"s_add_u32 s62, s60, 4096\n"
"s_add_u32 s63, s61, 4096\n"
"v_mfma_f32_32x32x16_bf16 v[60:75], a[20:23], a[36:39], v[60:75]\n"
"s_add_u32 s95, s96, 48128\n"
"buffer_load_dwordx4 a[216:219], v189, s[48:51], s95 offen sc1\n"
"v_mfma_f32_32x32x16_bf16 v[20:35], v[140:143], v[176:179], v[20:35]\n"
"v_mfma_f32_32x32x16_bf16 v[120:135], v[144:147], v[176:179], v[120:135]\n"
"buffer_load_dwordx4 a[204:207], v189, s[48:51], s95 offen offset:1024 sc1\n"
"v_mfma_f32_32x32x16_bf16 v[160:175], v[148:151], v[176:179], v[160:175]\n"
"v_mfma_f32_32x32x16_bf16 v[60:75], v[152:155], v[176:179], v[60:75]\n"
"s_waitcnt vmcnt(24)\n"
"v_subrev_u32 v144, s16, v104\n"
"v_lshrrev_b32 v144, 1, v144\n"
"v_bitop3_b32 v144, v105, s81, v144 bitop3:0xbe\n"
"v_cmp_ne_u32 vcc, 0, v144\n"
"s_nop 1\n"
"s_cmp_lg_u32 vcc_lo, 0\n"
"s_cbranch_scc1 SLOWAH0_%=\n"
"BACKAH0_%=:\n"
"s_add_u32 s29, s93, 0\n"
"buffer_load_dwordx4 v[152:155], v193, s[48:51], s29 offen sc1\n"
"s_barrier\n"
"v_pk_mul_f32 v[140:141], v[20:21], v[20:21]\n"
"v_pk_mul_f32 v[142:143], v[22:23], v[22:23]\n"
"v_pk_mul_f32 v[144:145], v[24:25], v[24:25]\n"
"v_pk_mul_f32 v[146:147], v[26:27], v[26:27]\n"
"v_pk_fma_f32 v[140:141], v[140:141], v[136:137], v[158:159] op_sel_hi:[1,0,0]\n"
"v_pk_fma_f32 v[142:143], v[142:143], v[136:137], v[158:159] op_sel_hi:[1,0,0]\n"
"v_pk_fma_f32 v[144:145], v[144:145], v[136:137], v[158:159] op_sel_hi:[1,0,0]\n"
"buffer_load_dwordx4 v[148:151], v196, s[48:51], s29 offen offset:1024 sc1\n"
"v_pk_fma_f32 v[146:147], v[146:147], v[136:137], v[158:159] op_sel_hi:[1,0,0]\n"
"v_pk_mul_f32 v[140:141], v[140:141], v[20:21]\n"
"v_pk_mul_f32 v[142:143], v[142:143], v[22:23]\n"
"v_pk_mul_f32 v[144:145], v[144:145], v[24:25]\n"
"v_pk_mul_f32 v[146:147], v[146:147], v[26:27]\n"
"v_exp_f32 v140, v140\n"
"v_exp_f32 v141, v141\n"
"v_pk_mul_f32 v[28:29], v[20:21], v[28:29]\n"
"v_exp_f32 v142, v142\n"
"v_exp_f32 v143, v143\n"
"v_pk_mul_f32 v[30:31], v[22:23], v[30:31]\n"
"v_exp_f32 v144, v144\n"
"v_exp_f32 v145, v145\n"
"v_pk_mul_f32 v[32:33], v[24:25], v[32:33]\n"
"buffer_load_dwordx4 v[36:39], v195, s[48:51], s29 offen offset:2048 sc1\n"
"v_exp_f32 v146, v146\n"
"v_exp_f32 v147, v147\n"
"v_pk_mul_f32 v[34:35], v[26:27], v[34:35]\n"
"v_pk_fma_f32 v[140:141], v[140:141], v[156:157], v[156:157] op_sel_hi:[1,0,0]\n"
"v_pk_fma_f32 v[142:143], v[142:143], v[156:157], v[156:157] op_sel_hi:[1,0,0]\n"
"v_pk_fma_f32 v[144:145], v[144:145], v[156:157], v[156:157] op_sel_hi:[1,0,0]\n"
"v_pk_fma_f32 v[146:147], v[146:147], v[156:157], v[156:157] op_sel_hi:[1,0,0]\n"
"v_rcp_f32 v140, v140\n"
"v_rcp_f32 v141, v141\n"
"v_rcp_f32 v142, v142\n"
"v_rcp_f32 v143, v143\n"
"v_rcp_f32 v144, v144\n"
"v_rcp_f32 v145, v145\n"
"buffer_load_dwordx4 a[36:39], v194, s[48:51], s29 offen offset:3072 sc1\n"
"v_rcp_f32 v146, v146\n"
"v_rcp_f32 v147, v147\n"
"v_pk_mul_f32 v[28:29], v[28:29], v[140:141]\n"
"v_pk_mul_f32 v[30:31], v[30:31], v[142:143]\n"
"v_pk_mul_f32 v[32:33], v[32:33], v[144:145]\n"
"v_pk_mul_f32 v[34:35], v[34:35], v[146:147]\n"
"v_cvt_pk_bf16_f32 v176, v28, v29\n"
"v_cvt_pk_bf16_f32 v177, v30, v31\n"
"v_cvt_pk_bf16_f32 v178, v32, v33\n"
"v_cvt_pk_bf16_f32 v179, v34, v35\n"
"s_add_u32 s14, s92, 618496\n"
"buffer_store_dwordx4 v[176:179], v138, s[48:51], s14 offen sc1\n"
"v_pk_mul_f32 v[140:141], v[120:121], v[120:121]\n"
"v_pk_mul_f32 v[142:143], v[122:123], v[122:123]\n"
"v_pk_mul_f32 v[144:145], v[124:125], v[124:125]\n"
"v_pk_mul_f32 v[146:147], v[126:127], v[126:127]\n"
"v_pk_fma_f32 v[140:141], v[140:141], v[136:137], v[158:159] op_sel_hi:[1,0,0]\n"
"v_pk_fma_f32 v[142:143], v[142:143], v[136:137], v[158:159] op_sel_hi:[1,0,0]\n"
"v_pk_fma_f32 v[144:145], v[144:145], v[136:137], v[158:159] op_sel_hi:[1,0,0]\n"
"v_pk_fma_f32 v[146:147], v[146:147], v[136:137], v[158:159] op_sel_hi:[1,0,0]\n"
"v_pk_mul_f32 v[140:141], v[140:141], v[120:121]\n"
"v_pk_mul_f32 v[142:143], v[142:143], v[122:123]\n"
"v_pk_mul_f32 v[144:145], v[144:145], v[124:125]\n"
"v_pk_mul_f32 v[146:147], v[146:147], v[126:127]\n"
"v_exp_f32 v140, v140\n"
"s_add_u32 s29, s93, 4096\n"
"buffer_load_dwordx4 v[20:23], v193, s[48:51], s29 offen sc1\n"
"v_exp_f32 v141, v141\n"
"v_pk_mul_f32 v[128:129], v[120:121], v[128:129]\n"
"v_exp_f32 v142, v142\n"
"v_exp_f32 v143, v143\n"
"v_pk_mul_f32 v[130:131], v[122:123], v[130:131]\n"
"v_exp_f32 v144, v144\n"
"v_exp_f32 v145, v145\n"
"v_pk_mul_f32 v[132:133], v[124:125], v[132:133]\n"
"v_exp_f32 v146, v146\n"
"v_exp_f32 v147, v147\n"
"v_pk_mul_f32 v[134:135], v[126:127], v[134:135]\n"
"v_pk_fma_f32 v[140:141], v[140:141], v[156:157], v[156:157] op_sel_hi:[1,0,0]\n"
"v_pk_fma_f32 v[142:143], v[142:143], v[156:157], v[156:157] op_sel_hi:[1,0,0]\n"
"v_pk_fma_f32 v[144:145], v[144:145], v[156:157], v[156:157] op_sel_hi:[1,0,0]\n"
"buffer_load_dwordx4 v[24:27], v196, s[48:51], s29 offen offset:1024 sc1\n"
"v_pk_fma_f32 v[146:147], v[146:147], v[156:157], v[156:157] op_sel_hi:[1,0,0]\n"
"v_rcp_f32 v140, v140\n"
"v_rcp_f32 v141, v141\n"
"v_rcp_f32 v142, v142\n"
"v_rcp_f32 v143, v143\n"
"v_rcp_f32 v144, v144\n"
"v_rcp_f32 v145, v145\n"
"v_rcp_f32 v146, v146\n"
"v_rcp_f32 v147, v147\n"
"v_pk_mul_f32 v[128:129], v[128:129], v[140:141]\n"
"v_pk_mul_f32 v[130:131], v[130:131], v[142:143]\n"
"v_pk_mul_f32 v[132:133], v[132:133], v[144:145]\n"
"buffer_load_dwordx4 v[28:31], v195, s[48:51], s29 offen offset:2048 sc1\n"
"v_pk_mul_f32 v[134:135], v[134:135], v[146:147]\n"
"v_cvt_pk_bf16_f32 v176, v128, v129\n"
"v_cvt_pk_bf16_f32 v177, v130, v131\n"
"v_cvt_pk_bf16_f32 v178, v132, v133\n"
"v_cvt_pk_bf16_f32 v179, v134, v135\n"
"buffer_store_dwordx4 v[176:179], v138, s[48:51], s14 offen offset:512 sc1\n"
"v_pk_mul_f32 v[140:141], v[160:161], v[160:161]\n"
"v_pk_mul_f32 v[142:143], v[162:163], v[162:163]\n"
"v_pk_mul_f32 v[144:145], v[164:165], v[164:165]\n"
"v_pk_mul_f32 v[146:147], v[166:167], v[166:167]\n"
"v_pk_fma_f32 v[140:141], v[140:141], v[136:137], v[158:159] op_sel_hi:[1,0,0]\n"
"v_pk_fma_f32 v[142:143], v[142:143], v[136:137], v[158:159] op_sel_hi:[1,0,0]\n"
"v_pk_fma_f32 v[144:145], v[144:145], v[136:137], v[158:159] op_sel_hi:[1,0,0]\n"
"v_pk_fma_f32 v[146:147], v[146:147], v[136:137], v[158:159] op_sel_hi:[1,0,0]\n"
"v_pk_mul_f32 v[140:141], v[140:141], v[160:161]\n"
"v_pk_mul_f32 v[142:143], v[142:143], v[162:163]\n"
"v_pk_mul_f32 v[144:145], v[144:145], v[164:165]\n"
"v_pk_mul_f32 v[146:147], v[146:147], v[166:167]\n"
"v_exp_f32 v140, v140\n"
"v_exp_f32 v141, v141\n"
"v_pk_mul_f32 v[168:169], v[160:161], v[168:169]\n"
"v_exp_f32 v142, v142\n"
"v_exp_f32 v143, v143\n"
"v_pk_mul_f32 v[170:171], v[162:163], v[170:171]\n"
"v_exp_f32 v144, v144\n"
"buffer_load_dwordx4 v[32:35], v194, s[48:51], s29 offen offset:3072 sc1\n"
"v_exp_f32 v145, v145\n"
"v_pk_mul_f32 v[172:173], v[164:165], v[172:173]\n"
"v_exp_f32 v146, v146\n"
"v_exp_f32 v147, v147\n"
"v_pk_mul_f32 v[174:175], v[166:167], v[174:175]\n"
"v_pk_fma_f32 v[140:141], v[140:141], v[156:157], v[156:157] op_sel_hi:[1,0,0]\n"
"v_pk_fma_f32 v[142:143], v[142:143], v[156:157], v[156:157] op_sel_hi:[1,0,0]\n"
"v_pk_fma_f32 v[144:145], v[144:145], v[156:157], v[156:157] op_sel_hi:[1,0,0]\n"
"v_pk_fma_f32 v[146:147], v[146:147], v[156:157], v[156:157] op_sel_hi:[1,0,0]\n"
"v_rcp_f32 v140, v140\n"
"v_rcp_f32 v141, v141\n"
"v_rcp_f32 v142, v142\n"
"v_rcp_f32 v143, v143\n"
"s_add_u32 s29, s97, 0\n"
"buffer_load_dwordx4 a[24:27], v193, s[48:51], s29 offen sc1\n"
"v_rcp_f32 v144, v144\n"
"v_rcp_f32 v145, v145\n"
"v_rcp_f32 v146, v146\n"
"v_rcp_f32 v147, v147\n"
"v_pk_mul_f32 v[168:169], v[168:169], v[140:141]\n"
"v_pk_mul_f32 v[170:171], v[170:171], v[142:143]\n"
"v_pk_mul_f32 v[172:173], v[172:173], v[144:145]\n"
"v_pk_mul_f32 v[174:175], v[174:175], v[146:147]\n"
"v_cvt_pk_bf16_f32 v140, v168, v169\n"
"v_cvt_pk_bf16_f32 v141, v170, v171\n"
"v_cvt_pk_bf16_f32 v142, v172, v173\n"
"v_cvt_pk_bf16_f32 v143, v174, v175\n"
"s_add_u32 s14, s92, 622592\n"
"buffer_store_dwordx4 v[140:143], v138, s[48:51], s14 offen sc1\n"
"v_pk_mul_f32 v[160:161], v[60:61], v[60:61]\n"
"v_pk_mul_f32 v[162:163], v[62:63], v[62:63]\n"
"v_pk_mul_f32 v[164:165], v[64:65], v[64:65]\n"
"v_pk_mul_f32 v[166:167], v[66:67], v[66:67]\n"
"v_pk_fma_f32 v[160:161], v[160:161], v[136:137], v[158:159] op_sel_hi:[1,0,0]\n"
"v_pk_fma_f32 v[162:163], v[162:163], v[136:137], v[158:159] op_sel_hi:[1,0,0]\n"
"v_pk_fma_f32 v[164:165], v[164:165], v[136:137], v[158:159] op_sel_hi:[1,0,0]\n"
"v_pk_fma_f32 v[166:167], v[166:167], v[136:137], v[158:159] op_sel_hi:[1,0,0]\n"
"v_pk_mul_f32 v[160:161], v[160:161], v[60:61]\n"
"v_pk_mul_f32 v[162:163], v[162:163], v[62:63]\n"
"v_pk_mul_f32 v[164:165], v[164:165], v[64:65]\n"
"buffer_load_dwordx2 v[104:105], v190, s[48:51], 0 offen sc1\n"
"v_pk_mul_f32 v[166:167], v[166:167], v[66:67]\n"
"v_exp_f32 v160, v160\n"
"v_exp_f32 v161, v161\n"
"v_pk_mul_f32 v[68:69], v[60:61], v[68:69]\n"
"v_exp_f32 v162, v162\n"
"v_exp_f32 v163, v163\n"
"v_pk_mul_f32 v[70:71], v[62:63], v[70:71]\n"
"v_exp_f32 v164, v164\n"
"v_exp_f32 v165, v165\n"
"v_pk_mul_f32 v[72:73], v[64:65], v[72:73]\n"
"v_exp_f32 v166, v166\n"
"v_exp_f32 v167, v167\n"
"v_pk_mul_f32 v[74:75], v[66:67], v[74:75]\n"
"buffer_load_dwordx4 a[20:23], v196, s[48:51], s29 offen offset:1024 sc1\n"
"v_pk_fma_f32 v[160:161], v[160:161], v[156:157], v[156:157] op_sel_hi:[1,0,0]\n"
"v_pk_fma_f32 v[162:163], v[162:163], v[156:157], v[156:157] op_sel_hi:[1,0,0]\n"
"v_pk_fma_f32 v[164:165], v[164:165], v[156:157], v[156:157] op_sel_hi:[1,0,0]\n"
"v_pk_fma_f32 v[166:167], v[166:167], v[156:157], v[156:157] op_sel_hi:[1,0,0]\n"
"v_rcp_f32 v160, v160\n"
"v_rcp_f32 v161, v161\n"
"v_rcp_f32 v162, v162\n"
"v_rcp_f32 v163, v163\n"
"v_rcp_f32 v164, v164\n"
"v_rcp_f32 v165, v165\n"
"v_rcp_f32 v166, v166\n"
"v_rcp_f32 v167, v167\n"
"v_pk_mul_f32 v[68:69], v[68:69], v[160:161]\n"
"buffer_load_dwordx4 a[56:59], v195, s[48:51], s29 offen offset:2048 sc1\n"
"v_pk_mul_f32 v[70:71], v[70:71], v[162:163]\n"
"v_pk_mul_f32 v[72:73], v[72:73], v[164:165]\n"
"v_pk_mul_f32 v[74:75], v[74:75], v[166:167]\n"
"v_cvt_pk_bf16_f32 v160, v68, v69\n"
"v_cvt_pk_bf16_f32 v161, v70, v71\n"
"v_cvt_pk_bf16_f32 v162, v72, v73\n"
"v_cvt_pk_bf16_f32 v163, v74, v75\n"
"buffer_store_dwordx4 v[160:163], v138, s[48:51], s14 offen offset:512 sc1\n"
"s_waitcnt vmcnt(13)\n"
"v_mfma_f32_16x16x32_bf16 v[60:63], v[152:155], a[120:123], 0\n"
"v_lshlrev_b32 v184, 16, v184\n"
"v_mfma_f32_16x16x32_bf16 v[64:67], v[152:155], a[68:71], 0\n"
"v_lshlrev_b32 v185, 16, v185\n"
"v_lshlrev_b32 v186, 16, v186\n"
"v_mfma_f32_16x16x32_bf16 v[68:71], v[152:155], a[112:115], 0\n"
"v_lshlrev_b32 v187, 16, v187\n"
"v_lshlrev_b32 v188, 16, v188\n"
"v_mfma_f32_16x16x32_bf16 v[72:75], v[152:155], a[96:99], 0\n"
"v_and_b32 v180, s82, v40\n"
"v_mfma_f32_16x16x32_bf16 v[76:79], v[152:155], a[168:171], 0\n"
"v_rcp_f32 v180, v180\n"
"s_waitcnt vmcnt(12)\n"
"v_mfma_f32_16x16x32_bf16 v[160:163], v[148:151], a[120:123], 0\n"
"v_lshlrev_b32 v181, 16, v40\n"
"buffer_load_dwordx4 a[44:47], v194, s[48:51], s29 offen offset:3072 sc1\n"
"v_mfma_f32_16x16x32_bf16 v[164:167], v[148:151], a[68:71], 0\n"
"v_lshlrev_b32 v0, 16, v0\n"
"v_lshlrev_b32 v4, 16, v4\n"
"v_mfma_f32_16x16x32_bf16 v[168:171], v[148:151], a[112:115], 0\n"
"v_lshlrev_b32 v8, 16, v8\n"
"v_lshlrev_b32 v12, 16, v12\n"
"v_mfma_f32_16x16x32_bf16 v[172:175], v[148:151], a[96:99], 0\n"
"v_lshlrev_b32 v16, 16, v16\n"
"v_add_f32 v182, v0, v181\n"
"v_mfma_f32_16x16x32_bf16 v[176:179], v[148:151], a[168:171], 0\n"
"v_mul_f32 v182, v182, v180\n"
"v_fmac_f32 v0, v182, v184\n"
"s_add_u32 s29, s97, 4096\n"
"buffer_load_dwordx4 a[8:11], v193, s[48:51], s29 offen sc1\n"
"v_mfma_f32_16x16x32_bf16 v[140:143], v[36:39], a[120:123], 0\n"
"v_add_f32 v183, v4, v181\n"
"v_mfma_f32_16x16x32_bf16 v[144:147], v[36:39], a[68:71], 0\n"
"v_mul_f32 v183, v183, v180\n"
"v_fmac_f32 v4, v183, v185\n"
"v_mfma_f32_16x16x32_bf16 v[148:151], v[36:39], a[112:115], 0\n"
"v_add_f32 v182, v8, v181\n"
"v_mul_f32 v182, v182, v180\n"
"v_mfma_f32_16x16x32_bf16 v[152:155], v[36:39], a[96:99], 0\n"
"v_fmac_f32 v8, v182, v186\n"
"buffer_load_dwordx4 a[0:3], v196, s[48:51], s29 offen offset:1024 sc1\n"
"v_mfma_f32_16x16x32_bf16 v[156:159], v[36:39], a[168:171], 0\n"
"v_add_f32 v183, v12, v181\n"
"v_mul_f32 v183, v183, v180\n"
"s_waitcnt vmcnt(13)\n"
"v_mfma_f32_16x16x32_bf16 v[120:123], a[36:39], a[120:123], 0\n"
"v_fmac_f32 v12, v183, v187\n"
"v_mfma_f32_16x16x32_bf16 v[124:127], a[36:39], a[68:71], 0\n"
"v_add_f32 v182, v16, v181\n"
"v_mul_f32 v182, v182, v180\n"
"v_mfma_f32_16x16x32_bf16 v[128:131], a[36:39], a[112:115], 0\n"
"v_fmac_f32 v16, v182, v188\n"
"v_and_b32 v180, s82, v41\n"
"buffer_load_dwordx4 a[12:15], v195, s[48:51], s29 offen offset:2048 sc1\n"
"v_mfma_f32_16x16x32_bf16 v[132:135], a[36:39], a[96:99], 0\n"
"v_rcp_f32 v180, v180\n"
"v_mfma_f32_16x16x32_bf16 v[136:139], a[36:39], a[168:171], 0\n"
"v_lshlrev_b32 v181, 16, v41\n"
"v_lshlrev_b32 v1, 16, v1\n"
"s_waitcnt vmcnt(13)\n"
"v_mfma_f32_16x16x32_bf16 v[60:63], v[20:23], a[116:119], v[60:63]\n"
"v_lshlrev_b32 v5, 16, v5\n"
"v_mfma_f32_16x16x32_bf16 v[64:67], v[20:23], a[140:143], v[64:67]\n"
"v_lshlrev_b32 v9, 16, v9\n"
"buffer_load_dwordx4 a[32:35], v194, s[48:51], s29 offen offset:3072 sc1\n"
"v_mfma_f32_16x16x32_bf16 v[68:71], v[20:23], a[72:75], v[68:71]\n"
"v_lshlrev_b32 v13, 16, v13\n"
"v_lshlrev_b32 v17, 16, v17\n"
"v_mfma_f32_16x16x32_bf16 v[72:75], v[20:23], a[64:67], v[72:75]\n"
"v_add_f32 v182, v1, v181\n"
"v_mul_f32 v182, v182, v180\n"
"v_mfma_f32_16x16x32_bf16 v[76:79], v[20:23], a[52:55], v[76:79]\n"
"v_fmac_f32 v1, v182, v184\n"
"v_add_f32 v183, v5, v181\n"
"s_waitcnt vmcnt(13)\n"
"v_mfma_f32_16x16x32_bf16 v[160:163], v[24:27], a[116:119], v[160:163]\n"
"v_mul_f32 v183, v183, v180\n"
"buffer_load_dwordx4 a[220:223], v189, s[48:51], s95 offen offset:2048 sc1\n"
"v_mfma_f32_16x16x32_bf16 v[164:167], v[24:27], a[140:143], v[164:167]\n"
"v_fmac_f32 v5, v183, v185\n"
"v_add_f32 v182, v9, v181\n"
"v_mfma_f32_16x16x32_bf16 v[168:171], v[24:27], a[72:75], v[168:171]\n"
"v_mul_f32 v182, v182, v180\n"
"v_fmac_f32 v9, v182, v186\n"
"v_mfma_f32_16x16x32_bf16 v[172:175], v[24:27], a[64:67], v[172:175]\n"
"v_add_f32 v183, v13, v181\n"
"v_mul_f32 v183, v183, v180\n"
"v_mfma_f32_16x16x32_bf16 v[176:179], v[24:27], a[52:55], v[176:179]\n"
"v_fmac_f32 v13, v183, v187\n"
"buffer_load_dwordx4 a[224:227], v189, s[48:51], s95 offen offset:3072 sc1\n"
"v_mfma_f32_16x16x32_bf16 v[140:143], v[28:31], a[116:119], v[140:143]\n"
"v_add_f32 v182, v17, v181\n"
"v_mfma_f32_16x16x32_bf16 v[144:147], v[28:31], a[140:143], v[144:147]\n"
"v_mul_f32 v182, v182, v180\n"
"v_fmac_f32 v17, v182, v188\n"
"v_mfma_f32_16x16x32_bf16 v[148:151], v[28:31], a[72:75], v[148:151]\n"
"v_and_b32 v180, s82, v42\n"
"v_mfma_f32_16x16x32_bf16 v[152:155], v[28:31], a[64:67], v[152:155]\n"
"v_rcp_f32 v180, v180\n"
"s_add_u32 s95, s96, 52224\n"
"buffer_load_dwordx4 a[228:231], v189, s[48:51], s95 offen sc1\n"
"v_mfma_f32_16x16x32_bf16 v[156:159], v[28:31], a[52:55], v[156:159]\n"
"v_lshlrev_b32 v181, 16, v42\n"
"v_lshlrev_b32 v2, 16, v2\n"
"s_waitcnt vmcnt(14)\n"
"v_mfma_f32_16x16x32_bf16 v[120:123], v[32:35], a[116:119], v[120:123]\n"
"v_lshlrev_b32 v6, 16, v6\n"
"v_mfma_f32_16x16x32_bf16 v[124:127], v[32:35], a[140:143], v[124:127]\n"
"v_lshlrev_b32 v10, 16, v10\n"
"v_lshlrev_b32 v14, 16, v14\n"
"v_mfma_f32_16x16x32_bf16 v[128:131], v[32:35], a[72:75], v[128:131]\n"
"v_lshlrev_b32 v18, 16, v18\n"
"buffer_load_dwordx4 a[232:235], v189, s[48:51], s95 offen offset:1024 sc1\n"
"v_mfma_f32_16x16x32_bf16 v[132:135], v[32:35], a[64:67], v[132:135]\n"
"v_add_f32 v182, v2, v181\n"
"v_mul_f32 v182, v182, v180\n"
"v_mfma_f32_16x16x32_bf16 v[136:139], v[32:35], a[52:55], v[136:139]\n"
"v_fmac_f32 v2, v182, v184\n"
"v_add_f32 v183, v6, v181\n"
"s_waitcnt vmcnt(14)\n"
"v_mfma_f32_16x16x32_bf16 v[20:23], a[24:27], a[120:123], 0\n"
"v_mul_f32 v183, v183, v180\n"
"v_mfma_f32_16x16x32_bf16 v[24:27], a[24:27], a[68:71], 0\n"
"s_waitcnt vmcnt(12)\n"
"v_subrev_u32 v100, s16, v104\n"
"v_lshrrev_b32 v100, 1, v100\n"
"v_bitop3_b32 v100, v105, s81, v100 bitop3:0xbe\n"
"v_cmp_ne_u32 vcc, 0, v100\n"
"s_nop 1\n"
"s_cmp_lg_u32 vcc_hi, 0\n"
"s_cbranch_scc1 SLOWAH3_%=\n"
"BACKAH3_%=:\n"
"s_add_u32 m0, s79, 8192\n"
"s_add_u32 s29, s93, 8192\n"
"buffer_load_dwordx4 v193, s[48:51], s29 offen sc1 lds\n"
"v_mfma_f32_16x16x32_bf16 v[28:31], a[24:27], a[112:115], 0\n"
"v_fmac_f32 v6, v183, v185\n"
"v_add_f32 v182, v10, v181\n"
"v_mfma_f32_16x16x32_bf16 v[32:35], a[24:27], a[96:99], 0\n"
"v_mul_f32 v182, v182, v180\n"
"v_fmac_f32 v10, v182, v186\n"
"v_mfma_f32_16x16x32_bf16 v[36:39], a[24:27], a[168:171], 0\n"
"v_add_f32 v183, v14, v181\n"
"buffer_load_dwordx4 v196, s[48:51], s29 offen offset:1024 sc1 lds\n"
"s_waitcnt vmcnt(13)\n"
"v_mfma_f32_16x16x32_bf16 v[100:103], a[20:23], a[120:123], 0\n"
"v_mul_f32 v183, v183, v180\n"
"v_mfma_f32_16x16x32_bf16 v[104:107], a[20:23], a[68:71], 0\n"
"v_fmac_f32 v14, v183, v187\n"
"v_add_f32 v182, v18, v181\n"
"v_mfma_f32_16x16x32_bf16 v[108:111], a[20:23], a[112:115], 0\n"
"v_mul_f32 v182, v182, v180\n"
"v_fmac_f32 v18, v182, v188\n"
"v_mfma_f32_16x16x32_bf16 v[112:115], a[20:23], a[96:99], 0\n"
"v_and_b32 v180, s82, v43\n"
"buffer_load_dwordx4 v195, s[48:51], s29 offen offset:2048 sc1 lds\n"
"v_mfma_f32_16x16x32_bf16 v[116:119], a[20:23], a[168:171], 0\n"
"v_rcp_f32 v180, v180\n"
"s_waitcnt vmcnt(13)\n"
"v_mfma_f32_16x16x32_bf16 v[80:83], a[56:59], a[120:123], 0\n"
"v_lshlrev_b32 v181, 16, v43\n"
"v_mfma_f32_16x16x32_bf16 v[84:87], a[56:59], a[68:71], 0\n"
"v_lshlrev_b32 v3, 16, v3\n"
"v_lshlrev_b32 v7, 16, v7\n"
"v_mfma_f32_16x16x32_bf16 v[88:91], a[56:59], a[112:115], 0\n"
"v_lshlrev_b32 v11, 16, v11\n"
"v_lshlrev_b32 v15, 16, v15\n"
"buffer_load_dwordx4 v194, s[48:51], s29 offen offset:3072 sc1 lds\n"
"v_mfma_f32_16x16x32_bf16 v[92:95], a[56:59], a[96:99], 0\n"
"v_lshlrev_b32 v19, 16, v19\n"
"v_add_f32 v182, v3, v181\n"
"v_mfma_f32_16x16x32_bf16 v[96:99], a[56:59], a[168:171], 0\n"
"v_mul_f32 v182, v182, v180\n"
"v_fmac_f32 v3, v182, v184\n"
"s_waitcnt vmcnt(12)\n"
"v_mfma_f32_16x16x32_bf16 v[40:43], a[44:47], a[120:123], 0\n"
"v_add_f32 v183, v7, v181\n"
"v_mfma_f32_16x16x32_bf16 v[44:47], a[44:47], a[68:71], 0\n"
"v_mul_f32 v183, v183, v180\n"
"s_add_u32 s89, s16, 1\n"
"v_mov_b32 v250, s89\n"
"v_mov_b32 v251, s81\n"
"s_mov_b64 exec, 1\n"
"buffer_store_dwordx2 v[250:251], off, s[48:51], s28 sc1\n"
"s_mov_b64 exec, -1\n"
"v_mfma_f32_16x16x32_bf16 v[48:51], a[44:47], a[112:115], 0\n"
"v_fmac_f32 v7, v183, v185\n"
"v_add_f32 v182, v11, v181\n"
"v_mfma_f32_16x16x32_bf16 v[52:55], a[44:47], a[96:99], 0\n"
"v_mul_f32 v182, v182, v180\n"
"v_fmac_f32 v11, v182, v186\n"
"v_mfma_f32_16x16x32_bf16 v[56:59], a[44:47], a[168:171], 0\n"
"v_add_f32 v183, v15, v181\n"
"v_mul_f32 v183, v183, v180\n"
"s_add_u32 m0, s79, 12288\n"
"s_add_u32 s29, s93, 12288\n"
"buffer_load_dwordx4 v193, s[48:51], s29 offen sc1 lds\n"
"v_fmac_f32 v15, v183, v187\n"
"v_add_f32 v182, v19, v181\n"
"v_mul_f32 v182, v182, v180\n"
"v_fmac_f32 v19, v182, v188\n"
"buffer_load_dwordx4 v196, s[48:51], s29 offen offset:1024 sc1 lds\n"
"buffer_load_dwordx4 v195, s[48:51], s29 offen offset:2048 sc1 lds\n"
"buffer_load_dwordx4 v194, s[48:51], s29 offen offset:3072 sc1 lds\n"
"s_add_u32 m0, s79, 0\n"
"s_add_u32 s29, s93, 16384\n"
"buffer_load_dwordx4 v193, s[48:51], s29 offen sc1 lds\n"
"buffer_load_dwordx4 v196, s[48:51], s29 offen offset:1024 sc1 lds\n"
"buffer_load_dwordx4 v195, s[48:51], s29 offen offset:2048 sc1 lds\n"
"buffer_load_dwordx4 v194, s[48:51], s29 offen offset:3072 sc1 lds\n"
"s_add_u32 m0, s79, 4096\n"
"s_add_u32 s29, s93, 20480\n"
"buffer_load_dwordx4 v193, s[48:51], s29 offen sc1 lds\n"
"buffer_load_dwordx4 v196, s[48:51], s29 offen offset:1024 sc1 lds\n"
"buffer_load_dwordx4 v195, s[48:51], s29 offen offset:2048 sc1 lds\n"
"s_waitcnt vmcnt(20)\n"
"v_mfma_f32_16x16x32_bf16 v[20:23], a[8:11], a[116:119], v[20:23]\n"
"s_waitcnt vmcnt(12)\n"
"v_mfma_f32_16x16x32_bf16 v[24:27], a[8:11], a[140:143], v[24:27]\n"
"ds_read_b64_tr_b16 a[24:25], v197 offset:8192\n"
"v_mfma_f32_16x16x32_bf16 v[28:31], a[8:11], a[72:75], v[28:31]\n"
"ds_read_b128 a[36:39], v253 offset:28672\n"
"buffer_load_dwordx4 v194, s[48:51], s29 offen offset:3072 sc1 lds\n"
"v_mfma_f32_16x16x32_bf16 v[32:35], a[8:11], a[64:67], v[32:35]\n"
"ds_read_b64_tr_b16 a[26:27], v197 offset:8256\n"
"v_mfma_f32_16x16x32_bf16 v[36:39], a[8:11], a[52:55], v[36:39]\n"
"ds_read_b128 a[48:51], v253 offset:29696\n"
"v_mfma_f32_16x16x32_bf16 v[100:103], a[0:3], a[116:119], v[100:103]\n"
"ds_read_b64_tr_b16 a[20:21], v197 offset:9216\n"
"v_mfma_f32_16x16x32_bf16 v[104:107], a[0:3], a[140:143], v[104:107]\n"
"ds_read_b128 a[44:47], v253 offset:30720\n"
"s_add_u32 m0, s79, 16384\n"
"s_add_u32 s29, s93, 24576\n"
"buffer_load_dwordx4 v193, s[48:51], s29 offen sc1 lds\n"
"v_mfma_f32_16x16x32_bf16 v[108:111], a[0:3], a[72:75], v[108:111]\n"
"ds_read_b64_tr_b16 a[22:23], v197 offset:9280\n"
"v_mfma_f32_16x16x32_bf16 v[112:115], a[0:3], a[64:67], v[112:115]\n"
"ds_read_b64_tr_b16 a[8:9], v197 offset:10240\n"
"v_mfma_f32_16x16x32_bf16 v[116:119], a[0:3], a[52:55], v[116:119]\n"
"ds_read_b128 a[56:59], v253 offset:31744\n"
"buffer_load_dwordx4 v196, s[48:51], s29 offen offset:1024 sc1 lds\n"
"v_mfma_f32_16x16x32_bf16 v[80:83], a[12:15], a[116:119], v[80:83]\n"
"ds_read_b64_tr_b16 a[10:11], v197 offset:10304\n"
"v_mfma_f32_16x16x32_bf16 v[84:87], a[12:15], a[140:143], v[84:87]\n"
"ds_read_b128 a[4:7], v253 offset:32768\n"
"v_mfma_f32_16x16x32_bf16 v[88:91], a[12:15], a[72:75], v[88:91]\n"
"ds_read_b64_tr_b16 a[16:17], v197 offset:11264\n"
"v_mfma_f32_16x16x32_bf16 v[92:95], a[12:15], a[64:67], v[92:95]\n"
"v_mfma_f32_16x16x32_bf16 v[96:99], a[12:15], a[52:55], v[96:99]\n"
"ds_read_b64_tr_b16 a[18:19], v197 offset:11328\n"
"buffer_load_dwordx4 v195, s[48:51], s29 offen offset:2048 sc1 lds\n"
"v_mfma_f32_16x16x32_bf16 v[40:43], a[32:35], a[116:119], v[40:43]\n"
"v_mfma_f32_16x16x32_bf16 v[44:47], a[32:35], a[140:143], v[44:47]\n"
"v_mfma_f32_16x16x32_bf16 v[48:51], a[32:35], a[72:75], v[48:51]\n"
"v_mfma_f32_16x16x32_bf16 v[52:55], a[32:35], a[64:67], v[52:55]\n"
"buffer_load_dwordx4 v194, s[48:51], s29 offen offset:3072 sc1 lds\n"
"v_mfma_f32_16x16x32_bf16 v[56:59], a[32:35], a[52:55], v[56:59]\n"
"buffer_load_dwordx4 a[64:67], v189, s[48:51], s95 offen offset:2048 sc1\n"
"s_waitcnt lgkmcnt(0)\n"
"v_mfma_f32_16x16x32_bf16 v[60:63], a[24:27], a[36:39], v[60:63]\n"
"s_waitcnt vmcnt(13)\n"
"v_mfma_f32_16x16x32_bf16 v[64:67], a[24:27], a[48:51], v[64:67]\n"
"ds_read_b64_tr_b16 a[32:33], v197 offset:12288\n"
"v_mfma_f32_16x16x32_bf16 v[68:71], a[24:27], a[44:47], v[68:71]\n"
"ds_read_b128 a[0:3], v253 offset:33792\n"
"buffer_load_dwordx4 a[52:55], v189, s[48:51], s95 offen offset:3072 sc1\n"
"v_mfma_f32_16x16x32_bf16 v[72:75], a[24:27], a[56:59], v[72:75]\n"
"ds_read_b64_tr_b16 a[34:35], v197 offset:12352\n"
"v_mfma_f32_16x16x32_bf16 v[76:79], a[24:27], a[4:7], v[76:79]\n"
"ds_read_b128 a[12:15], v253 offset:34816\n"
"v_mfma_f32_16x16x32_bf16 v[160:163], a[20:23], a[36:39], v[160:163]\n"
"ds_read_b64_tr_b16 a[28:29], v197 offset:13312\n"
"s_add_u32 m0, s79, 8192\n"
"s_add_u32 s29, s97, 8192\n"
"buffer_load_dwordx4 v193, s[48:51], s29 offen sc1 lds\n"
"v_mfma_f32_16x16x32_bf16 v[164:167], a[20:23], a[48:51], v[164:167]\n"
"v_mfma_f32_16x16x32_bf16 v[168:171], a[20:23], a[44:47], v[168:171]\n"
"ds_read_b64_tr_b16 a[30:31], v197 offset:13376\n"
"v_mfma_f32_16x16x32_bf16 v[172:175], a[20:23], a[56:59], v[172:175]\n"
"ds_read_b64_tr_b16 a[24:25], v197 offset:14336\n"
"v_mfma_f32_16x16x32_bf16 v[176:179], a[20:23], a[4:7], v[176:179]\n"
"v_mfma_f32_16x16x32_bf16 v[140:143], a[8:11], a[36:39], v[140:143]\n"
"ds_read_b64_tr_b16 a[26:27], v197 offset:14400\n"
"buffer_load_dwordx4 v196, s[48:51], s29 offen offset:1024 sc1 lds\n"
"v_mfma_f32_16x16x32_bf16 v[144:147], a[8:11], a[48:51], v[144:147]\n"
"v_mfma_f32_16x16x32_bf16 v[148:151], a[8:11], a[44:47], v[148:151]\n"
"ds_read_b64_tr_b16 a[20:21], v197 offset:15360\n"
"v_mfma_f32_16x16x32_bf16 v[152:155], a[8:11], a[56:59], v[152:155]\n"
"v_mfma_f32_16x16x32_bf16 v[156:159], a[8:11], a[4:7], v[156:159]\n"
"ds_read_b64_tr_b16 a[22:23], v197 offset:15424\n"
"buffer_load_dwordx4 v195, s[48:51], s29 offen offset:2048 sc1 lds\n"
"v_mfma_f32_16x16x32_bf16 v[120:123], a[16:19], a[36:39], v[120:123]\n"
"v_mfma_f32_16x16x32_bf16 v[124:127], a[16:19], a[48:51], v[124:127]\n"
"v_mfma_f32_16x16x32_bf16 v[128:131], a[16:19], a[44:47], v[128:131]\n"
"v_mfma_f32_16x16x32_bf16 v[132:135], a[16:19], a[56:59], v[132:135]\n"
"buffer_load_dwordx4 v194, s[48:51], s29 offen offset:3072 sc1 lds\n"
"v_mfma_f32_16x16x32_bf16 v[136:139], a[16:19], a[4:7], v[136:139]\n"
"s_waitcnt lgkmcnt(0)\n"
"v_mfma_f32_16x16x32_bf16 v[60:63], a[32:35], a[0:3], v[60:63]\n"
"s_waitcnt vmcnt(14)\n"
"v_mfma_f32_16x16x32_bf16 v[64:67], a[32:35], a[12:15], v[64:67]\n"
"ds_read_b64_tr_b16 a[44:45], v197\n"
"v_mfma_f32_16x16x32_bf16 v[68:71], a[32:35], a[176:179], v[68:71]\n"
"s_add_u32 m0, s79, 12288\n"
"s_add_u32 s29, s97, 12288\n"
"buffer_load_dwordx4 v193, s[48:51], s29 offen sc1 lds\n"
"v_mfma_f32_16x16x32_bf16 v[72:75], a[32:35], a[188:191], v[72:75]\n"
"ds_read_b64_tr_b16 a[46:47], v197 offset:64\n"
"v_mfma_f32_16x16x32_bf16 v[76:79], a[32:35], a[160:163], v[76:79]\n"
"v_mfma_f32_16x16x32_bf16 v[160:163], a[28:31], a[0:3], v[160:163]\n"
"ds_read_b64_tr_b16 a[48:49], v197 offset:1024\n"
"v_mfma_f32_16x16x32_bf16 v[164:167], a[28:31], a[12:15], v[164:167]\n"
"buffer_load_dwordx4 v196, s[48:51], s29 offen offset:1024 sc1 lds\n"
"v_mfma_f32_16x16x32_bf16 v[168:171], a[28:31], a[176:179], v[168:171]\n"
"ds_read_b64_tr_b16 a[50:51], v197 offset:1088\n"
"v_mfma_f32_16x16x32_bf16 v[172:175], a[28:31], a[188:191], v[172:175]\n"
"ds_read_b64_tr_b16 a[56:57], v197 offset:2048\n"
"v_mfma_f32_16x16x32_bf16 v[176:179], a[28:31], a[160:163], v[176:179]\n"
"v_mfma_f32_16x16x32_bf16 v[140:143], a[24:27], a[0:3], v[140:143]\n"
"ds_read_b64_tr_b16 a[58:59], v197 offset:2112\n"
"buffer_load_dwordx4 v195, s[48:51], s29 offen offset:2048 sc1 lds\n"
"v_mfma_f32_16x16x32_bf16 v[144:147], a[24:27], a[12:15], v[144:147]\n"
"v_mfma_f32_16x16x32_bf16 v[148:151], a[24:27], a[176:179], v[148:151]\n"
"ds_read_b64_tr_b16 a[36:37], v197 offset:3072\n"
"v_mfma_f32_16x16x32_bf16 v[152:155], a[24:27], a[188:191], v[152:155]\n"
"v_mfma_f32_16x16x32_bf16 v[156:159], a[24:27], a[160:163], v[156:159]\n"
"ds_read_b64_tr_b16 a[38:39], v197 offset:3136\n"
"buffer_load_dwordx4 v194, s[48:51], s29 offen offset:3072 sc1 lds\n"
"v_mfma_f32_16x16x32_bf16 v[120:123], a[20:23], a[0:3], v[120:123]\n"
"v_mfma_f32_16x16x32_bf16 v[124:127], a[20:23], a[12:15], v[124:127]\n"
"v_mfma_f32_16x16x32_bf16 v[128:131], a[20:23], a[176:179], v[128:131]\n"
"v_mfma_f32_16x16x32_bf16 v[132:135], a[20:23], a[188:191], v[132:135]\n"
"v_mfma_f32_16x16x32_bf16 v[136:139], a[20:23], a[160:163], v[136:139]\n"
"s_add_u32 m0, s79, 20480\n"
"s_add_u32 s29, s97, 16384\n"
"buffer_load_dwordx4 v193, s[48:51], s29 offen sc1 lds\n"
"s_waitcnt lgkmcnt(0)\n"
"v_mfma_f32_16x16x32_bf16 v[60:63], a[44:47], a[172:175], v[60:63]\n"
"s_waitcnt vmcnt(15)\n"
"v_mfma_f32_16x16x32_bf16 v[64:67], a[44:47], a[180:183], v[64:67]\n"
"ds_read_b64_tr_b16 a[24:25], v197 offset:4096\n"
"v_mfma_f32_16x16x32_bf16 v[68:71], a[44:47], a[184:187], v[68:71]\n"
"v_mfma_f32_16x16x32_bf16 v[72:75], a[44:47], a[152:155], v[72:75]\n"
"ds_read_b64_tr_b16 a[26:27], v197 offset:4160\n"
"buffer_load_dwordx4 v196, s[48:51], s29 offen offset:1024 sc1 lds\n"
"v_mfma_f32_16x16x32_bf16 v[76:79], a[44:47], a[144:147], v[76:79]\n"
"v_mfma_f32_16x16x32_bf16 v[160:163], a[48:51], a[172:175], v[160:163]\n"
"ds_read_b64_tr_b16 a[12:13], v197 offset:5120\n"
"v_mfma_f32_16x16x32_bf16 v[164:167], a[48:51], a[180:183], v[164:167]\n"
"v_mfma_f32_16x16x32_bf16 v[168:171], a[48:51], a[184:187], v[168:171]\n"
"ds_read_b64_tr_b16 a[14:15], v197 offset:5184\n"
"buffer_load_dwordx4 v195, s[48:51], s29 offen offset:2048 sc1 lds\n"
"v_mfma_f32_16x16x32_bf16 v[172:175], a[48:51], a[152:155], v[172:175]\n"
"ds_read_b64_tr_b16 a[0:1], v197 offset:6144\n"
"v_mfma_f32_16x16x32_bf16 v[176:179], a[48:51], a[144:147], v[176:179]\n"
"v_mfma_f32_16x16x32_bf16 v[140:143], a[56:59], a[172:175], v[140:143]\n"
"ds_read_b64_tr_b16 a[2:3], v197 offset:6208\n"
"v_mfma_f32_16x16x32_bf16 v[144:147], a[56:59], a[180:183], v[144:147]\n"
"buffer_load_dwordx4 v194, s[48:51], s29 offen offset:3072 sc1 lds\n"
"v_mfma_f32_16x16x32_bf16 v[148:151], a[56:59], a[184:187], v[148:151]\n"
"ds_read_b64_tr_b16 a[48:49], v197 offset:7168\n"
"v_mfma_f32_16x16x32_bf16 v[152:155], a[56:59], a[152:155], v[152:155]\n"
"v_mfma_f32_16x16x32_bf16 v[156:159], a[56:59], a[144:147], v[156:159]\n"
"ds_read_b64_tr_b16 a[50:51], v197 offset:7232\n"
"v_mfma_f32_16x16x32_bf16 v[120:123], a[36:39], a[172:175], v[120:123]\n"
"s_add_u32 m0, s79, 24576\n"
"s_add_u32 s29, s97, 20480\n"
"buffer_load_dwordx4 v193, s[48:51], s29 offen sc1 lds\n"
"v_mfma_f32_16x16x32_bf16 v[124:127], a[36:39], a[180:183], v[124:127]\n"
"v_mfma_f32_16x16x32_bf16 v[128:131], a[36:39], a[184:187], v[128:131]\n"
"v_mfma_f32_16x16x32_bf16 v[132:135], a[36:39], a[152:155], v[132:135]\n"
"v_mfma_f32_16x16x32_bf16 v[136:139], a[36:39], a[144:147], v[136:139]\n"
"s_waitcnt lgkmcnt(0)\n"
"v_mfma_f32_16x16x32_bf16 v[60:63], a[24:27], a[192:195], v[60:63]\n"
"s_waitcnt vmcnt(15)\n"
"buffer_load_dwordx4 v196, s[48:51], s29 offen offset:1024 sc1 lds\n"
"v_mfma_f32_16x16x32_bf16 v[64:67], a[24:27], a[196:199], v[64:67]\n"
"ds_read_b64_tr_b16 a[44:45], v197 offset:16384\n"
"v_mfma_f32_16x16x32_bf16 v[68:71], a[24:27], a[164:167], v[68:71]\n"
"v_mfma_f32_16x16x32_bf16 v[72:75], a[24:27], a[136:139], v[72:75]\n"
"ds_read_b64_tr_b16 a[46:47], v197 offset:16448\n"
"buffer_load_dwordx4 v195, s[48:51], s29 offen offset:2048 sc1 lds\n"
"v_mfma_f32_16x16x32_bf16 v[76:79], a[24:27], a[156:159], v[76:79]\n"
"v_mfma_f32_16x16x32_bf16 v[160:163], a[12:15], a[192:195], v[160:163]\n"
"ds_read_b64_tr_b16 a[56:57], v197 offset:17408\n"
"v_mfma_f32_16x16x32_bf16 v[164:167], a[12:15], a[196:199], v[164:167]\n"
"v_mfma_f32_16x16x32_bf16 v[168:171], a[12:15], a[164:167], v[168:171]\n"
"ds_read_b64_tr_b16 a[58:59], v197 offset:17472\n"
"buffer_load_dwordx4 v194, s[48:51], s29 offen offset:3072 sc1 lds\n"
"v_mfma_f32_16x16x32_bf16 v[172:175], a[12:15], a[136:139], v[172:175]\n"
"ds_read_b64_tr_b16 a[24:25], v197 offset:18432\n"
"v_mfma_f32_16x16x32_bf16 v[176:179], a[12:15], a[156:159], v[176:179]\n"
"v_mfma_f32_16x16x32_bf16 v[140:143], a[0:3], a[192:195], v[140:143]\n"
"ds_read_b64_tr_b16 a[26:27], v197 offset:18496\n"
"v_mfma_f32_16x16x32_bf16 v[144:147], a[0:3], a[196:199], v[144:147]\n"
"v_mfma_f32_16x16x32_bf16 v[148:151], a[0:3], a[164:167], v[148:151]\n"
"ds_read_b64_tr_b16 a[36:37], v197 offset:19456\n"
"buffer_load_dwordx2 v[180:181], v190, s[48:51], 0 offen sc1\n"
"v_mfma_f32_16x16x32_bf16 v[152:155], a[0:3], a[136:139], v[152:155]\n"
"v_mfma_f32_16x16x32_bf16 v[156:159], a[0:3], a[156:159], v[156:159]\n"
"ds_read_b64_tr_b16 a[38:39], v197 offset:19520\n"
"v_mfma_f32_16x16x32_bf16 v[120:123], a[48:51], a[192:195], v[120:123]\n"
"v_mfma_f32_16x16x32_bf16 v[124:127], a[48:51], a[196:199], v[124:127]\n"
"s_add_u32 m0, s79, 35840\n"
"s_add_u32 s29, s97, 24576\n"
"buffer_load_dwordx4 v193, s[48:51], s29 offen sc1 lds\n"
"v_mfma_f32_16x16x32_bf16 v[128:131], a[48:51], a[164:167], v[128:131]\n"
"v_mfma_f32_16x16x32_bf16 v[132:135], a[48:51], a[136:139], v[132:135]\n"
"v_mfma_f32_16x16x32_bf16 v[136:139], a[48:51], a[156:159], v[136:139]\n"
"s_waitcnt lgkmcnt(0)\n"
"v_mfma_f32_16x16x32_bf16 v[60:63], a[44:47], a[200:203], v[60:63]\n"
"s_waitcnt vmcnt(14)\n"
"buffer_load_dwordx4 v196, s[48:51], s29 offen offset:1024 sc1 lds\n"
"v_mfma_f32_16x16x32_bf16 v[64:67], a[44:47], a[124:127], v[64:67]\n"
"ds_read_b64_tr_b16 a[12:13], v197 offset:8192\n"
"v_mfma_f32_16x16x32_bf16 v[68:71], a[44:47], a[80:83], v[68:71]\n"
"ds_read_b128 a[0:3], v253 offset:28672\n"
"v_mfma_f32_16x16x32_bf16 v[72:75], a[44:47], a[132:135], v[72:75]\n"
"ds_read_b64_tr_b16 a[14:15], v197 offset:8256\n"
"buffer_load_dwordx4 v195, s[48:51], s29 offen offset:2048 sc1 lds\n"
"v_mfma_f32_16x16x32_bf16 v[76:79], a[44:47], a[128:131], v[76:79]\n"
"ds_read_b128 a[48:51], v253 offset:29696\n"
"v_mfma_f32_16x16x32_bf16 v[160:163], a[56:59], a[200:203], v[160:163]\n"
"ds_read_b64_tr_b16 a[20:21], v197 offset:9216\n"
"v_mfma_f32_16x16x32_bf16 v[164:167], a[56:59], a[124:127], v[164:167]\n"
"ds_read_b128 a[44:47], v253 offset:30720\n"
"v_mfma_f32_16x16x32_bf16 v[168:171], a[56:59], a[80:83], v[168:171]\n"
"ds_read_b64_tr_b16 a[22:23], v197 offset:9280\n"
"buffer_load_dwordx4 v194, s[48:51], s29 offen offset:3072 sc1 lds\n"
"v_mfma_f32_16x16x32_bf16 v[172:175], a[56:59], a[132:135], v[172:175]\n"
"ds_read_b64_tr_b16 a[32:33], v197 offset:10240\n"
"v_mfma_f32_16x16x32_bf16 v[176:179], a[56:59], a[128:131], v[176:179]\n"
"ds_read_b128 a[4:7], v253 offset:31744\n"
"v_mfma_f32_16x16x32_bf16 v[140:143], a[24:27], a[200:203], v[140:143]\n"
"ds_read_b64_tr_b16 a[34:35], v197 offset:10304\n"
"v_mfma_f32_16x16x32_bf16 v[144:147], a[24:27], a[124:127], v[144:147]\n"
"ds_read_b128 a[56:59], v253 offset:32768\n"
"v_mfma_f32_16x16x32_bf16 v[148:151], a[24:27], a[80:83], v[148:151]\n"
"ds_read_b64_tr_b16 a[28:29], v197 offset:11264\n"
"v_mfma_f32_16x16x32_bf16 v[152:155], a[24:27], a[132:135], v[152:155]\n"
"v_mfma_f32_16x16x32_bf16 v[156:159], a[24:27], a[128:131], v[156:159]\n"
"ds_read_b64_tr_b16 a[30:31], v197 offset:11328\n"
"v_mfma_f32_16x16x32_bf16 v[120:123], a[36:39], a[200:203], v[120:123]\n"
"v_mfma_f32_16x16x32_bf16 v[124:127], a[36:39], a[124:127], v[124:127]\n"
"v_mfma_f32_16x16x32_bf16 v[128:131], a[36:39], a[80:83], v[128:131]\n"
"v_mfma_f32_16x16x32_bf16 v[132:135], a[36:39], a[132:135], v[132:135]\n"
"v_mfma_f32_16x16x32_bf16 v[136:139], a[36:39], a[128:131], v[136:139]\n"
"s_waitcnt lgkmcnt(0)\n"
"v_mfma_f32_16x16x32_bf16 v[20:23], a[12:15], a[0:3], v[20:23]\n"
"s_waitcnt vmcnt(13)\n"
"v_mfma_f32_16x16x32_bf16 v[24:27], a[12:15], a[48:51], v[24:27]\n"
"ds_read_b64_tr_b16 a[16:17], v197 offset:12288\n"
"v_mfma_f32_16x16x32_bf16 v[28:31], a[12:15], a[44:47], v[28:31]\n"
"ds_read_b128 a[24:27], v253 offset:33792\n"
"v_mfma_f32_16x16x32_bf16 v[32:35], a[12:15], a[4:7], v[32:35]\n"
"ds_read_b64_tr_b16 a[18:19], v197 offset:12352\n"
"v_mfma_f32_16x16x32_bf16 v[36:39], a[12:15], a[56:59], v[36:39]\n"
"ds_read_b128 a[36:39], v253 offset:34816\n"
"v_mfma_f32_16x16x32_bf16 v[100:103], a[20:23], a[0:3], v[100:103]\n"
"ds_read_b64_tr_b16 a[12:13], v197 offset:13312\n"
"v_mfma_f32_16x16x32_bf16 v[104:107], a[20:23], a[48:51], v[104:107]\n"
"v_mfma_f32_16x16x32_bf16 v[108:111], a[20:23], a[44:47], v[108:111]\n"
"ds_read_b64_tr_b16 a[14:15], v197 offset:13376\n"
"v_mfma_f32_16x16x32_bf16 v[112:115], a[20:23], a[4:7], v[112:115]\n"
"ds_read_b64_tr_b16 a[8:9], v197 offset:14336\n"
"v_mfma_f32_16x16x32_bf16 v[116:119], a[20:23], a[56:59], v[116:119]\n"
"v_mfma_f32_16x16x32_bf16 v[80:83], a[32:35], a[0:3], v[80:83]\n"
"ds_read_b64_tr_b16 a[10:11], v197 offset:14400\n"
"v_mfma_f32_16x16x32_bf16 v[84:87], a[32:35], a[48:51], v[84:87]\n"
"v_mfma_f32_16x16x32_bf16 v[88:91], a[32:35], a[44:47], v[88:91]\n"
"ds_read_b64_tr_b16 a[20:21], v197 offset:15360\n"
"v_mfma_f32_16x16x32_bf16 v[92:95], a[32:35], a[4:7], v[92:95]\n"
"v_mfma_f32_16x16x32_bf16 v[96:99], a[32:35], a[56:59], v[96:99]\n"
"ds_read_b64_tr_b16 a[22:23], v197 offset:15424\n"
"v_mfma_f32_16x16x32_bf16 v[40:43], a[28:31], a[0:3], v[40:43]\n"
"v_mfma_f32_16x16x32_bf16 v[44:47], a[28:31], a[48:51], v[44:47]\n"
"v_mfma_f32_16x16x32_bf16 v[48:51], a[28:31], a[44:47], v[48:51]\n"
"v_mfma_f32_16x16x32_bf16 v[52:55], a[28:31], a[4:7], v[52:55]\n"
"v_mfma_f32_16x16x32_bf16 v[56:59], a[28:31], a[56:59], v[56:59]\n"
"s_waitcnt lgkmcnt(0)\n"
"v_mfma_f32_16x16x32_bf16 v[20:23], a[16:19], a[24:27], v[20:23]\n"
"v_mfma_f32_16x16x32_bf16 v[24:27], a[16:19], a[36:39], v[24:27]\n"
"v_mfma_f32_16x16x32_bf16 v[28:31], a[16:19], a[176:179], v[28:31]\n"
"v_mfma_f32_16x16x32_bf16 v[32:35], a[16:19], a[188:191], v[32:35]\n"
"v_mfma_f32_16x16x32_bf16 v[36:39], a[16:19], a[160:163], v[36:39]\n"
"v_mfma_f32_16x16x32_bf16 v[100:103], a[12:15], a[24:27], v[100:103]\n"
"v_mfma_f32_16x16x32_bf16 v[104:107], a[12:15], a[36:39], v[104:107]\n"
"v_mfma_f32_16x16x32_bf16 v[108:111], a[12:15], a[176:179], v[108:111]\n"
"v_mfma_f32_16x16x32_bf16 v[112:115], a[12:15], a[188:191], v[112:115]\n"
"v_mfma_f32_16x16x32_bf16 v[116:119], a[12:15], a[160:163], v[116:119]\n"
"v_mfma_f32_16x16x32_bf16 v[80:83], a[8:11], a[24:27], v[80:83]\n"
"v_mfma_f32_16x16x32_bf16 v[84:87], a[8:11], a[36:39], v[84:87]\n"
"v_mfma_f32_16x16x32_bf16 v[88:91], a[8:11], a[176:179], v[88:91]\n"
"v_mfma_f32_16x16x32_bf16 v[92:95], a[8:11], a[188:191], v[92:95]\n"
"v_mfma_f32_16x16x32_bf16 v[96:99], a[8:11], a[160:163], v[96:99]\n"
"v_mfma_f32_16x16x32_bf16 v[40:43], a[20:23], a[24:27], v[40:43]\n"
"v_mfma_f32_16x16x32_bf16 v[44:47], a[20:23], a[36:39], v[44:47]\n"
"v_mfma_f32_16x16x32_bf16 v[48:51], a[20:23], a[176:179], v[48:51]\n"
"s_waitcnt vmcnt(4)\n"
"v_subrev_u32 v182, s16, v180\n"
"v_xor_b32 v182, 1, v182\n"
"v_bitop3_b32 v182, v181, s81, v182 bitop3:0xbe\n"
"v_cmp_ne_u32 vcc, 0, v182\n"
"s_nop 1\n"
"s_cmp_lg_u64 vcc, 0\n"
"s_cbranch_scc1 SLOWAH1_%=\n"
"BACKAH1_%=:\n"
"s_add_u32 s29, s93, 28672\n"
"buffer_load_dwordx4 a[24:27], v196, s[48:51], s29 offen offset:1024 sc1\n"
"v_mfma_f32_16x16x32_bf16 v[52:55], a[20:23], a[188:191], v[52:55]\n"
"v_mfma_f32_16x16x32_bf16 v[56:59], a[20:23], a[160:163], v[56:59]\n"
"s_add_u32 s29, s93, 32768\n"
"buffer_load_dwordx4 a[36:39], v196, s[48:51], s29 offen offset:1024 sc1\n"
"s_add_u32 s29, s93, 36864\n"
"buffer_load_dwordx4 a[0:3], v196, s[48:51], s29 offen offset:1024 sc1\n"
"s_add_u32 s29, s93, 40960\n"
"buffer_load_dwordx4 a[48:51], v196, s[48:51], s29 offen offset:1024 sc1\n"
"s_add_u32 s29, s93, 28672\n"
"buffer_load_dwordx4 a[44:47], v195, s[48:51], s29 offen offset:2048 sc1\n"
"s_add_u32 s29, s93, 32768\n"
"buffer_load_dwordx4 a[56:59], v195, s[48:51], s29 offen offset:2048 sc1\n"
"s_add_u32 s29, s93, 36864\n"
"buffer_load_dwordx4 v[180:183], v195, s[48:51], s29 offen offset:2048 sc1\n"
"s_add_u32 s29, s93, 40960\n"
"buffer_load_dwordx4 v[184:187], v195, s[48:51], s29 offen offset:2048 sc1\n"
"s_add_u32 s29, s93, 28672\n"
"buffer_load_dwordx4 a[20:23], v194, s[48:51], s29 offen offset:3072 sc1\n"
"s_add_u32 s29, s93, 32768\n"
"buffer_load_dwordx4 a[160:163], v194, s[48:51], s29 offen offset:3072 sc1\n"
"s_add_u32 s29, s93, 36864\n"
"buffer_load_dwordx4 a[188:191], v194, s[48:51], s29 offen offset:3072 sc1\n"
"s_waitcnt vmcnt(10)\n"
"v_mfma_f32_16x16x32_bf16 v[160:163], a[24:27], a[76:79], v[160:163]\n"
"v_mfma_f32_16x16x32_bf16 v[164:167], a[24:27], a[104:107], v[164:167]\n"
"s_add_u32 s29, s93, 40960\n"
"buffer_load_dwordx4 a[176:179], v194, s[48:51], s29 offen offset:3072 sc1\n"
"v_mfma_f32_16x16x32_bf16 v[168:171], a[24:27], a[148:151], v[168:171]\n"
"v_mfma_f32_16x16x32_bf16 v[172:175], a[24:27], a[40:43], v[172:175]\n"
"v_mfma_f32_16x16x32_bf16 v[176:179], a[24:27], a[88:91], v[176:179]\n"
"s_waitcnt vmcnt(10)\n"
"v_mfma_f32_16x16x32_bf16 v[160:163], a[36:39], a[60:63], v[160:163]\n"
"s_add_u32 s29, s93, 28672\n"
"buffer_load_dwordx4 a[8:11], v193, s[48:51], s29 offen sc1\n"
"v_mfma_f32_16x16x32_bf16 v[164:167], a[36:39], a[92:95], v[164:167]\n"
"v_mfma_f32_16x16x32_bf16 v[168:171], a[36:39], a[84:87], v[168:171]\n"
"v_mfma_f32_16x16x32_bf16 v[172:175], a[36:39], a[108:111], v[172:175]\n"
"v_mfma_f32_16x16x32_bf16 v[176:179], a[36:39], a[100:103], v[176:179]\n"
"s_waitcnt vmcnt(10)\n"
"v_mfma_f32_16x16x32_bf16 v[160:163], a[0:3], a[212:215], v[160:163]\n"
"s_add_u32 s29, s93, 32768\n"
"buffer_load_dwordx4 a[36:39], v193, s[48:51], s29 offen sc1\n"
"v_mfma_f32_16x16x32_bf16 v[164:167], a[0:3], a[208:211], v[164:167]\n"
"v_mfma_f32_16x16x32_bf16 v[168:171], a[0:3], a[216:219], v[168:171]\n"
"v_mfma_f32_16x16x32_bf16 v[172:175], a[0:3], a[204:207], v[172:175]\n"
"v_mfma_f32_16x16x32_bf16 v[176:179], a[0:3], a[220:223], v[176:179]\n"
"s_add_u32 s29, s93, 36864\n"
"buffer_load_dwordx4 a[12:15], v193, s[48:51], s29 offen sc1\n"
"s_waitcnt vmcnt(11)\n"
"v_mfma_f32_16x16x32_bf16 v[160:163], a[48:51], a[224:227], v[160:163]\n"
"v_mfma_f32_16x16x32_bf16 v[164:167], a[48:51], a[228:231], v[164:167]\n"
"v_mfma_f32_16x16x32_bf16 v[168:171], a[48:51], a[232:235], v[168:171]\n"
"v_mfma_f32_16x16x32_bf16 v[172:175], a[48:51], a[64:67], v[172:175]\n"
"s_add_u32 s29, s93, 40960\n"
"buffer_load_dwordx4 a[16:19], v193, s[48:51], s29 offen sc1\n"
"v_mfma_f32_16x16x32_bf16 v[176:179], a[48:51], a[52:55], v[176:179]\n"
"s_waitcnt vmcnt(11)\n"
"v_mfma_f32_16x16x32_bf16 v[140:143], a[44:47], a[76:79], v[140:143]\n"
"v_mfma_f32_16x16x32_bf16 v[144:147], a[44:47], a[104:107], v[144:147]\n"
"v_mfma_f32_16x16x32_bf16 v[148:151], a[44:47], a[148:151], v[148:151]\n"
"ds_write_b128 v253, v[160:163]\n"
"s_add_u32 s29, s97, 28672\n"
"buffer_load_dwordx4 a[28:31], v196, s[48:51], s29 offen offset:1024 sc1\n"
"v_mfma_f32_16x16x32_bf16 v[152:155], a[44:47], a[40:43], v[152:155]\n"
"v_mfma_f32_16x16x32_bf16 v[156:159], a[44:47], a[88:91], v[156:159]\n"
"s_waitcnt vmcnt(11)\n"
"v_mfma_f32_16x16x32_bf16 v[140:143], a[56:59], a[60:63], v[140:143]\n"
"ds_write_b128 v253, v[164:167] offset:1024\n"
"s_add_u32 s29, s97, 32768\n"
"buffer_load_dwordx4 a[4:7], v196, s[48:51], s29 offen offset:1024 sc1\n"
"v_mfma_f32_16x16x32_bf16 v[144:147], a[56:59], a[92:95], v[144:147]\n"
"v_mfma_f32_16x16x32_bf16 v[148:151], a[56:59], a[84:87], v[148:151]\n"
"v_mfma_f32_16x16x32_bf16 v[152:155], a[56:59], a[108:111], v[152:155]\n"
"ds_write_b128 v253, v[168:171] offset:2048\n"
"s_add_u32 s29, s97, 36864\n"
"buffer_load_dwordx4 a[32:35], v196, s[48:51], s29 offen offset:1024 sc1\n"
"v_mfma_f32_16x16x32_bf16 v[156:159], a[56:59], a[100:103], v[156:159]\n"
"s_waitcnt vmcnt(12)\n"
"v_mfma_f32_16x16x32_bf16 v[140:143], v[180:183], a[212:215], v[140:143]\n"
"s_add_u32 s29, s97, 40960\n"
"buffer_load_dwordx4 a[72:75], v196, s[48:51], s29 offen offset:1024 sc1\n"
"v_mfma_f32_16x16x32_bf16 v[144:147], v[180:183], a[208:211], v[144:147]\n"
"ds_write_b128 v253, v[172:175] offset:3072\n"
"v_mfma_f32_16x16x32_bf16 v[148:151], v[180:183], a[216:219], v[148:151]\n"
"v_mfma_f32_16x16x32_bf16 v[152:155], v[180:183], a[204:207], v[152:155]\n"
"s_add_u32 s29, s97, 28672\n"
"buffer_load_dwordx4 a[140:143], v195, s[48:51], s29 offen offset:2048 sc1\n"
"v_mfma_f32_16x16x32_bf16 v[156:159], v[180:183], a[220:223], v[156:159]\n"
"ds_write_b128 v253, v[176:179] offset:4096\n"
"s_waitcnt vmcnt(13)\n"
"v_mfma_f32_16x16x32_bf16 v[140:143], v[184:187], a[224:227], v[140:143]\n"
"s_add_u32 s29, s97, 32768\n"
"buffer_load_dwordx4 a[116:119], v195, s[48:51], s29 offen offset:2048 sc1\n"
"v_mfma_f32_16x16x32_bf16 v[144:147], v[184:187], a[228:231], v[144:147]\n"
"v_mfma_f32_16x16x32_bf16 v[148:151], v[184:187], a[232:235], v[148:151]\n"
"v_mfma_f32_16x16x32_bf16 v[152:155], v[184:187], a[64:67], v[152:155]\n"
"v_mfma_f32_16x16x32_bf16 v[156:159], v[184:187], a[52:55], v[156:159]\n"
"s_add_u32 s29, s97, 36864\n"
"buffer_load_dwordx4 a[168:171], v195, s[48:51], s29 offen offset:2048 sc1\n"
"s_waitcnt vmcnt(14)\n"
"v_mfma_f32_16x16x32_bf16 v[120:123], a[20:23], a[76:79], v[120:123]\n"
"v_mfma_f32_16x16x32_bf16 v[124:127], a[20:23], a[104:107], v[124:127]\n"
"v_mfma_f32_16x16x32_bf16 v[128:131], a[20:23], a[148:151], v[128:131]\n"
"ds_write_b128 v253, v[140:143] offset:5120\n"
"s_add_u32 s29, s97, 40960\n"
"buffer_load_dwordx4 a[96:99], v195, s[48:51], s29 offen offset:2048 sc1\n"
"v_mfma_f32_16x16x32_bf16 v[132:135], a[20:23], a[40:43], v[132:135]\n"
"v_mfma_f32_16x16x32_bf16 v[136:139], a[20:23], a[88:91], v[136:139]\n"
"s_waitcnt vmcnt(14)\n"
"v_mfma_f32_16x16x32_bf16 v[120:123], a[160:163], a[60:63], v[120:123]\n"
"ds_write_b128 v253, v[144:147] offset:6144\n"
"s_add_u32 s29, s97, 28672\n"
"buffer_load_dwordx4 a[20:23], v194, s[48:51], s29 offen offset:3072 sc1\n"
"v_mfma_f32_16x16x32_bf16 v[124:127], a[160:163], a[92:95], v[124:127]\n"
"v_mfma_f32_16x16x32_bf16 v[128:131], a[160:163], a[84:87], v[128:131]\n"
"v_mfma_f32_16x16x32_bf16 v[132:135], a[160:163], a[108:111], v[132:135]\n"
"ds_write_b128 v253, v[148:151] offset:7168\n"
"s_add_u32 s29, s97, 32768\n"
"buffer_load_dwordx4 a[112:115], v194, s[48:51], s29 offen offset:3072 sc1\n"
"v_mfma_f32_16x16x32_bf16 v[136:139], a[160:163], a[100:103], v[136:139]\n"
"s_waitcnt vmcnt(13)\n"
"v_mfma_f32_16x16x32_bf16 v[120:123], a[188:191], a[212:215], v[120:123]\n"
"s_add_u32 s29, s97, 36864\n"
"buffer_load_dwordx4 a[160:163], v194, s[48:51], s29 offen offset:3072 sc1\n"
"v_mfma_f32_16x16x32_bf16 v[124:127], a[188:191], a[208:211], v[124:127]\n"
"ds_write_b128 v253, v[152:155] offset:8192\n"
"v_mfma_f32_16x16x32_bf16 v[128:131], a[188:191], a[216:219], v[128:131]\n"
"v_mfma_f32_16x16x32_bf16 v[132:135], a[188:191], a[204:207], v[132:135]\n"
"s_add_u32 s29, s97, 40960\n"
"buffer_load_dwordx4 a[68:71], v194, s[48:51], s29 offen offset:3072 sc1\n"
"v_mfma_f32_16x16x32_bf16 v[136:139], a[188:191], a[220:223], v[136:139]\n"
"ds_write_b128 v253, v[156:159] offset:9216\n"
"s_waitcnt vmcnt(13)\n"
"v_mfma_f32_16x16x32_bf16 v[120:123], a[176:179], a[224:227], v[120:123]\n"
"s_add_u32 s29, s97, 28672\n"
"buffer_load_dwordx4 a[188:191], v193, s[48:51], s29 offen sc1\n"
"v_mfma_f32_16x16x32_bf16 v[124:127], a[176:179], a[228:231], v[124:127]\n"
"v_mfma_f32_16x16x32_bf16 v[128:131], a[176:179], a[232:235], v[128:131]\n"
"v_mfma_f32_16x16x32_bf16 v[132:135], a[176:179], a[64:67], v[132:135]\n"
"v_mfma_f32_16x16x32_bf16 v[136:139], a[176:179], a[52:55], v[136:139]\n"
"s_add_u32 s29, s97, 32768\n"
"buffer_load_dwordx4 a[120:123], v193, s[48:51], s29 offen sc1\n"
"s_waitcnt vmcnt(14)\n"
"v_mfma_f32_16x16x32_bf16 v[60:63], a[8:11], a[76:79], v[60:63]\n"
"v_mfma_f32_16x16x32_bf16 v[64:67], a[8:11], a[104:107], v[64:67]\n"
"ds_read_b64_tr_b16 a[24:25], v197 offset:20480\n"
"v_mfma_f32_16x16x32_bf16 v[68:71], a[8:11], a[148:151], v[68:71]\n"
"ds_write_b128 v253, v[120:123] offset:10240\n"
"s_add_u32 s29, s97, 36864\n"
"buffer_load_dwordx4 a[176:179], v193, s[48:51], s29 offen sc1\n"
"v_mfma_f32_16x16x32_bf16 v[72:75], a[8:11], a[40:43], v[72:75]\n"
"ds_read_b64_tr_b16 a[26:27], v197 offset:20544\n"
"v_mfma_f32_16x16x32_bf16 v[76:79], a[8:11], a[88:91], v[76:79]\n"
"ds_write_b128 v253, v[124:127] offset:11264\n"
"s_add_u32 s29, s97, 40960\n"
"buffer_load_dwordx4 a[236:239], v193, s[48:51], s29 offen sc1\n"
"v_mfma_f32_16x16x32_bf16 v[60:63], a[36:39], a[60:63], v[60:63]\n"
"ds_read_b64_tr_b16 a[0:1], v197 offset:21504\n"
"v_mfma_f32_16x16x32_bf16 v[64:67], a[36:39], a[92:95], v[64:67]\n"
"ds_write_b128 v253, v[128:131] offset:12288\n"
"v_mfma_f32_16x16x32_bf16 v[68:71], a[36:39], a[84:87], v[68:71]\n"
"ds_read_b64_tr_b16 a[2:3], v197 offset:21568\n"
"v_mfma_f32_16x16x32_bf16 v[72:75], a[36:39], a[108:111], v[72:75]\n"
"ds_write_b128 v253, v[132:135] offset:13312\n"
"ds_read_b64_tr_b16 a[48:49], v197 offset:22528\n"
"v_mfma_f32_16x16x32_bf16 v[76:79], a[36:39], a[100:103], v[76:79]\n"
"v_mfma_f32_16x16x32_bf16 v[60:63], a[12:15], a[212:215], v[60:63]\n"
"ds_write_b128 v253, v[136:139] offset:14336\n"
"ds_read_b64_tr_b16 a[50:51], v197 offset:22592\n"
"v_mfma_f32_16x16x32_bf16 v[64:67], a[12:15], a[208:211], v[64:67]\n"
"v_mfma_f32_16x16x32_bf16 v[68:71], a[12:15], a[216:219], v[68:71]\n"
"ds_read_b64_tr_b16 a[36:37], v197 offset:23552\n"
"v_mfma_f32_16x16x32_bf16 v[72:75], a[12:15], a[204:207], v[72:75]\n"
"v_mfma_f32_16x16x32_bf16 v[76:79], a[12:15], a[220:223], v[76:79]\n"
"ds_read_b64_tr_b16 a[38:39], v197 offset:23616\n"
"v_mfma_f32_16x16x32_bf16 v[60:63], a[16:19], a[224:227], v[60:63]\n"
"v_mfma_f32_16x16x32_bf16 v[64:67], a[16:19], a[228:231], v[64:67]\n"
"v_mfma_f32_16x16x32_bf16 v[68:71], a[16:19], a[232:235], v[68:71]\n"
"v_mfma_f32_16x16x32_bf16 v[72:75], a[16:19], a[64:67], v[72:75]\n"
"v_mfma_f32_16x16x32_bf16 v[76:79], a[16:19], a[52:55], v[76:79]\n"
"s_waitcnt lgkmcnt(0)\n"
"v_mfma_f32_16x16x32_bf16 v[20:23], a[24:27], a[172:175], v[20:23]\n"
"s_barrier\n"
"ds_read_b128 v[120:123], v192\n"
"ds_read_b128 v[124:127], v192 offset:1024\n"
"ds_read_b128 v[128:131], v192 offset:2048\n"
"v_mfma_f32_16x16x32_bf16 v[24:27], a[24:27], a[180:183], v[24:27]\n"
"ds_read_b64_tr_b16 a[44:45], v197 offset:24576\n"
"ds_read_b128 v[132:135], v192 offset:3072\n"
"ds_read_b128 v[136:139], v192 offset:4096\n"
"ds_read_b128 v[140:143], v191 offset:5120\n"
"ds_read_b128 v[144:147], v191 offset:6144\n"
"ds_read_b128 v[148:151], v191 offset:7168\n"
"ds_read_b128 v[152:155], v191 offset:8192\n"
"ds_read_b128 v[156:159], v191 offset:9216\n"
"ds_read_b128 v[160:163], v254 offset:10240\n"
"v_mfma_f32_16x16x32_bf16 v[28:31], a[24:27], a[184:187], v[28:31]\n"
"ds_read_b128 v[164:167], v254 offset:11264\n"
"ds_read_b128 v[168:171], v254 offset:12288\n"
"ds_read_b128 v[172:175], v254 offset:13312\n"
"ds_read_b128 v[176:179], v254 offset:14336\n"
"v_mfma_f32_16x16x32_bf16 v[32:35], a[24:27], a[152:155], v[32:35]\n"
"ds_read_b64_tr_b16 a[46:47], v197 offset:24640\n"
"v_mfma_f32_16x16x32_bf16 v[36:39], a[24:27], a[144:147], v[36:39]\n"
"v_mfma_f32_16x16x32_bf16 v[100:103], a[0:3], a[172:175], v[100:103]\n"
"ds_read_b64_tr_b16 a[56:57], v197 offset:25600\n"
"v_mfma_f32_16x16x32_bf16 v[104:107], a[0:3], a[180:183], v[104:107]\n"
"v_mfma_f32_16x16x32_bf16 v[108:111], a[0:3], a[184:187], v[108:111]\n"
"ds_read_b64_tr_b16 a[58:59], v197 offset:25664\n"
"v_mov_b32 v181, s80\n"
"v_mov_b32 v183, s81\n"
"s_waitcnt lgkmcnt(12)\n"
"v_add_f32 v120, v120, v140\n"
"v_add_f32 v121, v121, v141\n"
"v_add_f32 v122, v122, v142\n"
"v_add_f32 v123, v123, v143\n"
"s_waitcnt lgkmcnt(7)\n"
"v_add_f32 v60, v60, v160\n"
"v_add_f32 v61, v61, v161\n"
"v_add_f32 v62, v62, v162\n"
"v_add_f32 v63, v63, v163\n"
"v_mfma_f32_16x16x32_bf16 v[112:115], a[0:3], a[152:155], v[112:115]\n"
"ds_read_b64_tr_b16 a[24:25], v197 offset:26624\n"
"v_add_f32 v60, v60, v120\n"
"v_add_f32 v61, v61, v121\n"
"v_add_f32 v62, v62, v122\n"
"v_add_f32 v63, v63, v123\n"
"v_cvt_pk_bf16_f32 v180, v60, v61\n"
"v_cvt_pk_bf16_f32 v182, v62, v63\n"
"buffer_store_dwordx4 v[180:183], v189, s[48:51], s61 offen sc1\n"
"v_mfma_f32_16x16x32_bf16 v[116:119], a[0:3], a[144:147], v[116:119]\n"
"v_mov_b32 v185, s80\n"
"v_mov_b32 v187, s81\n"
"v_add_f32 v124, v124, v144\n"
"v_add_f32 v125, v125, v145\n"
"v_add_f32 v126, v126, v146\n"
"v_add_f32 v127, v127, v147\n"
"s_waitcnt lgkmcnt(7)\n"
"v_add_f32 v64, v64, v164\n"
"v_add_f32 v65, v65, v165\n"
"v_add_f32 v66, v66, v166\n"
"v_add_f32 v67, v67, v167\n"
"v_add_f32 v64, v64, v124\n"
"v_mfma_f32_16x16x32_bf16 v[80:83], a[48:51], a[172:175], v[80:83]\n"
"ds_read_b64_tr_b16 a[26:27], v197 offset:26688\n"
"v_add_f32 v65, v65, v125\n"
"v_add_f32 v66, v66, v126\n"
"v_add_f32 v67, v67, v127\n"
"v_cvt_pk_bf16_f32 v184, v64, v65\n"
"v_cvt_pk_bf16_f32 v186, v66, v67\n"
"buffer_store_dwordx4 v[184:187], v189, s[48:51], s61 offen offset:1024 sc1\n"
"v_mfma_f32_16x16x32_bf16 v[84:87], a[48:51], a[180:183], v[84:87]\n"
"v_mov_b32 v181, s80\n"
"v_mov_b32 v183, s81\n"
"v_add_f32 v128, v128, v148\n"
"v_add_f32 v129, v129, v149\n"
"v_add_f32 v130, v130, v150\n"
"v_add_f32 v131, v131, v151\n"
"s_waitcnt lgkmcnt(7)\n"
"v_add_f32 v68, v68, v168\n"
"v_add_f32 v69, v69, v169\n"
"v_add_f32 v70, v70, v170\n"
"v_add_f32 v71, v71, v171\n"
"v_add_f32 v68, v68, v128\n"
"v_mfma_f32_16x16x32_bf16 v[88:91], a[48:51], a[184:187], v[88:91]\n"
"ds_read_b64_tr_b16 a[0:1], v197 offset:27648\n"
"v_add_f32 v69, v69, v129\n"
"v_add_f32 v70, v70, v130\n"
"v_add_f32 v71, v71, v131\n"
"v_cvt_pk_bf16_f32 v180, v68, v69\n"
"v_cvt_pk_bf16_f32 v182, v70, v71\n"
"buffer_store_dwordx4 v[180:183], v189, s[48:51], s61 offen offset:2048 sc1\n"
"v_mfma_f32_16x16x32_bf16 v[92:95], a[48:51], a[152:155], v[92:95]\n"
"v_mov_b32 v185, s80\n"
"v_mov_b32 v187, s81\n"
"v_add_f32 v132, v132, v152\n"
"v_add_f32 v133, v133, v153\n"
"v_add_f32 v134, v134, v154\n"
"v_add_f32 v135, v135, v155\n"
"s_waitcnt lgkmcnt(7)\n"
"v_add_f32 v72, v72, v172\n"
"v_add_f32 v73, v73, v173\n"
"v_add_f32 v74, v74, v174\n"
"v_add_f32 v75, v75, v175\n"
"v_add_f32 v72, v72, v132\n"
"v_mfma_f32_16x16x32_bf16 v[96:99], a[48:51], a[144:147], v[96:99]\n"
"ds_read_b64_tr_b16 a[2:3], v197 offset:27712\n"
"v_add_f32 v73, v73, v133\n"
"v_add_f32 v74, v74, v134\n"
"v_add_f32 v75, v75, v135\n"
"v_cvt_pk_bf16_f32 v184, v72, v73\n"
"v_cvt_pk_bf16_f32 v186, v74, v75\n"
"buffer_store_dwordx4 v[184:187], v189, s[48:51], s61 offen offset:3072 sc1\n"
"v_mfma_f32_16x16x32_bf16 v[40:43], a[36:39], a[172:175], v[40:43]\n"
"v_mov_b32 v181, s80\n"
"v_mov_b32 v183, s81\n"
"v_add_f32 v136, v136, v156\n"
"v_add_f32 v137, v137, v157\n"
"v_add_f32 v138, v138, v158\n"
"v_add_f32 v139, v139, v159\n"
"s_waitcnt lgkmcnt(7)\n"
"v_add_f32 v76, v76, v176\n"
"v_add_f32 v77, v77, v177\n"
"v_add_f32 v78, v78, v178\n"
"v_add_f32 v79, v79, v179\n"
"v_add_f32 v76, v76, v136\n"
"v_mfma_f32_16x16x32_bf16 v[44:47], a[36:39], a[180:183], v[44:47]\n"
"v_add_f32 v77, v77, v137\n"
"v_add_f32 v78, v78, v138\n"
"v_add_f32 v79, v79, v139\n"
"v_cvt_pk_bf16_f32 v180, v76, v77\n"
"v_cvt_pk_bf16_f32 v182, v78, v79\n"
"buffer_store_dwordx4 v[180:183], v189, s[48:51], s63 offen sc1\n"
"v_mfma_f32_16x16x32_bf16 v[48:51], a[36:39], a[184:187], v[48:51]\n"
"v_mfma_f32_16x16x32_bf16 v[52:55], a[36:39], a[152:155], v[52:55]\n"
"v_mfma_f32_16x16x32_bf16 v[56:59], a[36:39], a[144:147], v[56:59]\n"
"s_waitcnt lgkmcnt(0)\n"
"v_mfma_f32_16x16x32_bf16 v[20:23], a[44:47], a[192:195], v[20:23]\n"
"v_mfma_f32_16x16x32_bf16 v[24:27], a[44:47], a[196:199], v[24:27]\n"
"ds_read_b64_tr_b16 a[36:37], v197 offset:35840\n"
"v_mfma_f32_16x16x32_bf16 v[28:31], a[44:47], a[164:167], v[28:31]\n"
"v_mfma_f32_16x16x32_bf16 v[32:35], a[44:47], a[136:139], v[32:35]\n"
"ds_read_b64_tr_b16 a[38:39], v197 offset:35904\n"
"v_mfma_f32_16x16x32_bf16 v[36:39], a[44:47], a[156:159], v[36:39]\n"
"v_mfma_f32_16x16x32_bf16 v[100:103], a[56:59], a[192:195], v[100:103]\n"
"ds_read_b64_tr_b16 a[48:49], v197 offset:36864\n"
"v_mfma_f32_16x16x32_bf16 v[104:107], a[56:59], a[196:199], v[104:107]\n"
"v_mfma_f32_16x16x32_bf16 v[108:111], a[56:59], a[164:167], v[108:111]\n"
"ds_read_b64_tr_b16 a[50:51], v197 offset:36928\n"
"v_mfma_f32_16x16x32_bf16 v[112:115], a[56:59], a[136:139], v[112:115]\n"
"ds_read_b64_tr_b16 a[44:45], v197 offset:37888\n"
"v_mfma_f32_16x16x32_bf16 v[116:119], a[56:59], a[156:159], v[116:119]\n"
"v_mfma_f32_16x16x32_bf16 v[80:83], a[24:27], a[192:195], v[80:83]\n"
"ds_read_b64_tr_b16 a[46:47], v197 offset:37952\n"
"v_mfma_f32_16x16x32_bf16 v[84:87], a[24:27], a[196:199], v[84:87]\n"
"v_mfma_f32_16x16x32_bf16 v[88:91], a[24:27], a[164:167], v[88:91]\n"
"ds_read_b64_tr_b16 a[56:57], v197 offset:38912\n"
"v_mfma_f32_16x16x32_bf16 v[92:95], a[24:27], a[136:139], v[92:95]\n"
"v_mfma_f32_16x16x32_bf16 v[96:99], a[24:27], a[156:159], v[96:99]\n"
"ds_read_b64_tr_b16 a[58:59], v197 offset:38976\n"
"v_mfma_f32_16x16x32_bf16 v[40:43], a[0:3], a[192:195], v[40:43]\n"
"v_mfma_f32_16x16x32_bf16 v[44:47], a[0:3], a[196:199], v[44:47]\n"
"v_mfma_f32_16x16x32_bf16 v[48:51], a[0:3], a[164:167], v[48:51]\n"
"v_mfma_f32_16x16x32_bf16 v[52:55], a[0:3], a[136:139], v[52:55]\n"
"v_mfma_f32_16x16x32_bf16 v[56:59], a[0:3], a[156:159], v[56:59]\n"
"s_waitcnt lgkmcnt(0)\n"
"v_mfma_f32_16x16x32_bf16 v[20:23], a[36:39], a[200:203], v[20:23]\n"
"v_mfma_f32_16x16x32_bf16 v[24:27], a[36:39], a[124:127], v[24:27]\n"
"v_mfma_f32_16x16x32_bf16 v[28:31], a[36:39], a[80:83], v[28:31]\n"
"v_mfma_f32_16x16x32_bf16 v[32:35], a[36:39], a[132:135], v[32:35]\n"
"v_mfma_f32_16x16x32_bf16 v[36:39], a[36:39], a[128:131], v[36:39]\n"
"v_mfma_f32_16x16x32_bf16 v[100:103], a[48:51], a[200:203], v[100:103]\n"
"v_mfma_f32_16x16x32_bf16 v[104:107], a[48:51], a[124:127], v[104:107]\n"
"v_mfma_f32_16x16x32_bf16 v[108:111], a[48:51], a[80:83], v[108:111]\n"
"v_mfma_f32_16x16x32_bf16 v[112:115], a[48:51], a[132:135], v[112:115]\n"
"v_mfma_f32_16x16x32_bf16 v[116:119], a[48:51], a[128:131], v[116:119]\n"
"v_mfma_f32_16x16x32_bf16 v[80:83], a[44:47], a[200:203], v[80:83]\n"
"v_mfma_f32_16x16x32_bf16 v[84:87], a[44:47], a[124:127], v[84:87]\n"
"v_mfma_f32_16x16x32_bf16 v[88:91], a[44:47], a[80:83], v[88:91]\n"
"v_mfma_f32_16x16x32_bf16 v[92:95], a[44:47], a[132:135], v[92:95]\n"
"v_mfma_f32_16x16x32_bf16 v[96:99], a[44:47], a[128:131], v[96:99]\n"
"v_mfma_f32_16x16x32_bf16 v[40:43], a[56:59], a[200:203], v[40:43]\n"
"v_mfma_f32_16x16x32_bf16 v[44:47], a[56:59], a[124:127], v[44:47]\n"
"v_mfma_f32_16x16x32_bf16 v[48:51], a[56:59], a[80:83], v[48:51]\n"
"v_mfma_f32_16x16x32_bf16 v[52:55], a[56:59], a[132:135], v[52:55]\n"
"v_mfma_f32_16x16x32_bf16 v[56:59], a[56:59], a[128:131], v[56:59]\n"
"s_waitcnt vmcnt(17)\n"
"v_mfma_f32_16x16x32_bf16 v[100:103], a[28:31], a[76:79], v[100:103]\n"
"v_mfma_f32_16x16x32_bf16 v[104:107], a[28:31], a[104:107], v[104:107]\n"
"v_mfma_f32_16x16x32_bf16 v[108:111], a[28:31], a[148:151], v[108:111]\n"
"v_mfma_f32_16x16x32_bf16 v[112:115], a[28:31], a[40:43], v[112:115]\n"
"v_mfma_f32_16x16x32_bf16 v[116:119], a[28:31], a[88:91], v[116:119]\n"
"s_waitcnt vmcnt(16)\n"
"v_mfma_f32_16x16x32_bf16 v[100:103], a[4:7], a[60:63], v[100:103]\n"
"v_mfma_f32_16x16x32_bf16 v[104:107], a[4:7], a[92:95], v[104:107]\n"
"v_mfma_f32_16x16x32_bf16 v[108:111], a[4:7], a[84:87], v[108:111]\n"
"v_mfma_f32_16x16x32_bf16 v[112:115], a[4:7], a[108:111], v[112:115]\n"
"v_mfma_f32_16x16x32_bf16 v[116:119], a[4:7], a[100:103], v[116:119]\n"
"s_waitcnt vmcnt(15)\n"
"v_mfma_f32_16x16x32_bf16 v[100:103], a[32:35], a[212:215], v[100:103]\n"
"v_mfma_f32_16x16x32_bf16 v[104:107], a[32:35], a[208:211], v[104:107]\n"
"v_mfma_f32_16x16x32_bf16 v[108:111], a[32:35], a[216:219], v[108:111]\n"
"v_mfma_f32_16x16x32_bf16 v[112:115], a[32:35], a[204:207], v[112:115]\n"
"v_mfma_f32_16x16x32_bf16 v[116:119], a[32:35], a[220:223], v[116:119]\n"
"s_waitcnt vmcnt(14)\n"
"v_mfma_f32_16x16x32_bf16 v[100:103], a[72:75], a[224:227], v[100:103]\n"
"v_mfma_f32_16x16x32_bf16 v[104:107], a[72:75], a[228:231], v[104:107]\n"
"v_mfma_f32_16x16x32_bf16 v[108:111], a[72:75], a[232:235], v[108:111]\n"
"v_mfma_f32_16x16x32_bf16 v[112:115], a[72:75], a[64:67], v[112:115]\n"
"v_mfma_f32_16x16x32_bf16 v[116:119], a[72:75], a[52:55], v[116:119]\n"
"s_waitcnt vmcnt(13)\n"
"v_mfma_f32_16x16x32_bf16 v[80:83], a[140:143], a[76:79], v[80:83]\n"
"v_mfma_f32_16x16x32_bf16 v[84:87], a[140:143], a[104:107], v[84:87]\n"
"v_mfma_f32_16x16x32_bf16 v[88:91], a[140:143], a[148:151], v[88:91]\n"
"ds_write_b128 v253, v[100:103] offset:16384\n"
"v_mfma_f32_16x16x32_bf16 v[92:95], a[140:143], a[40:43], v[92:95]\n"
"v_mfma_f32_16x16x32_bf16 v[96:99], a[140:143], a[88:91], v[96:99]\n"
"s_waitcnt vmcnt(12)\n"
"v_mfma_f32_16x16x32_bf16 v[80:83], a[116:119], a[60:63], v[80:83]\n"
"ds_write_b128 v253, v[104:107] offset:17408\n"
"v_mfma_f32_16x16x32_bf16 v[84:87], a[116:119], a[92:95], v[84:87]\n"
"v_mfma_f32_16x16x32_bf16 v[88:91], a[116:119], a[84:87], v[88:91]\n"
"v_mfma_f32_16x16x32_bf16 v[92:95], a[116:119], a[108:111], v[92:95]\n"
"ds_write_b128 v253, v[108:111] offset:18432\n"
"v_mfma_f32_16x16x32_bf16 v[96:99], a[116:119], a[100:103], v[96:99]\n"
"s_waitcnt vmcnt(11)\n"
"v_mfma_f32_16x16x32_bf16 v[80:83], a[168:171], a[212:215], v[80:83]\n"
"v_mfma_f32_16x16x32_bf16 v[84:87], a[168:171], a[208:211], v[84:87]\n"
"ds_write_b128 v253, v[112:115] offset:19456\n"
"v_mfma_f32_16x16x32_bf16 v[88:91], a[168:171], a[216:219], v[88:91]\n"
"v_mfma_f32_16x16x32_bf16 v[92:95], a[168:171], a[204:207], v[92:95]\n"
"v_mfma_f32_16x16x32_bf16 v[96:99], a[168:171], a[220:223], v[96:99]\n"
"ds_write_b128 v253, v[116:119] offset:20480\n"
"s_waitcnt vmcnt(10)\n"
"v_mfma_f32_16x16x32_bf16 v[80:83], a[96:99], a[224:227], v[80:83]\n"
"v_mfma_f32_16x16x32_bf16 v[84:87], a[96:99], a[228:231], v[84:87]\n"
"v_mfma_f32_16x16x32_bf16 v[88:91], a[96:99], a[232:235], v[88:91]\n"
"v_mfma_f32_16x16x32_bf16 v[92:95], a[96:99], a[64:67], v[92:95]\n"
"v_mfma_f32_16x16x32_bf16 v[96:99], a[96:99], a[52:55], v[96:99]\n"
"s_waitcnt vmcnt(9)\n"
"v_mfma_f32_16x16x32_bf16 v[40:43], a[20:23], a[76:79], v[40:43]\n"
"v_mfma_f32_16x16x32_bf16 v[44:47], a[20:23], a[104:107], v[44:47]\n"
"v_mfma_f32_16x16x32_bf16 v[48:51], a[20:23], a[148:151], v[48:51]\n"
"ds_write_b128 v253, v[80:83] offset:21504\n"
"v_mfma_f32_16x16x32_bf16 v[52:55], a[20:23], a[40:43], v[52:55]\n"
"v_mfma_f32_16x16x32_bf16 v[56:59], a[20:23], a[88:91], v[56:59]\n"
"s_waitcnt vmcnt(8)\n"
"v_mfma_f32_16x16x32_bf16 v[40:43], a[112:115], a[60:63], v[40:43]\n"
"ds_write_b128 v253, v[84:87] offset:22528\n"
"v_mfma_f32_16x16x32_bf16 v[44:47], a[112:115], a[92:95], v[44:47]\n"
"v_mfma_f32_16x16x32_bf16 v[48:51], a[112:115], a[84:87], v[48:51]\n"
"v_mfma_f32_16x16x32_bf16 v[52:55], a[112:115], a[108:111], v[52:55]\n"
"ds_write_b128 v253, v[88:91] offset:23552\n"
"v_mfma_f32_16x16x32_bf16 v[56:59], a[112:115], a[100:103], v[56:59]\n"
"s_waitcnt vmcnt(7)\n"
"v_mfma_f32_16x16x32_bf16 v[40:43], a[160:163], a[212:215], v[40:43]\n"
"v_mfma_f32_16x16x32_bf16 v[44:47], a[160:163], a[208:211], v[44:47]\n"
"ds_write_b128 v253, v[92:95] offset:24576\n"
"v_mfma_f32_16x16x32_bf16 v[48:51], a[160:163], a[216:219], v[48:51]\n"
"v_mfma_f32_16x16x32_bf16 v[52:55], a[160:163], a[204:207], v[52:55]\n"
"v_mfma_f32_16x16x32_bf16 v[56:59], a[160:163], a[220:223], v[56:59]\n"
"ds_write_b128 v253, v[96:99] offset:25600\n"
"s_waitcnt vmcnt(6)\n"
"v_mfma_f32_16x16x32_bf16 v[40:43], a[68:71], a[224:227], v[40:43]\n"
"v_mfma_f32_16x16x32_bf16 v[44:47], a[68:71], a[228:231], v[44:47]\n"
"v_mfma_f32_16x16x32_bf16 v[48:51], a[68:71], a[232:235], v[48:51]\n"
"v_mfma_f32_16x16x32_bf16 v[52:55], a[68:71], a[64:67], v[52:55]\n"
"v_mfma_f32_16x16x32_bf16 v[56:59], a[68:71], a[52:55], v[56:59]\n"
"s_waitcnt vmcnt(5)\n"
"v_mfma_f32_16x16x32_bf16 v[20:23], a[188:191], a[76:79], v[20:23]\n"
"v_mfma_f32_16x16x32_bf16 v[24:27], a[188:191], a[104:107], v[24:27]\n"
"v_mfma_f32_16x16x32_bf16 v[28:31], a[188:191], a[148:151], v[28:31]\n"
"ds_write_b128 v253, v[40:43] offset:26624\n"
"v_mfma_f32_16x16x32_bf16 v[32:35], a[188:191], a[40:43], v[32:35]\n"
"v_mfma_f32_16x16x32_bf16 v[36:39], a[188:191], a[88:91], v[36:39]\n"
"ds_write_b128 v253, v[44:47] offset:27648\n"
"v_mfma_f32_16x16x32_bf16 v[20:23], a[120:123], a[60:63], v[20:23]\n"
"v_mfma_f32_16x16x32_bf16 v[24:27], a[120:123], a[92:95], v[24:27]\n"
"ds_write_b128 v253, v[48:51] offset:28672\n"
"v_mfma_f32_16x16x32_bf16 v[28:31], a[120:123], a[84:87], v[28:31]\n"
"v_mfma_f32_16x16x32_bf16 v[32:35], a[120:123], a[108:111], v[32:35]\n"
"ds_write_b128 v253, v[52:55] offset:29696\n"
"buffer_load_dwordx4 v[128:131], v189, s[48:51], s60 offen sc1\n"
"v_mfma_f32_16x16x32_bf16 v[36:39], a[120:123], a[100:103], v[36:39]\n"
"v_mfma_f32_16x16x32_bf16 v[20:23], a[176:179], a[212:215], v[20:23]\n"
"ds_write_b128 v253, v[56:59] offset:30720\n"
"buffer_load_dwordx4 v[124:127], v189, s[48:51], s60 offen offset:1024 sc1\n"
"v_mfma_f32_16x16x32_bf16 v[24:27], a[176:179], a[208:211], v[24:27]\n"
"v_mfma_f32_16x16x32_bf16 v[28:31], a[176:179], a[216:219], v[28:31]\n"
"v_mfma_f32_16x16x32_bf16 v[32:35], a[176:179], a[204:207], v[32:35]\n"
"buffer_load_dwordx4 v[76:79], v189, s[48:51], s60 offen offset:2048 sc1\n"
"v_mfma_f32_16x16x32_bf16 v[36:39], a[176:179], a[220:223], v[36:39]\n"
"s_waitcnt lgkmcnt(0)\n"
"s_barrier\n"
"v_mfma_f32_16x16x32_bf16 v[20:23], a[236:239], a[224:227], v[20:23]\n"
"ds_read_b128 v[40:43], v192 offset:16384\n"
"ds_read_b128 v[44:47], v192 offset:17408\n"
"ds_read_b128 v[48:51], v192 offset:18432\n"
"buffer_load_dwordx4 v[72:75], v189, s[48:51], s60 offen offset:3072 sc1\n"
"v_mfma_f32_16x16x32_bf16 v[24:27], a[236:239], a[228:231], v[24:27]\n"
"ds_read_b128 v[52:55], v192 offset:19456\n"
"ds_read_b128 v[56:59], v192 offset:20480\n"
"ds_read_b128 v[100:103], v191 offset:21504\n"
"v_mfma_f32_16x16x32_bf16 v[28:31], a[236:239], a[232:235], v[28:31]\n"
"ds_read_b128 v[104:107], v191 offset:22528\n"
"ds_read_b128 v[108:111], v191 offset:23552\n"
"ds_read_b128 v[112:115], v191 offset:24576\n"
"v_mfma_f32_16x16x32_bf16 v[32:35], a[236:239], a[64:67], v[32:35]\n"
"ds_read_b128 v[116:119], v191 offset:25600\n"
"ds_read_b128 v[80:83], v254 offset:26624\n"
"ds_read_b128 v[84:87], v254 offset:27648\n"
"buffer_load_dwordx4 v[120:123], v189, s[48:51], s62 offen sc1\n"
"v_mfma_f32_16x16x32_bf16 v[36:39], a[236:239], a[52:55], v[36:39]\n"
"ds_read_b128 v[88:91], v254 offset:28672\n"
"ds_read_b128 v[92:95], v254 offset:29696\n"
"ds_read_b128 v[96:99], v254 offset:30720\n"
"s_or_b32 s84, %1, s76\n"
"s_cbranch_scc1 NOUPD1_%=\n"
"v_lshlrev_b32 v68, 11, v255\n"
"v_add_u32 v68, 131072, v68\n"
"buffer_load_dwordx2 v[60:61], v68, s[48:51], 0 offen offset:0 sc1\n"
"buffer_load_dwordx2 v[62:63], v68, s[48:51], 0 offen offset:512 sc1\n"
"buffer_load_dwordx2 v[64:65], v68, s[48:51], 0 offen offset:1024 sc1\n"
"buffer_load_dwordx2 v[66:67], v68, s[48:51], 0 offen offset:1536 sc1\n"
"NOUPD1_%=:\n"
"s_waitcnt lgkmcnt(9)\n"
"v_pk_add_f32 v[40:41], v[40:41], v[100:101]\n"
"v_pk_add_f32 v[42:43], v[42:43], v[102:103]\n"
"s_waitcnt lgkmcnt(8)\n"
"v_pk_add_f32 v[44:45], v[44:45], v[104:105]\n"
"v_pk_add_f32 v[46:47], v[46:47], v[106:107]\n"
"s_waitcnt lgkmcnt(7)\n"
"v_pk_add_f32 v[48:49], v[48:49], v[108:109]\n"
"v_pk_add_f32 v[50:51], v[50:51], v[110:111]\n"
"s_waitcnt lgkmcnt(6)\n"
"v_pk_add_f32 v[52:53], v[52:53], v[112:113]\n"
"v_pk_add_f32 v[54:55], v[54:55], v[114:115]\n"
"s_waitcnt lgkmcnt(5)\n"
"v_pk_add_f32 v[56:57], v[56:57], v[116:117]\n"
"v_pk_add_f32 v[58:59], v[58:59], v[118:119]\n"
"s_waitcnt lgkmcnt(4)\n"
"v_pk_add_f32 v[0:1], v[0:1], v[80:81]\n"
"v_pk_add_f32 v[2:3], v[2:3], v[82:83]\n"
"s_waitcnt lgkmcnt(3)\n"
"v_pk_add_f32 v[4:5], v[4:5], v[84:85]\n"
"v_pk_add_f32 v[6:7], v[6:7], v[86:87]\n"
"s_waitcnt lgkmcnt(2)\n"
"v_pk_add_f32 v[8:9], v[8:9], v[88:89]\n"
"v_pk_add_f32 v[10:11], v[10:11], v[90:91]\n"
"s_waitcnt lgkmcnt(1)\n"
"v_pk_add_f32 v[12:13], v[12:13], v[92:93]\n"
"v_pk_add_f32 v[14:15], v[14:15], v[94:95]\n"
"s_waitcnt lgkmcnt(0)\n"
"v_pk_add_f32 v[16:17], v[16:17], v[96:97]\n"
"v_pk_add_f32 v[18:19], v[18:19], v[98:99]\n"
"v_pk_add_f32 v[20:21], v[20:21], v[40:41]\n"
"v_pk_add_f32 v[22:23], v[22:23], v[42:43]\n"
"v_pk_add_f32 v[24:25], v[24:25], v[44:45]\n"
"v_pk_add_f32 v[26:27], v[26:27], v[46:47]\n"
"v_pk_add_f32 v[28:29], v[28:29], v[48:49]\n"
"v_pk_add_f32 v[30:31], v[30:31], v[50:51]\n"
"v_pk_add_f32 v[32:33], v[32:33], v[52:53]\n"
"v_pk_add_f32 v[34:35], v[34:35], v[54:55]\n"
"v_pk_add_f32 v[36:37], v[36:37], v[56:57]\n"
"v_pk_add_f32 v[38:39], v[38:39], v[58:59]\n"
"v_pk_add_f32 v[20:21], v[20:21], v[0:1]\n"
"v_pk_add_f32 v[22:23], v[22:23], v[2:3]\n"
"v_pk_add_f32 v[24:25], v[24:25], v[4:5]\n"
"v_pk_add_f32 v[26:27], v[26:27], v[6:7]\n"
"v_pk_add_f32 v[28:29], v[28:29], v[8:9]\n"
"v_pk_add_f32 v[30:31], v[30:31], v[10:11]\n"
"v_pk_add_f32 v[32:33], v[32:33], v[12:13]\n"
"v_pk_add_f32 v[34:35], v[34:35], v[14:15]\n"
"v_pk_add_f32 v[36:37], v[36:37], v[16:17]\n"
"v_pk_add_f32 v[38:39], v[38:39], v[18:19]\n"
"s_waitcnt vmcnt(4)\n"
"v_xor_b32 v0, s80, v129\n"
"v_bitop3_b32 v0, v131, s81, v0 bitop3:0xbe\n"
"s_waitcnt vmcnt(3)\n"
"v_bitop3_b32 v0, v125, s80, v0 bitop3:0xbe\n"
"v_bitop3_b32 v0, v127, s81, v0 bitop3:0xbe\n"
"s_waitcnt vmcnt(2)\n"
"v_bitop3_b32 v0, v77, s80, v0 bitop3:0xbe\n"
"v_bitop3_b32 v0, v79, s81, v0 bitop3:0xbe\n"
"s_waitcnt vmcnt(1)\n"
"v_bitop3_b32 v0, v73, s80, v0 bitop3:0xbe\n"
"v_bitop3_b32 v0, v75, s81, v0 bitop3:0xbe\n"
"s_waitcnt vmcnt(0)\n"
"v_bitop3_b32 v0, v121, s80, v0 bitop3:0xbe\n"
"v_bitop3_b32 v0, v123, s81, v0 bitop3:0xbe\n"
"v_cmp_ne_u32 vcc, 0, v0\n"
"s_nop 1\n"
"s_cmp_lg_u64 vcc, 0\n"
"s_cbranch_scc1 SLOWAY_%=\n"
"BACKAY_%=:\n"
"v_and_b32 v129, s82, v128\n"
"v_lshlrev_b32 v128, 16, v128\n"
"v_pk_add_f32 v[20:21], v[20:21], v[128:129]\n"
"v_and_b32 v131, s82, v130\n"
"v_lshlrev_b32 v130, 16, v130\n"
"v_pk_add_f32 v[22:23], v[22:23], v[130:131]\n"
"buffer_store_dword v20, v252, s[44:47], 0 offen sc1\n"
"buffer_store_dword v21, v252, s[44:47], 0 offen offset:2560 sc1\n"
"v_and_b32 v125, s82, v124\n"
"v_lshlrev_b32 v124, 16, v124\n"
"v_pk_add_f32 v[24:25], v[24:25], v[124:125]\n"
"buffer_store_dword v22, v252, s[44:47], s88 offen sc1\n"
"buffer_store_dword v23, v252, s[44:47], s88 offen offset:2560 sc1\n"
"v_and_b32 v127, s82, v126\n"
"v_lshlrev_b32 v126, 16, v126\n"
"v_pk_add_f32 v[26:27], v[26:27], v[126:127]\n"
"buffer_store_dword v24, v252, s[44:47], 0 offen offset:64 sc1\n"
"buffer_store_dword v25, v252, s[44:47], 0 offen offset:2624 sc1\n"
"v_and_b32 v77, s82, v76\n"
"v_lshlrev_b32 v76, 16, v76\n"
"v_pk_add_f32 v[28:29], v[28:29], v[76:77]\n"
"buffer_store_dword v26, v252, s[44:47], s88 offen offset:64 sc1\n"
"buffer_store_dword v27, v252, s[44:47], s88 offen offset:2624 sc1\n"
"v_and_b32 v79, s82, v78\n"
"v_lshlrev_b32 v78, 16, v78\n"
"v_pk_add_f32 v[30:31], v[30:31], v[78:79]\n"
"buffer_store_dword v28, v252, s[44:47], 0 offen offset:128 sc1\n"
"buffer_store_dword v29, v252, s[44:47], 0 offen offset:2688 sc1\n"
"v_and_b32 v73, s82, v72\n"
"v_lshlrev_b32 v72, 16, v72\n"
"v_pk_add_f32 v[32:33], v[32:33], v[72:73]\n"
"buffer_store_dword v30, v252, s[44:47], s88 offen offset:128 sc1\n"
"buffer_store_dword v31, v252, s[44:47], s88 offen offset:2688 sc1\n"
"v_and_b32 v75, s82, v74\n"
"v_lshlrev_b32 v74, 16, v74\n"
"v_pk_add_f32 v[34:35], v[34:35], v[74:75]\n"
"buffer_store_dword v32, v252, s[44:47], 0 offen offset:192 sc1\n"
"buffer_store_dword v33, v252, s[44:47], 0 offen offset:2752 sc1\n"
"v_and_b32 v121, s82, v120\n"
"v_lshlrev_b32 v120, 16, v120\n"
"v_pk_add_f32 v[36:37], v[36:37], v[120:121]\n"
"buffer_store_dword v34, v252, s[44:47], s88 offen offset:192 sc1\n"
"buffer_store_dword v35, v252, s[44:47], s88 offen offset:2752 sc1\n"
"v_and_b32 v123, s82, v122\n"
"v_lshlrev_b32 v122, 16, v122\n"
"v_pk_add_f32 v[38:39], v[38:39], v[122:123]\n"
"buffer_store_dword v36, v252, s[44:47], 0 offen offset:256 sc1\n"
"buffer_store_dword v37, v252, s[44:47], 0 offen offset:2816 sc1\n"
"buffer_store_dword v38, v252, s[44:47], s88 offen offset:256 sc1\n"
"buffer_store_dword v39, v252, s[44:47], s88 offen offset:2816 sc1\n"
"s_or_b32 s84, %1, s76\n"
"s_cbranch_scc1 NOUPD2_%=\n"
"s_waitcnt vmcnt(20)\n"
"UPDCHK_%=:\n"
"v_subrev_u32 v60, s16, v60\n"
"v_xor_b32 v61, s81, v61\n"
"v_lshrrev_b32 v60, 1, v60\n"
"v_or_b32 v60, v60, v61\n"
"v_subrev_u32 v62, s16, v62\n"
"v_xor_b32 v63, s81, v63\n"
"v_lshrrev_b32 v62, 1, v62\n"
"v_or_b32 v62, v62, v63\n"
"v_subrev_u32 v64, s16, v64\n"
"v_xor_b32 v65, s81, v65\n"
"v_lshrrev_b32 v64, 1, v64\n"
"v_or_b32 v64, v64, v65\n"
"v_subrev_u32 v66, s16, v66\n"
"v_xor_b32 v67, s81, v67\n"
"v_lshrrev_b32 v66, 1, v66\n"
"v_or_b32 v66, v66, v67\n"
"v_or3_b32 v60, v60, v62, v64\n"
"v_or_b32 v60, v60, v66\n"
"v_cmp_ne_u32 vcc, 0, v60\n"
"s_nop 1\n"
"s_cbranch_vccz UPDOK_%=\n"
"s_add_u32 s17, s17, 1\n"
"s_cmp_gt_u32 s17, 100000\n"
"s_cbranch_scc1 UPDOK_%=\n"
"s_sleep 4\n"
"buffer_load_dwordx2 v[60:61], v68, s[48:51], 0 offen offset:0 sc1\n"
"buffer_load_dwordx2 v[62:63], v68, s[48:51], 0 offen offset:512 sc1\n"
"buffer_load_dwordx2 v[64:65], v68, s[48:51], 0 offen offset:1024 sc1\n"
"buffer_load_dwordx2 v[66:67], v68, s[48:51], 0 offen offset:1536 sc1\n"
"s_waitcnt vmcnt(0)\n"
"s_branch UPDCHK_%=\n"
"UPDOK_%=:\n"
"v_mov_b32 v60, s80\n"
"v_mov_b32 v61, s81\n"
"s_mov_b64 exec, 1\n"
"buffer_store_dwordx2 v[60:61], off, s[48:51], 0 sc1\n"
"s_mov_b64 exec, -1\n"
"NOUPD2_%=:\n"
"s_branch KEND_%=\n"
"SLOWAS_%=:\n"
"s_nop 7\n"
"s_nop 7\n"
"s_add_u32 s17, s17, 1\n"
"s_cmp_gt_u32 s17, 100000\n"
"s_cbranch_scc1 BACKAS_%=\n"
"s_sleep 4\n"
"buffer_load_dwordx4 v[176:179], v157, s[48:51], 0 offen sc1\n"
"s_waitcnt vmcnt(0)\n"
"v_xor_b32 v41, s80, v177\n"
"v_bitop3_b32 v41, v179, s81, v41 bitop3:0xbe\n"
"v_cmp_ne_u32 vcc, 0, v41\n"
"s_nop 1\n"
"s_cmp_lg_u64 vcc, 0\n"
"s_cbranch_scc1 SLOWAS_%=\n"
"s_branch BACKAS_%=\n"
"SLOWAX0_%=:\n"
"s_nop 7\n"
"s_nop 7\n"
"s_add_u32 s17, s17, 1\n"
"s_cmp_gt_u32 s17, 100000\n"
"s_cbranch_scc1 BACKAX0_%=\n"
"s_sleep 4\n"
"buffer_load_dwordx4 v[20:23], v16, s[48:51], 0 offen sc1\n"
"buffer_load_dwordx4 v[24:27], v16, s[48:51], 0 offen offset:1024 sc1\n"
"buffer_load_dwordx4 v[28:31], v16, s[48:51], 0 offen offset:2048 sc1\n"
"buffer_load_dwordx4 v[120:123], v16, s[48:51], 0 offen offset:3072 sc1\n"
"s_waitcnt vmcnt(0)\n"
"v_xor_b32 v128, s8, v21\n"
"v_bitop3_b32 v128, v23, s9, v128 bitop3:0xbe\n"
"v_bitop3_b32 v128, v25, s8, v128 bitop3:0xbe\n"
"v_bitop3_b32 v128, v27, s9, v128 bitop3:0xbe\n"
"v_bitop3_b32 v128, v29, s8, v128 bitop3:0xbe\n"
"v_bitop3_b32 v128, v31, s9, v128 bitop3:0xbe\n"
"v_bitop3_b32 v128, v121, s8, v128 bitop3:0xbe\n"
"v_bitop3_b32 v128, v123, s9, v128 bitop3:0xbe\n"
"v_cmp_ne_u32 vcc, 0, v128\n"
"s_nop 1\n"
"s_cmp_lg_u64 vcc, 0\n"
"s_cbranch_scc1 SLOWAX0_%=\n"
"s_branch BACKAX0_%=\n"
"SLOWAX1_%=:\n"
"s_nop 7\n"
"s_nop 7\n"
"s_add_u32 s17, s17, 1\n"
"s_cmp_gt_u32 s17, 100000\n"
"s_cbranch_scc1 BACKAX1_%=\n"
"s_sleep 4\n"
"buffer_load_dwordx4 v[140:143], v16, s[48:51], s90 offen sc1\n"
"buffer_load_dwordx4 v[144:147], v16, s[48:51], s90 offen offset:1024 sc1\n"
"buffer_load_dwordx4 v[148:151], v16, s[48:51], s90 offen offset:2048 sc1\n"
"buffer_load_dwordx4 v[152:155], v16, s[48:51], s90 offen offset:3072 sc1\n"
"s_waitcnt vmcnt(0)\n"
"v_xor_b32 v104, s8, v141\n"
"v_bitop3_b32 v104, v143, s9, v104 bitop3:0xbe\n"
"v_bitop3_b32 v104, v145, s8, v104 bitop3:0xbe\n"
"v_bitop3_b32 v104, v147, s9, v104 bitop3:0xbe\n"
"v_bitop3_b32 v104, v149, s8, v104 bitop3:0xbe\n"
"v_bitop3_b32 v104, v151, s9, v104 bitop3:0xbe\n"
"v_bitop3_b32 v104, v153, s8, v104 bitop3:0xbe\n"
"v_bitop3_b32 v104, v155, s9, v104 bitop3:0xbe\n"
"v_cmp_ne_u32 vcc, 0, v104\n"
"s_nop 1\n"
"s_cmp_lg_u64 vcc, 0\n"
"s_cbranch_scc1 SLOWAX1_%=\n"
"s_branch BACKAX1_%=\n"
"SLOWAJW_%=:\n"
"s_nop 7\n"
"s_nop 7\n"
"s_add_u32 s17, s17, 1\n"
"s_cmp_gt_u32 s17, 100000\n"
"s_cbranch_scc1 BACKAJW_%=\n"
"s_sleep 4\n"
"buffer_load_dwordx2 v[104:105], v42, s[48:51], 0 offen sc1\n"
"s_waitcnt vmcnt(0)\n"
"v_xor_b32 v180, s80, v104\n"
"v_bitop3_b32 v180, v105, s81, v180 bitop3:0xbe\n"
"v_cmp_ne_u32 vcc, 0, v180\n"
"s_nop 1\n"
"s_cmp_lg_u64 vcc, 0\n"
"s_cbranch_scc1 SLOWAJW_%=\n"
"s_branch BACKAJW_%=\n"
"SLOWAJC_%=:\n"
"s_nop 7\n"
"s_nop 7\n"
"s_add_u32 s17, s17, 1\n"
"s_cmp_gt_u32 s17, 100000\n"
"s_cbranch_scc1 BACKAJC_%=\n"
"s_sleep 4\n"
"buffer_load_dwordx2 v[40:41], v193, s[48:51], 0 offen sc1\n"
"s_waitcnt vmcnt(0)\n"
"v_xor_b32 v252, s80, v40\n"
"v_bitop3_b32 v252, v41, s81, v252 bitop3:0xbe\n"
"v_cmp_ne_u32 vcc, 0, v252\n"
"s_nop 1\n"
"s_cmp_lg_u64 vcc, 0\n"
"s_cbranch_scc1 SLOWAJC_%=\n"
"s_branch BACKAJC_%=\n"
"SLOWAH0_%=:\n"
"s_nop 7\n"
"s_nop 7\n"
"s_add_u32 s17, s17, 1\n"
"s_cmp_gt_u32 s17, 100000\n"
"s_cbranch_scc1 BACKAH0_%=\n"
"s_sleep 4\n"
"buffer_load_dwordx2 v[104:105], v190, s[48:51], 0 offen sc1\n"
"s_waitcnt vmcnt(0)\n"
"v_subrev_u32 v144, s16, v104\n"
"v_lshrrev_b32 v144, 1, v144\n"
"v_bitop3_b32 v144, v105, s81, v144 bitop3:0xbe\n"
"v_cmp_ne_u32 vcc, 0, v144\n"
"s_nop 1\n"
"s_cmp_lg_u32 vcc_lo, 0\n"
"s_cbranch_scc1 SLOWAH0_%=\n"
"s_branch BACKAH0_%=\n"
"SLOWAH3_%=:\n"
"s_nop 7\n"
"s_nop 7\n"
"s_add_u32 s17, s17, 1\n"
"s_cmp_gt_u32 s17, 100000\n"
"s_cbranch_scc1 BACKAH3_%=\n"
"s_sleep 4\n"
"buffer_load_dwordx2 v[104:105], v190, s[48:51], 0 offen sc1\n"
"s_waitcnt vmcnt(0)\n"
"v_subrev_u32 v100, s16, v104\n"
"v_lshrrev_b32 v100, 1, v100\n"
"v_bitop3_b32 v100, v105, s81, v100 bitop3:0xbe\n"
"v_cmp_ne_u32 vcc, 0, v100\n"
"s_nop 1\n"
"s_cmp_lg_u32 vcc_hi, 0\n"
"s_cbranch_scc1 SLOWAH3_%=\n"
"s_branch BACKAH3_%=\n"
"SLOWAH1_%=:\n"
"s_nop 7\n"
"s_nop 7\n"
"s_add_u32 s17, s17, 1\n"
"s_cmp_gt_u32 s17, 100000\n"
"s_cbranch_scc1 BACKAH1_%=\n"
"s_sleep 4\n"
"buffer_load_dwordx2 v[180:181], v190, s[48:51], 0 offen sc1\n"
"s_waitcnt vmcnt(0)\n"
"v_subrev_u32 v182, s16, v180\n"
"v_xor_b32 v182, 1, v182\n"
"v_bitop3_b32 v182, v181, s81, v182 bitop3:0xbe\n"
"v_cmp_ne_u32 vcc, 0, v182\n"
"s_nop 1\n"
"s_cmp_lg_u64 vcc, 0\n"
"s_cbranch_scc1 SLOWAH1_%=\n"
"s_branch BACKAH1_%=\n"
"SLOWAY_%=:\n"
"s_nop 7\n"
"s_nop 7\n"
"s_add_u32 s17, s17, 1\n"
"s_cmp_gt_u32 s17, 100000\n"
"s_cbranch_scc1 BACKAY_%=\n"
"s_sleep 4\n"
"buffer_load_dwordx4 v[128:131], v189, s[48:51], s60 offen sc1\n"
"buffer_load_dwordx4 v[124:127], v189, s[48:51], s60 offen offset:1024 sc1\n"
"buffer_load_dwordx4 v[76:79], v189, s[48:51], s60 offen offset:2048 sc1\n"
"buffer_load_dwordx4 v[72:75], v189, s[48:51], s60 offen offset:3072 sc1\n"
"buffer_load_dwordx4 v[120:123], v189, s[48:51], s62 offen sc1\n"
"s_waitcnt vmcnt(0)\n"
"v_xor_b32 v0, s80, v129\n"
"v_bitop3_b32 v0, v131, s81, v0 bitop3:0xbe\n"
"v_bitop3_b32 v0, v125, s80, v0 bitop3:0xbe\n"
"v_bitop3_b32 v0, v127, s81, v0 bitop3:0xbe\n"
"v_bitop3_b32 v0, v77, s80, v0 bitop3:0xbe\n"
"v_bitop3_b32 v0, v79, s81, v0 bitop3:0xbe\n"
"v_bitop3_b32 v0, v73, s80, v0 bitop3:0xbe\n"
"v_bitop3_b32 v0, v75, s81, v0 bitop3:0xbe\n"
"v_bitop3_b32 v0, v121, s80, v0 bitop3:0xbe\n"
"v_bitop3_b32 v0, v123, s81, v0 bitop3:0xbe\n"
"v_cmp_ne_u32 vcc, 0, v0\n"
"s_nop 1\n"
"s_cmp_lg_u64 vcc, 0\n"
"s_cbranch_scc1 SLOWAY_%=\n"
"s_branch BACKAY_%=\n"
"TYPEB_%=:\n"
"s_cmp_lt_u32 s76, 2\n"
"s_cselect_b32 s44, s26, s28\n"
"s_cselect_b32 s45, s27, s29\n"
"v_lshrrev_b32 v225, 3, v255\n"
"v_and_b32 v22, 7, v255\n"
"s_and_b32 s84, s76, 1\n"
"s_lshl_b32 s85, s84, 3\n"
"s_mov_b32 s40, s14\n"
"s_and_b32 s41, s15, 0xffff\n"
"s_mov_b32 s42, 0xc8000\n"
"s_mov_b32 s43, 0x20000\n"
"s_mov_b32 s52, s16\n"
"s_and_b32 s53, s17, 0xffff\n"
"s_mov_b32 s54, 0xc8000\n"
"s_mov_b32 s55, 0x20000\n"
"s_mul_i32 s86, s78, 80\n"
"v_add_u32 v219, s86, v225\n"
"v_mul_u32_u24 v219, 1280, v219\n"
"v_lshrrev_b32 v136, 1, v225\n"
"s_lshl_b32 s86, s84, 2\n"
"v_or_b32 v136, s86, v136\n"
"v_xor_b32 v136, v136, v22\n"
"v_lshl_add_u32 v219, v136, 4, v219\n"
"s_mul_i32 s86, s76, 10240\n"
"v_add_u32 v80, s86, v219\n"
"v_add_u32 v82, 38912, v80\n"
"s_sub_u32 s52, s52, 1024\n"
"s_subb_u32 s53, s53, 0\n"
"s_add_u32 s54, s54, 1024\n"
"s_mul_i32 s86, s84, 10240\n"
"s_lshr_b32 s87, s76, 1\n"
"s_lshl_b32 s87, s87, 10\n"
"s_add_u32 s86, s86, s87\n"
"s_add_u32 s86, s86, 81920\n"
"s_add_u32 m0, s79, 0\n"
"v_add_u32 v84, s86, v219\n"
"buffer_load_dwordx4 v80, s[40:43], s56 offen lds\n"
"buffer_load_dwordx4 v80, s[52:55], s56 offen offset:1024 lds\n"
"v_and_b32 v231, 15, v255\n"
"v_and_b32 v56, 31, v255\n"
"s_mul_i32 s86, s78, 160\n"
"v_lshlrev_b32 v157, 1, v56\n"
"v_add_u32 v157, s86, v157\n"
"v_lshlrev_b32 v180, 1, v231\n"
"v_add_u32 v180, s86, v180\n"
"s_lshl_b32 s87, s76, 5\n"
"v_add_u32 v223, s87, v180\n"
"s_and_b32 s45, s45, 0xffff\n"
"s_mov_b32 s46, 1966080\n"
"s_mov_b32 s47, 0x20000\n"
"s_mul_i32 s66, s56, 1536\n"
"s_mul_i32 s67, s57, 1536\n"
"s_mul_i32 s68, s58, 1536\n"
"s_mul_i32 s69, s59, 1536\n"
"s_mul_i32 s70, s60, 1536\n"
"buffer_load_dwordx4 v82, s[40:43], s56 offen offset:2048 lds\n"
"s_mul_i32 s71, s61, 1536\n"
"s_mul_i32 s72, s62, 1536\n"
"s_mul_i32 s73, s63, 1536\n"
"s_mul_i32 s74, s64, 1536\n"
"s_mul_i32 s75, s65, 1536\n"
"v_add_u32 v219, s85, v225\n"
"v_mul_u32_u24 v219, 3072, v219\n"
"v_and_b32 v136, 2, v225\n"
"s_and_b32 s86, s76, 2\n"
"s_lshl_b32 s86, s86, 1\n"
"v_or_b32 v136, s86, v136\n"
"v_xor_b32 v136, v136, v22\n"
"v_lshl_add_u32 v219, v136, 4, v219\n"
"s_add_u32 s86, s78, 16\n"
"s_lshl_b32 s86, s86, 7\n"
"v_add_u32 v225, s86, v219\n"
"v_add_u32 v226, 48128, v225\n"
"buffer_load_dwordx4 v82, s[52:55], s56 offen offset:3072 lds\n"
"v_add_u32 v227, 96256, v225\n"
"v_add_u32 v228, 144384, v225\n"
"v_lshlrev_b32 v218, 4, v255\n"
"s_lshl_b32 s86, s86, 5\n"
"s_add_u32 s86, s86, 442368\n"
"v_add_u32 v224, s86, v218\n"
"v_accvgpr_write_b32 a3, v224\n"
"s_mov_b32 s82, 0xffff0000\n"
"s_mov_b32 s83, 0x3f803f80\n"
"s_mov_b32 s48, s34\n"
"s_and_b32 s49, s35, 0xffff\n"
"s_mov_b32 s50, 0x1400000\n"
"s_mov_b32 s51, 0x20000\n"
"v_and_b32 v219, 31, v255\n"
"v_lshrrev_b32 v68, 5, v255\n"
"v_bfe_u32 v224, v255, 1, 3\n"
"v_xor_b32 v224, v68, v224\n"
"global_load_ushort v202, v157, s[10:11]\n"
"v_lshlrev_b32 v224, 4, v224\n"
"v_lshl_add_u32 v219, v219, 7, v224\n"
"v_add_u32 v48, s79, v219\n"
"global_load_ushort v203, v157, s[12:13]\n"
"v_xor_b32 v49, 32, v48\n"
"v_xor_b32 v50, 64, v48\n"
"v_xor_b32 v51, 96, v48\n"
"global_load_ushort v204, v157, s[22:23]\n"
"v_bfe_u32 v219, v255, 3, 2\n"
"v_mul_u32_u24 v219, 40960, v219\n"
"global_load_ushort v112, v157, s[10:11] offset:64\n"
"v_lshl_add_u32 v219, v22, 7, v219\n"
"v_add_u32 v22, v219, v224\n"
"v_xor_b32 v23, 32, v22\n"
"global_load_ushort v113, v157, s[12:13] offset:64\n"
"v_xor_b32 v24, 64, v22\n"
"v_xor_b32 v25, 96, v22\n"
"global_load_ushort v114, v157, s[22:23] offset:64\n"
"v_bfe_u32 v219, v255, 4, 1\n"
"v_lshl_add_u32 v136, v219, 1, v68\n"
"v_mul_u32_u24 v136, 40960, v136\n"
"global_load_ushort v210, v180, s[10:11] offset:128\n"
"v_bfe_u32 v224, v255, 2, 2\n"
"v_lshl_add_u32 v136, v224, 7, v136\n"
"global_load_ushort v211, v180, s[12:13] offset:128\n"
"v_lshrrev_b32 v224, 1, v224\n"
"v_lshl_add_u32 v224, v219, 1, v224\n"
"v_lshl_add_u32 v136, v224, 5, v136\n"
"global_load_ushort v212, v180, s[22:23] offset:128\n"
"v_and_b32 v224, 3, v255\n"
"v_lshl_add_u32 v219, v224, 3, v136\n"
"global_load_ushort v136, v223, s[10:11]\n"
"v_xor_b32 v220, 32, v219\n"
"v_xor_b32 v221, 64, v219\n"
"v_xor_b32 v222, 96, v219\n"
"global_load_ushort v137, v223, s[12:13]\n"
"v_lshlrev_b32 v156, 3, v157\n"
"v_add_u32 v156, 40960, v156\n"
"v_lshlrev_b32 v200, 3, v180\n"
"global_load_ushort v138, v223, s[22:23]\n"
"v_bfe_u32 v52, v255, 4, 1\n"
"s_add_u32 m0, s79, 28672\n"
"v_mul_u32_u24 v157, 10240, v52\n"
"buffer_load_dwordx4 v251, s[36:39], s57 offen lds\n"
"v_add_u32 v157, v157, v200\n"
"v_add_u32 v157, 41984, v157\n"
"v_lshlrev_b32 v180, 10, v68\n"
"v_lshl_add_u32 v180, v231, 4, v180\n"
"v_lshl_add_u32 v223, v52, 12, v180\n"
"v_accvgpr_write_b32 a4, v223\n"
"v_lshl_add_u32 v180, v52, 8, v180\n"
"v_accvgpr_write_b32 a2, v180\n"
"v_lshrrev_b32 v52, 4, v255\n"
"v_cvt_f32_u32 v176, v52\n"
"s_sub_u32 s86, s77, 1\n"
"s_mul_i32 s86, s86, 10240\n"
"v_add_u32 v200, s86, v200\n"
"v_mul_u32_u24 v230, 40960, v52\n"
"v_sub_u32 v200, v200, v230\n"
"s_lshl_b32 s87, s76, 8\n"
"v_add_u32 v233, s87, v200\n"
"buffer_load_dwordx4 v252, s[36:39], s57 offen offset:1024 lds\n"
"v_add_u32 v232, 1024, v200\n"
"s_add_u32 s87, s87, 35840\n"
"v_lshl_add_u32 v224, v231, 4, v230\n"
"v_add_u32 v224, s87, v224\n"
"v_lshl_add_u32 v230, v231, 5, v230\n"
"v_add_u32 v230, 36864, v230\n"
"v_lshlrev_b32 v200, 5, v231\n"
"v_lshl_add_u32 v200, v52, 3, v200\n"
"v_add_u32 v231, s79, v200\n"
"v_lshlrev_b32 v200, 4, v56\n"
"v_lshl_add_u32 v200, v68, 3, v200\n"
"v_add_u32 v223, s79, v200\n"
"v_lshlrev_b32 v200, 5, v52\n"
"v_add_u32 v56, s79, v200\n"
"v_lshlrev_b32 v200, 6, v68\n"
"v_add_u32 v52, s79, v200\n"
"s_mov_b32 s90, 10240\n"
"s_waitcnt lgkmcnt(0)\n"
"s_add_u32 s80, s80, 0x9e3779b1\n"
"s_add_u32 s81, s81, 0x85ebca77\n"
"s_add_u32 s16, s80, 1\n"
"s_mov_b32 s17, 0\n"
"s_and_b32 s8, s80, 0xbfffbfff\n"
"s_and_b32 s9, s81, 0xbfffbfff\n"
"v_and_b32 v200, 3, v255\n"
"v_bfe_u32 v180, v255, 2, 1\n"
"v_lshl_add_u32 v200, v180, 4, v200\n"
"v_bfe_u32 v180, v255, 3, 2\n"
"v_lshl_add_u32 v200, v180, 2, v200\n"
"v_lshrrev_b32 v180, 4, v200\n"
"v_lshlrev_b32 v180, 10, v180\n"
"v_and_b32 v53, 15, v200\n"
"v_lshl_add_u32 v53, v68, 4, v53\n"
"v_lshl_add_u32 v68, v53, 4, v180\n"
"v_accvgpr_write_b32 a0, v68\n"
"s_lshl_b32 s86, s77, 2\n"
"s_add_u32 s86, s86, s76\n"
"s_lshl_b32 s86, s86, 9\n"
"s_add_u32 s86, s86, 4096\n"
"v_lshlrev_b32 v180, 4, v200\n"
"v_add_u32 v158, s86, v180\n"
"v_lshlrev_b32 v200, 2, v200\n"
"v_add_u32 v200, s79, v200\n"
"s_lshl_b32 s86, s77, 1\n"
"s_lshr_b32 s87, s76, 1\n"
"s_add_u32 s86, s86, s87\n"
"s_mul_i32 s86, s86, 88\n"
"s_mul_i32 s87, s78, 11\n"
"s_add_u32 s86, s86, s87\n"
"s_lshl_b32 s86, s86, 12\n"
"s_and_b32 s87, s76, 1\n"
"s_lshl_b32 s87, s87, 11\n"
"s_add_u32 s92, s86, s87\n"
"s_lshr_b32 s27, s94, 3\n"
"s_and_b32 s26, s94, 7\n"
"s_lshl_b32 s89, s76, 1\n"
"s_add_u32 s89, s89, s27\n"
"s_lshl_b32 s86, s26, 3\n"
"s_add_u32 s86, s86, s89\n"
"s_mul_i32 s96, s86, 56320\n"
"s_add_u32 s96, s96, 12124160\n"
"s_lshl_b32 s86, s77, 1\n"
"s_add_u32 s86, s86, s27\n"
"s_xor_b32 s87, s86, 1\n"
"s_lshl_b32 s86, s86, 3\n"
"s_lshl_b32 s87, s87, 3\n"
"s_add_u32 s86, s86, s89\n"
"s_add_u32 s87, s87, s89\n"
"s_mul_i32 s97, s86, 45056\n"
"s_mul_i32 s93, s87, 45056\n"
"s_add_u32 s97, s97, 586752\n"
"s_add_u32 s93, s93, 586752\n"
"s_lshl_b32 s86, s77, 4\n"
"s_add_u32 s87, s86, s94\n"
"s_lshl_b32 s87, s87, 2\n"
"s_add_u32 s87, s87, s76\n"
"s_lshl_b32 s87, s87, 7\n"
"s_add_u32 s28, s87, 131072\n"
"s_lshl_b32 s87, s89, 1\n"
"s_add_u32 s86, s86, s87\n"
"s_lshl_b32 s86, s86, 9\n"
"s_add_u32 s86, s86, 131072\n"
"v_lshrrev_b32 v180, 3, v255\n"
"v_lshlrev_b32 v180, 7, v180\n"
"v_add_u32 v189, s86, v180\n"
"v_accvgpr_write_b32 a1, v189\n"
"buffer_load_dwordx4 v253, s[36:39], s57 offen offset:2048 lds\n"
"buffer_load_dwordx4 v254, s[36:39], s57 offen offset:3072 lds\n"
"s_add_u32 m0, s79, 4096\n"
"s_nop 0\n"
"buffer_load_dwordx4 v80, s[40:43], s57 offen lds\n"
"buffer_load_dwordx4 v80, s[52:55], s57 offen offset:1024 lds\n"
"buffer_load_dwordx4 v82, s[40:43], s57 offen offset:2048 lds\n"
"buffer_load_dwordx4 v82, s[52:55], s57 offen offset:3072 lds\n"
"s_add_u32 m0, s79, 32768\n"
"s_nop 0\n"
"buffer_load_dwordx4 v251, s[36:39], s58 offen lds\n"
"buffer_load_dwordx4 v252, s[36:39], s58 offen offset:1024 lds\n"
"s_waitcnt vmcnt(22)\n"
"s_barrier\n"
"ds_read_b128 a[160:163], v48 offset:24576\n"
"ds_read_b128 a[140:143], v22\n"
"ds_read_b128 a[144:147], v22 offset:1024\n"
"ds_read_b128 a[164:167], v22 offset:2048\n"
"ds_read_b128 a[108:111], v22 offset:3072\n"
"ds_read_b128 a[128:131], v49 offset:24576\n"
"ds_read_b128 a[148:151], v23\n"
"ds_read_b128 a[152:155], v23 offset:1024\n"
"ds_read_b128 a[156:159], v23 offset:2048\n"
"ds_read_b128 a[120:123], v23 offset:3072\n"
"s_waitcnt lgkmcnt(5)\n"
"v_mfma_f32_32x32x16_bf16 v[60:75], a[160:163], a[140:143], 0\n"
"ds_read_b128 a[116:119], v50 offset:24576\n"
"ds_read_b128 a[136:139], v24\n"
"buffer_load_dwordx4 v253, s[36:39], s58 offen offset:2048 lds\n"
"v_mfma_f32_32x32x16_bf16 v[0:15], a[160:163], a[144:147], 0\n"
"ds_read_b128 a[140:143], v24 offset:1024\n"
"v_lshrrev_b32 v180, 3, v255\n"
"v_bfe_u32 v53, v255, 2, 1\n"
"v_min_u32 v189, 5, v180\n"
"s_lshl_b32 s86, s89, 2\n"
"s_add_u32 s87, s87, 28\n"
"buffer_load_dwordx4 v254, s[36:39], s58 offen offset:3072 lds\n"
"v_mfma_f32_32x32x16_bf16 v[140:155], a[160:163], a[164:167], 0\n"
"ds_read_b128 a[144:147], v24 offset:2048\n"
"s_sub_u32 s87, s87, s86\n"
"v_lshrrev_b32 v229, 2, v189\n"
"v_mul_u32_u24 v229, s87, v229\n"
"v_mfma_f32_32x32x16_bf16 v[160:175], a[160:163], a[108:111], 0\n"
"ds_read_b128 a[164:167], v24 offset:3072\n"
"v_add3_u32 v189, v189, v229, s86\n"
"s_add_u32 m0, s79, 8192\n"
"v_mul_u32_u24 v189, 10, v189\n"
"buffer_load_dwordx4 v80, s[40:43], s58 offen lds\n"
"s_waitcnt lgkmcnt(5)\n"
"v_mfma_f32_32x32x16_bf16 v[60:75], a[128:131], a[148:151], v[60:75]\n"
"ds_read_b128 a[108:111], v51 offset:24576\n"
"ds_read_b128 a[124:127], v25\n"
"s_mul_i32 s86, s26, 5\n"
"s_lshr_b32 s86, s86, 2\n"
"v_mfma_f32_32x32x16_bf16 v[0:15], a[128:131], a[152:155], v[0:15]\n"
"ds_read_b128 a[148:151], v25 offset:1024\n"
"v_add3_u32 v189, v189, v53, s86\n"
"buffer_load_dwordx4 v80, s[52:55], s58 offen offset:1024 lds\n"
"v_lshlrev_b32 v189, 7, v189\n"
"v_mfma_f32_32x32x16_bf16 v[140:155], a[128:131], a[156:159], v[140:155]\n"
"ds_read_b128 a[152:155], v25 offset:2048\n"
"v_add_u32 v229, 65600, v189\n"
"v_min_u32 v180, 4, v180\n"
"v_mul_u32_u24 v180, 10, v180\n"
"s_mul_i32 s86, s26, 50\n"
"s_mul_i32 s87, s89, 5\n"
"v_mfma_f32_32x32x16_bf16 v[160:175], a[128:131], a[120:123], v[160:175]\n"
"ds_read_b128 a[156:159], v25 offset:3072\n"
"s_lshr_b32 s87, s87, 2\n"
"buffer_load_dwordx4 v82, s[40:43], s58 offen offset:2048 lds\n"
"s_add_u32 s86, s86, s87\n"
"v_add3_u32 v180, v180, v53, s86\n"
"v_lshlrev_b32 v180, 7, v180\n"
"v_add_u32 v189, 65536, v180\n"
"s_lshl_b32 s18, s77, 3\n"
"s_add_u32 s18, s18, s78\n"
"s_lshl_b32 s18, s18, 2\n"
"s_add_u32 s18, s18, s76\n"
"s_mul_i32 s19, s18, 205\n"
"s_lshr_b32 s19, s19, 11\n"
"s_mul_i32 s86, s19, 10\n"
"s_sub_u32 s20, s18, s86\n"
"s_min_u32 s19, s19, 47\n"
"buffer_load_dwordx4 v82, s[52:55], s58 offen offset:3072 lds\n"
"v_lshrrev_b32 v20, 3, v255\n"
"v_and_b32 v21, 7, v255\n"
"v_mul_u32_u24 v53, 1280, v20\n"
"v_lshl_add_u32 v53, v21, 4, v53\n"
"s_lshl_b32 s88, s20, 7\n"
"s_mul_i32 s86, s19, 40960\n"
"s_add_u32 s86, s86, s88\n"
"v_add_u32 v53, s86, v53\n"
"s_lshr_b32 s86, s19, 1\n"
"s_lshr_b32 s87, s19, 2\n"
"s_sub_u32 s88, s86, 16\n"
"s_and_b32 s89, s86, 1\n"
"s_mul_i32 s89, s89, 7\n"
"s_add_u32 m0, s79, 36864\n"
"s_nop 0\n"
"buffer_load_dwordx4 v251, s[36:39], s59 offen lds\n"
"s_cmp_lt_u32 s86, 16\n"
"s_cselect_b32 s87, s87, s88\n"
"s_cselect_b32 s89, s89, 9\n"
"s_mul_i32 s86, s87, 11\n"
"s_add_u32 s86, s86, s89\n"
"s_and_b32 s87, s19, 1\n"
"s_add_u32 s86, s86, s87\n"
"s_mul_i32 s86, s86, 5120\n"
"s_add_u32 s86, s86, 12124160\n"
"s_lshl_b32 s88, s20, 2\n"
"s_mul_i32 s87, s88, 205\n"
"s_lshr_b32 s87, s87, 10\n"
"buffer_load_dwordx4 v252, s[36:39], s59 offen offset:1024 lds\n"
"s_mul_i32 s87, s87, 445440\n"
"s_lshl_b32 s10, s88, 10\n"
"s_add_u32 s10, s10, s87\n"
"s_add_u32 s10, s10, s86\n"
"s_add_u32 s88, s88, 1\n"
"s_mul_i32 s87, s88, 205\n"
"s_lshr_b32 s87, s87, 10\n"
"s_mul_i32 s87, s87, 445440\n"
"s_lshl_b32 s11, s88, 10\n"
"s_add_u32 s11, s11, s87\n"
"s_add_u32 s11, s11, s86\n"
"s_add_u32 s88, s88, 1\n"
"s_mul_i32 s87, s88, 205\n"
"s_lshr_b32 s87, s87, 10\n"
"buffer_load_dwordx4 v253, s[36:39], s59 offen offset:2048 lds\n"
"s_mul_i32 s87, s87, 445440\n"
"s_lshl_b32 s12, s88, 10\n"
"s_add_u32 s12, s12, s87\n"
"s_add_u32 s12, s12, s86\n"
"s_add_u32 s88, s88, 1\n"
"s_mul_i32 s87, s88, 205\n"
"s_lshr_b32 s87, s87, 10\n"
"s_mul_i32 s87, s87, 445440\n"
"s_lshl_b32 s13, s88, 10\n"
"s_add_u32 s13, s13, s87\n"
"s_add_u32 s13, s13, s86\n"
"v_lshrrev_b32 v20, 5, v255\n"
"v_bfe_u32 v21, v255, 2, 2\n"
"buffer_load_dwordx4 v254, s[36:39], s59 offen offset:3072 lds\n"
"s_waitcnt lgkmcnt(5)\n"
"v_mfma_f32_32x32x16_bf16 v[60:75], a[116:119], a[136:139], v[60:75]\n"
"s_waitcnt vmcnt(12)\n"
"s_barrier\n"
"v_lshl_add_u32 v180, v20, 4, v21\n"
"v_mfma_f32_32x32x16_bf16 v[0:15], a[116:119], a[140:143], v[0:15]\n"
"ds_read_b128 a[136:139], v48 offset:28672\n"
"s_add_u32 m0, s79, 12288\n"
"ds_read_b128 a[104:107], v22 offset:4096\n"
"buffer_load_dwordx4 v80, s[40:43], s59 offen lds\n"
"v_mfma_f32_32x32x16_bf16 v[140:155], a[116:119], a[144:147], v[140:155]\n"
"ds_read_b128 a[132:135], v22 offset:5120\n"
"ds_read_b128 a[120:123], v22 offset:6144\n"
"v_bfe_u32 v20, v255, 4, 1\n"
"v_lshl_add_u32 v180, v20, 2, v180\n"
"v_lshlrev_b32 v180, 7, v180\n"
"v_mfma_f32_32x32x16_bf16 v[160:175], a[116:119], a[164:167], v[160:175]\n"
"ds_read_b128 a[144:147], v22 offset:7168\n"
"v_and_b32 v20, 3, v255\n"
"buffer_load_dwordx4 v80, s[52:55], s59 offen offset:1024 lds\n"
"v_lshlrev_b32 v20, 3, v20\n"
"s_waitcnt lgkmcnt(5)\n"
"v_mfma_f32_32x32x16_bf16 v[60:75], a[108:111], a[124:127], v[60:75]\n"
"ds_read_b128 a[140:143], v49 offset:28672\n"
"ds_read_b128 a[96:99], v23 offset:4096\n"
"v_add3_u32 v180, v180, v20, s79\n"
"s_lshl_b32 s91, s18, 7\n"
"s_add_u32 s91, s91, 65600\n"
"v_mfma_f32_32x32x16_bf16 v[0:15], a[108:111], a[148:151], v[0:15]\n"
"ds_read_b128 a[112:115], v23 offset:5120\n"
"s_mov_b32 s18, 0xffff0000\n"
"buffer_load_dwordx4 v82, s[40:43], s59 offen offset:2048 lds\n"
"s_mov_b32 s19, -1\n"
"v_mfma_f32_32x32x16_bf16 v[140:155], a[108:111], a[152:155], v[140:155]\n"
"ds_read_b128 a[100:103], v23 offset:6144\n"
"s_mov_b32 s20, 0xffff0000\n"
"s_mov_b32 s21, 0xffff0000\n"
"v_lshlrev_b32 v204, 16, v204\n"
"v_lshlrev_b32 v114, 16, v114\n"
"v_lshlrev_b32 v202, 16, v202\n"
"v_mfma_f32_32x32x16_bf16 v[160:175], a[108:111], a[156:159], v[160:175]\n"
"ds_read_b128 a[88:91], v23 offset:7168\n"
"v_lshlrev_b32 v112, 16, v112\n"
"buffer_load_dwordx4 v82, s[52:55], s59 offen offset:3072 lds\n"
"v_mul_f32 v204, 0x3fb8aa3b, v204\n"
"s_waitcnt lgkmcnt(5)\n"
"v_mfma_f32_32x32x16_bf16 v[60:75], a[136:139], a[104:107], v[60:75]\n"
"ds_read_b128 a[148:151], v50 offset:28672\n"
"ds_read_b128 a[156:159], v24 offset:4096\n"
"v_mul_f32 v114, 0x3fb8aa3b, v114\n"
"v_lshlrev_b32 v203, 16, v203\n"
"v_lshlrev_b32 v113, 16, v113\n"
"v_mfma_f32_32x32x16_bf16 v[0:15], a[136:139], a[132:135], v[0:15]\n"
"s_add_u32 m0, s79, 24576\n"
"ds_read_b128 a[104:107], v24 offset:5120\n"
"buffer_load_dwordx4 v251, s[36:39], s60 offen lds\n"
"v_mfma_f32_32x32x16_bf16 v[140:155], a[136:139], a[120:123], v[140:155]\n"
"ds_read_b128 a[132:135], v24 offset:6144\n"
"v_exp_f32 v204, v204\n"
"v_exp_f32 v114, v114\n"
"v_cvt_pk_bf16_f32 v204, v204, v204\n"
"v_mfma_f32_32x32x16_bf16 v[160:175], a[136:139], a[144:147], v[160:175]\n"
"ds_read_b128 a[164:167], v24 offset:7168\n"
"buffer_load_dwordx4 v252, s[36:39], s60 offen offset:1024 lds\n"
"v_cvt_pk_bf16_f32 v114, v114, v114\n"
"v_lshlrev_b32 v204, 16, v204\n"
"s_waitcnt lgkmcnt(5)\n"
"v_mfma_f32_32x32x16_bf16 v[60:75], a[140:143], a[96:99], v[60:75]\n"
"ds_read_b128 a[152:155], v51 offset:28672\n"
"ds_read_b128 a[144:147], v25 offset:4096\n"
"v_lshlrev_b32 v114, 16, v114\n"
"v_mul_f32 v202, v204, v202\n"
"v_mul_f32 v112, v114, v112\n"
"v_mfma_f32_32x32x16_bf16 v[0:15], a[140:143], a[112:115], v[0:15]\n"
"ds_read_b128 a[124:127], v25 offset:5120\n"
"buffer_load_dwordx4 v253, s[36:39], s60 offen offset:2048 lds\n"
"v_mul_f32 v203, v204, v203\n"
"v_mul_f32 v113, v114, v113\n"
"v_mfma_f32_32x32x16_bf16 v[140:155], a[140:143], a[100:103], v[140:155]\n"
"ds_read_b128 a[120:123], v25 offset:6144\n"
"v_mul_f32 v202, 0x3fb8aa3b, v202\n"
"v_mul_f32 v112, 0x3fb8aa3b, v112\n"
"v_mul_f32 v203, 0x3e22f983, v203\n"
"v_mul_f32 v113, 0x3e22f983, v113\n"
"v_mfma_f32_32x32x16_bf16 v[160:175], a[140:143], a[88:91], v[160:175]\n"
"ds_read_b128 a[112:115], v25 offset:7168\n"
"buffer_load_dwordx4 v254, s[36:39], s60 offen offset:3072 lds\n"
"v_exp_f32 v20, v202\n"
"v_exp_f32 v78, v112\n"
"v_fract_f32 v21, v203\n"
"v_fract_f32 v79, v113\n"
"v_cos_f32 v186, v21\n"
"v_cos_f32 v202, v79\n"
"v_sin_f32 v187, v21\n"
"s_add_u32 m0, s79, 16384\n"
"v_sin_f32 v203, v79\n"
"buffer_load_dwordx4 v80, s[40:43], s60 offen lds\n"
"s_waitcnt lgkmcnt(5)\n"
"v_mfma_f32_32x32x16_bf16 v[60:75], a[148:151], a[156:159], v[60:75]\n"
"s_waitcnt vmcnt(13)\n"
"s_barrier\n"
"v_mul_f32 v186, v20, v186\n"
"v_mfma_f32_32x32x16_bf16 v[0:15], a[148:151], a[104:107], v[0:15]\n"
"ds_read_b128 a[156:159], v48 offset:32768\n"
"ds_read_b128 a[96:99], v22 offset:8192\n"
"buffer_load_dwordx4 v80, s[52:55], s60 offen offset:1024 lds\n"
"v_mul_f32 v202, v78, v202\n"
"v_mfma_f32_32x32x16_bf16 v[140:155], a[148:151], a[132:135], v[140:155]\n"
"ds_read_b128 a[104:107], v22 offset:9216\n"
"ds_read_b128 a[100:103], v22 offset:10240\n"
"v_mul_f32 v187, v20, v187\n"
"v_mul_f32 v203, v78, v203\n"
"v_lshlrev_b32 v212, 16, v212\n"
"v_lshlrev_b32 v138, 16, v138\n"
"v_mfma_f32_32x32x16_bf16 v[160:175], a[148:151], a[164:167], v[160:175]\n"
"ds_read_b128 a[88:91], v22 offset:11264\n"
"buffer_load_dwordx4 v82, s[40:43], s60 offen offset:2048 lds\n"
"v_lshlrev_b32 v210, 16, v210\n"
"v_lshlrev_b32 v136, 16, v136\n"
"s_waitcnt lgkmcnt(5)\n"
"v_mfma_f32_32x32x16_bf16 v[60:75], a[152:155], a[144:147], v[60:75]\n"
"ds_read_b128 a[164:167], v49 offset:32768\n"
"ds_read_b128 a[92:95], v23 offset:8192\n"
"v_mul_f32 v212, 0x3fb8aa3b, v212\n"
"v_lshlrev_b32 v211, 16, v211\n"
"v_mfma_f32_32x32x16_bf16 v[0:15], a[152:155], a[124:127], v[0:15]\n"
"ds_read_b128 a[132:135], v23 offset:9216\n"
"buffer_load_dwordx4 v82, s[52:55], s60 offen offset:3072 lds\n"
"v_mul_f32 v138, 0x3fb8aa3b, v138\n"
"v_mfma_f32_32x32x16_bf16 v[140:155], a[152:155], a[120:123], v[140:155]\n"
"ds_read_b128 a[76:79], v23 offset:10240\n"
"v_exp_f32 v212, v212\n"
"v_lshlrev_b32 v137, 16, v137\n"
"v_cvt_pk_bf16_f32 v212, v212, v212\n"
"v_lshlrev_b32 v212, 16, v212\n"
"v_mfma_f32_32x32x16_bf16 v[160:175], a[152:155], a[112:115], v[160:175]\n"
"s_add_u32 m0, s79, 28672\n"
"ds_read_b128 a[68:71], v23 offset:11264\n"
"buffer_load_dwordx4 v251, s[36:39], s61 offen lds\n"
"s_waitcnt lgkmcnt(5)\n"
"v_mfma_f32_32x32x16_bf16 v[60:75], a[156:159], a[96:99], v[60:75]\n"
"ds_read_b128 a[144:147], v50 offset:32768\n"
"ds_read_b128 a[120:123], v24 offset:8192\n"
"v_exp_f32 v138, v138\n"
"v_mul_f32 v210, v212, v210\n"
"v_mfma_f32_32x32x16_bf16 v[0:15], a[156:159], a[104:107], v[0:15]\n"
"ds_read_b128 a[112:115], v24 offset:9216\n"
"buffer_load_dwordx4 v252, s[36:39], s61 offen offset:1024 lds\n"
"v_cvt_pk_bf16_f32 v138, v138, v138\n"
"v_mul_f32 v211, v212, v211\n"
"v_mfma_f32_32x32x16_bf16 v[140:155], a[156:159], a[100:103], v[140:155]\n"
"ds_read_b128 a[104:107], v24 offset:10240\n"
"v_mul_f32 v210, 0x3fb8aa3b, v210\n"
"v_lshlrev_b32 v138, 16, v138\n"
"v_mul_f32 v211, 0x3e22f983, v211\n"
"v_mul_f32 v136, v138, v136\n"
"v_mfma_f32_32x32x16_bf16 v[160:175], a[156:159], a[88:91], v[160:175]\n"
"ds_read_b128 a[100:103], v24 offset:11264\n"
"buffer_load_dwordx4 v253, s[36:39], s61 offen offset:2048 lds\n"
"v_exp_f32 v20, v210\n"
"s_waitcnt lgkmcnt(5)\n"
"v_mfma_f32_32x32x16_bf16 v[60:75], a[164:167], a[92:95], v[60:75]\n"
"ds_read_b128 a[124:127], v51 offset:32768\n"
"ds_read_b128 a[96:99], v25 offset:8192\n"
"v_fract_f32 v21, v211\n"
"v_mul_f32 v137, v138, v137\n"
"v_mfma_f32_32x32x16_bf16 v[0:15], a[164:167], a[132:135], v[0:15]\n"
"ds_read_b128 a[88:91], v25 offset:9216\n"
"buffer_load_dwordx4 v254, s[36:39], s61 offen offset:3072 lds\n"
"v_cos_f32 v204, v21\n"
"v_mfma_f32_32x32x16_bf16 v[140:155], a[164:167], a[76:79], v[140:155]\n"
"ds_read_b128 a[92:95], v25 offset:10240\n"
"v_mul_f32 v136, 0x3fb8aa3b, v136\n"
"v_sin_f32 v205, v21\n"
"v_mul_f32 v204, v20, v204\n"
"v_mul_f32 v137, 0x3e22f983, v137\n"
"v_mfma_f32_32x32x16_bf16 v[160:175], a[164:167], a[68:71], v[160:175]\n"
"s_add_u32 m0, s79, 20480\n"
"ds_read_b128 a[76:79], v25 offset:11264\n"
"buffer_load_dwordx4 v80, s[40:43], s61 offen lds\n"
"v_mul_f32 v205, v20, v205\n"
"v_mul_f32 v20, 0x41800000, v136\n"
"v_mul_f32 v78, 0x42000000, v136\n"
"v_mul_f32 v76, 0x42800000, v136\n"
"v_mul_f32 v21, 0x41800000, v137\n"
"v_mul_f32 v79, 0x42000000, v137\n"
"v_mul_f32 v77, 0x42800000, v137\n"
"v_exp_f32 v20, v20\n"
"v_exp_f32 v78, v78\n"
"buffer_load_dwordx4 v80, s[52:55], s61 offen offset:1024 lds\n"
"s_waitcnt lgkmcnt(5)\n"
"v_mfma_f32_32x32x16_bf16 v[60:75], a[144:147], a[120:123], v[60:75]\n"
"s_waitcnt vmcnt(14)\n"
"s_barrier\n"
"v_mfma_f32_32x32x16_bf16 v[0:15], a[144:147], a[112:115], v[0:15]\n"
"ds_read_b128 a[132:135], v48 offset:36864\n"
"ds_read_b128 a[80:83], v22 offset:12288\n"
"v_exp_f32 v76, v76\n"
"buffer_load_dwordx4 v82, s[40:43], s61 offen offset:2048 lds\n"
"v_mfma_f32_32x32x16_bf16 v[140:155], a[144:147], a[104:107], v[140:155]\n"
"ds_read_b128 a[56:59], v22 offset:13312\n"
"ds_read_b128 a[48:51], v22 offset:14336\n"
"v_fract_f32 v21, v21\n"
"v_fract_f32 v79, v79\n"
"v_fract_f32 v77, v77\n"
"v_mfma_f32_32x32x16_bf16 v[160:175], a[144:147], a[100:103], v[160:175]\n"
"ds_read_b128 a[104:107], v22 offset:15360\n"
"v_cos_f32 v58, v21\n"
"v_cos_f32 v114, v79\n"
"buffer_load_dwordx4 v82, s[52:55], s61 offen offset:3072 lds\n"
"s_waitcnt lgkmcnt(5)\n"
"v_mfma_f32_32x32x16_bf16 v[60:75], a[124:127], a[96:99], v[60:75]\n"
"ds_read_b128 a[120:123], v49 offset:36864\n"
"ds_read_b128 a[84:87], v23 offset:12288\n"
"v_mfma_f32_32x32x16_bf16 v[0:15], a[124:127], a[88:91], v[0:15]\n"
"ds_read_b128 a[68:71], v23 offset:13312\n"
"v_cos_f32 v112, v77\n"
"s_add_u32 m0, s79, 32768\n"
"v_sin_f32 v59, v21\n"
"buffer_load_dwordx4 v251, s[36:39], s62 offen lds\n"
"v_mfma_f32_32x32x16_bf16 v[140:155], a[124:127], a[92:95], v[140:155]\n"
"ds_read_b128 a[100:103], v23 offset:14336\n"
"v_sin_f32 v115, v79\n"
"v_mfma_f32_32x32x16_bf16 v[160:175], a[124:127], a[76:79], v[160:175]\n"
"ds_read_b128 a[96:99], v23 offset:15360\n"
"v_sin_f32 v113, v77\n"
"v_mul_f32 v58, v20, v58\n"
"buffer_load_dwordx4 v252, s[36:39], s62 offen offset:1024 lds\n"
"s_waitcnt lgkmcnt(5)\n"
"v_mfma_f32_32x32x16_bf16 v[60:75], a[132:135], a[80:83], v[60:75]\n"
"ds_read_b128 a[112:115], v50 offset:36864\n"
"ds_read_b128 a[88:91], v24 offset:12288\n"
"v_mul_f32 v114, v78, v114\n"
"v_mul_f32 v112, v76, v112\n"
"v_mfma_f32_32x32x16_bf16 v[0:15], a[132:135], a[56:59], v[0:15]\n"
"ds_read_b128 a[76:79], v24 offset:13312\n"
"v_mul_f32 v59, v20, v59\n"
"v_mul_f32 v115, v78, v115\n"
"v_mul_f32 v113, v76, v113\n"
"v_mul_f32 v20, 0x43000000, v136\n"
"buffer_load_dwordx4 v253, s[36:39], s62 offen offset:2048 lds\n"
"v_mfma_f32_32x32x16_bf16 v[140:155], a[132:135], a[48:51], v[140:155]\n"
"ds_read_b128 a[92:95], v24 offset:14336\n"
"v_mul_f32 v116, 0x44000000, v176\n"
"v_mul_f32 v139, 0x42000000, v176\n"
"v_mfma_f32_32x32x16_bf16 v[160:175], a[132:135], a[104:107], v[160:175]\n"
"ds_read_b128 a[48:51], v24 offset:15360\n"
"v_mul_f32 v21, 0x43000000, v137\n"
"v_mul_f32 v78, v116, v136\n"
"v_mul_f32 v76, v139, v136\n"
"v_exp_f32 v20, v20\n"
"buffer_load_dwordx4 v254, s[36:39], s62 offen offset:3072 lds\n"
"s_waitcnt lgkmcnt(5)\n"
"v_mfma_f32_32x32x16_bf16 v[60:75], a[120:123], a[84:87], v[60:75]\n"
"ds_read_b128 a[104:107], v51 offset:36864\n"
"ds_read_b128 a[56:59], v25 offset:12288\n"
"v_mul_f32 v79, v116, v137\n"
"v_mfma_f32_32x32x16_bf16 v[0:15], a[120:123], a[68:71], v[0:15]\n"
"ds_read_b128 a[84:87], v25 offset:13312\n"
"v_mul_f32 v77, v139, v137\n"
"v_fract_f32 v21, v21\n"
"s_add_u32 m0, s79, 0\n"
"v_exp_f32 v78, v78\n"
"buffer_load_dwordx4 v80, s[40:43], s62 offen lds\n"
"v_mfma_f32_32x32x16_bf16 v[140:155], a[120:123], a[100:103], v[140:155]\n"
"ds_read_b128 a[80:83], v25 offset:14336\n"
"v_exp_f32 v76, v76\n"
"v_mfma_f32_32x32x16_bf16 v[160:175], a[120:123], a[96:99], v[160:175]\n"
"ds_read_b128 a[68:71], v25 offset:15360\n"
"v_cos_f32 v138, v21\n"
"buffer_load_dwordx4 v80, s[52:55], s62 offen offset:1024 lds\n"
"s_waitcnt lgkmcnt(5)\n"
"v_mfma_f32_32x32x16_bf16 v[60:75], a[112:115], a[88:91], v[60:75]\n"
"s_waitcnt vmcnt(14)\n"
"s_barrier\n"
"v_fract_f32 v79, v79\n"
"v_mfma_f32_32x32x16_bf16 v[0:15], a[112:115], a[76:79], v[0:15]\n"
"ds_read_b128 a[100:103], v48 offset:24576\n"
"ds_read_b128 a[40:43], v22 offset:16384\n"
"v_fract_f32 v77, v77\n"
"v_sin_f32 v139, v21\n"
"buffer_load_dwordx4 v82, s[40:43], s62 offen offset:2048 lds\n"
"v_mfma_f32_32x32x16_bf16 v[140:155], a[112:115], a[92:95], v[140:155]\n"
"ds_read_b128 a[72:75], v22 offset:17408\n"
"ds_read_b128 a[64:67], v22 offset:18432\n"
"v_cos_f32 v116, v79\n"
"v_mfma_f32_32x32x16_bf16 v[160:175], a[112:115], a[48:51], v[160:175]\n"
"ds_read_b128 a[88:91], v22 offset:19456\n"
"v_cos_f32 v136, v77\n"
"v_mul_f32 v138, v20, v138\n"
"v_sin_f32 v117, v79\n"
"buffer_load_dwordx4 v82, s[52:55], s62 offen offset:3072 lds\n"
"s_waitcnt lgkmcnt(5)\n"
"v_mfma_f32_32x32x16_bf16 v[60:75], a[104:107], a[56:59], v[60:75]\n"
"ds_read_b128 a[96:99], v49 offset:24576\n"
"ds_read_b128 a[60:63], v23 offset:16384\n"
"v_mfma_f32_32x32x16_bf16 v[0:15], a[104:107], a[84:87], v[0:15]\n"
"ds_read_b128 a[76:79], v23 offset:17408\n"
"v_sin_f32 v137, v77\n"
"s_add_u32 m0, s79, 36864\n"
"v_mul_f32 v139, v20, v139\n"
"buffer_load_dwordx4 v251, s[36:39], s63 offen lds\n"
"v_mfma_f32_32x32x16_bf16 v[140:155], a[104:107], a[80:83], v[140:155]\n"
"ds_read_b128 a[48:51], v23 offset:18432\n"
"v_mul_f32 v116, v78, v116\n"
"v_mul_f32 v136, v76, v136\n"
"v_mul_f32 v117, v78, v117\n"
"v_mfma_f32_32x32x16_bf16 v[160:175], a[104:107], a[68:71], v[160:175]\n"
"ds_read_b128 a[56:59], v23 offset:19456\n"
"v_mul_f32 v137, v76, v137\n"
"v_cndmask_b32 v20, 1.0, v114, s[20:21]  \n"
"v_cndmask_b32 v21, 0, v115, s[20:21]  \n"
"buffer_load_dwordx4 v252, s[36:39], s63 offen offset:1024 lds\n"
"s_waitcnt lgkmcnt(5)\n"
"v_mfma_f32_32x32x16_bf16 v[60:75], a[100:103], a[40:43], v[60:75]\n"
"ds_read_b128 a[92:95], v50 offset:24576\n"
"ds_read_b128 a[84:87], v24 offset:16384\n"
"v_mfma_f32_32x32x16_bf16 v[0:15], a[100:103], a[72:75], v[0:15]\n"
"ds_read_b128 a[68:71], v24 offset:17408\n"
"v_cmp_lt_u32 vcc, 31, v255\n"
"s_nop 1\n"
"s_nop 0\n"
"v_cndmask_b32 v20, 0, v20, vcc\n"
"v_cndmask_b32 v21, 0, v21, vcc\n"
"buffer_load_dwordx4 v[76:79], v158, s[48:51], 0 offen sc1\n"
"v_mfma_f32_32x32x16_bf16 v[140:155], a[100:103], a[64:67], v[140:155]\n"
"ds_read_b128 a[80:83], v24 offset:18432\n"
"v_mul_f32 v42, 0x41000000, v210\n"
"v_mul_f32 v216, 0x42000000, v210\n"
"v_mul_f32 v40, 0x42800000, v210\n"
"v_mfma_f32_32x32x16_bf16 v[160:175], a[100:103], a[88:91], v[160:175]\n"
"ds_read_b128 a[64:67], v24 offset:19456\n"
"v_mul_f32 v43, 0x41000000, v211\n"
"v_mul_f32 v217, 0x42000000, v211\n"
"v_mul_f32 v41, 0x42800000, v211\n"
"s_waitcnt lgkmcnt(5)\n"
"v_mfma_f32_32x32x16_bf16 v[60:75], a[96:99], a[60:63], v[60:75]\n"
"ds_read_b128 a[88:91], v51 offset:24576\n"
"ds_read_b128 a[40:43], v25 offset:16384\n"
"buffer_load_dwordx4 v253, s[36:39], s63 offen offset:2048 lds\n"
"v_mfma_f32_32x32x16_bf16 v[0:15], a[96:99], a[76:79], v[0:15]\n"
"ds_read_b128 a[60:63], v25 offset:17408\n"
"v_exp_f32 v42, v42\n"
"v_exp_f32 v216, v216\n"
"v_mfma_f32_32x32x16_bf16 v[140:155], a[96:99], a[48:51], v[140:155]\n"
"ds_read_b128 a[72:75], v25 offset:18432\n"
"buffer_load_dwordx4 v254, s[36:39], s63 offen offset:3072 lds\n"
"v_exp_f32 v40, v40\n"
"v_mfma_f32_32x32x16_bf16 v[160:175], a[96:99], a[56:59], v[160:175]\n"
"ds_read_b128 a[76:79], v25 offset:19456\n"
"v_fract_f32 v43, v43\n"
"v_fract_f32 v217, v217\n"
"v_fract_f32 v41, v41\n"
"v_cos_f32 v206, v43\n"
"v_cos_f32 v214, v217\n"
"v_cos_f32 v118, v41\n"
"buffer_load_dwordx4 v[36:39], v156, s[48:51], 0 offen sc1\n"
"s_waitcnt lgkmcnt(5)\n"
"v_mfma_f32_32x32x16_bf16 v[60:75], a[92:95], a[84:87], v[60:75]\n"
"s_waitcnt vmcnt(14)\n"
"s_barrier\n"
"v_mfma_f32_32x32x16_bf16 v[0:15], a[92:95], a[68:71], v[0:15]\n"
"ds_read_b128 a[84:87], v48 offset:28672\n"
"ds_read_b128 a[32:35], v22 offset:20480\n"
"v_sin_f32 v207, v43\n"
"s_add_u32 m0, s79, 4096\n"
"v_sin_f32 v215, v217\n"
"buffer_load_dwordx4 v80, s[40:43], s63 offen lds\n"
"v_mfma_f32_32x32x16_bf16 v[140:155], a[92:95], a[80:83], v[140:155]\n"
"ds_read_b128 a[12:15], v22 offset:21504\n"
"ds_read_b128 a[48:51], v22 offset:22528\n"
"v_sin_f32 v119, v41\n"
"v_mfma_f32_32x32x16_bf16 v[160:175], a[92:95], a[64:67], v[160:175]\n"
"ds_read_b128 a[68:71], v22 offset:23552\n"
"v_mul_f32 v206, v42, v206\n"
"v_mul_f32 v214, v216, v214\n"
"buffer_load_dwordx4 v80, s[52:55], s63 offen offset:1024 lds\n"
"s_waitcnt lgkmcnt(5)\n"
"v_mfma_f32_32x32x16_bf16 v[60:75], a[88:91], a[40:43], v[60:75]\n"
"ds_read_b128 a[80:83], v49 offset:28672\n"
"ds_read_b128 a[64:67], v23 offset:20480\n"
"v_mul_f32 v118, v40, v118\n"
"v_mul_f32 v207, v42, v207\n"
"v_mul_f32 v215, v216, v215\n"
"v_mfma_f32_32x32x16_bf16 v[0:15], a[88:91], a[60:63], v[0:15]\n"
"ds_read_b128 a[56:59], v23 offset:21504\n"
"v_mul_f32 v119, v40, v119\n"
"v_mul_f32 v178, 0x43000000, v210\n"
"v_mul_f32 v44, 0x44000000, v176\n"
"buffer_load_dwordx4 v[40:43], v156, s[48:51], s90 offen sc1\n"
"v_mfma_f32_32x32x16_bf16 v[140:155], a[88:91], a[72:75], v[140:155]\n"
"ds_read_b128 a[60:63], v23 offset:22528\n"
"v_mul_f32 v213, 0x42000000, v176\n"
"v_mul_f32 v179, 0x43000000, v211\n"
"v_mul_f32 v176, v44, v210\n"
"v_mfma_f32_32x32x16_bf16 v[160:175], a[88:91], a[76:79], v[160:175]\n"
"ds_read_b128 a[52:55], v23 offset:23552\n"
"v_mul_f32 v216, v213, v210\n"
"v_exp_f32 v178, v178\n"
"v_mul_f32 v177, v44, v211\n"
"buffer_load_dwordx4 v82, s[40:43], s63 offen offset:2048 lds\n"
"s_waitcnt lgkmcnt(5)\n"
"v_mfma_f32_32x32x16_bf16 v[60:75], a[84:87], a[32:35], v[60:75]\n"
"ds_read_b128 a[76:79], v50 offset:28672\n"
"ds_read_b128 a[44:47], v24 offset:20480\n"
"v_mul_f32 v217, v213, v211\n"
"v_mfma_f32_32x32x16_bf16 v[0:15], a[84:87], a[12:15], v[0:15]\n"
"ds_read_b128 a[36:39], v24 offset:21504\n"
"v_fract_f32 v179, v179\n"
"v_exp_f32 v176, v176\n"
"v_exp_f32 v216, v216\n"
"buffer_load_dwordx4 v82, s[52:55], s63 offen offset:3072 lds\n"
"v_mfma_f32_32x32x16_bf16 v[140:155], a[84:87], a[48:51], v[140:155]\n"
"ds_read_b128 a[12:15], v24 offset:22528\n"
"v_cos_f32 v212, v179\n"
"v_fract_f32 v177, v177\n"
"v_mfma_f32_32x32x16_bf16 v[160:175], a[84:87], a[68:71], v[160:175]\n"
"ds_read_b128 a[32:35], v24 offset:23552\n"
"v_fract_f32 v217, v217\n"
"v_sin_f32 v213, v179\n"
"v_cos_f32 v210, v177\n"
"buffer_load_dwordx4 v[44:47], v156, s[48:51], 0 offen offset:512 sc1\n"
"s_waitcnt lgkmcnt(5)\n"
"v_mfma_f32_32x32x16_bf16 v[60:75], a[80:83], a[64:67], v[60:75]\n"
"ds_read_b128 a[72:75], v51 offset:28672\n"
"ds_read_b128 a[40:43], v25 offset:20480\n"
"v_mfma_f32_32x32x16_bf16 v[0:15], a[80:83], a[56:59], v[0:15]\n"
"ds_read_b128 a[48:51], v25 offset:21504\n"
"v_cos_f32 v208, v217\n"
"s_add_u32 m0, s79, 24576\n"
"v_mul_f32 v212, v178, v212\n"
"buffer_load_dwordx4 v251, s[36:39], s64 offen lds\n"
"v_mfma_f32_32x32x16_bf16 v[140:155], a[80:83], a[60:63], v[140:155]\n"
"ds_read_b128 a[56:59], v25 offset:22528\n"
"v_sin_f32 v211, v177\n"
"v_mfma_f32_32x32x16_bf16 v[160:175], a[80:83], a[52:55], v[160:175]\n"
"ds_read_b128 a[60:63], v25 offset:23552\n"
"v_sin_f32 v209, v217\n"
"v_mul_f32 v213, v178, v213\n"
"buffer_load_dwordx4 v252, s[36:39], s64 offen offset:1024 lds\n"
"s_waitcnt lgkmcnt(5)\n"
"v_mfma_f32_32x32x16_bf16 v[60:75], a[76:79], a[44:47], v[60:75]\n"
"s_waitcnt vmcnt(14)\n"
"s_barrier\n"
"v_mfma_f32_32x32x16_bf16 v[0:15], a[76:79], a[36:39], v[0:15]\n"
"ds_read_b128 a[68:71], v48 offset:32768\n"
"ds_read_b128 a[44:47], v22\n"
"v_mul_f32 v210, v176, v210\n"
"v_mul_f32 v208, v216, v208\n"
"v_mul_f32 v211, v176, v211\n"
"buffer_load_dwordx4 v[176:179], v156, s[48:51], s90 offen offset:512 sc1\n"
"v_mfma_f32_32x32x16_bf16 v[140:155], a[76:79], a[12:15], v[140:155]\n"
"ds_read_b128 a[52:55], v22 offset:1024\n"
"ds_read_b128 a[36:39], v22 offset:2048\n"
"v_mul_f32 v209, v216, v209\n"
"v_cndmask_b32 v216, 1.0, v214, s[20:21]  \n"
"v_mfma_f32_32x32x16_bf16 v[160:175], a[76:79], a[32:35], v[160:175]\n"
"ds_read_b128 a[12:15], v22 offset:3072\n"
"v_cndmask_b32 v217, 0, v215, s[20:21]  \n"
"v_cmp_lt_u32 vcc, 31, v255\n"
"s_nop 1\n"
"s_nop 0\n"
"v_cndmask_b32 v216, 0, v216, vcc\n"
"v_cndmask_b32 v217, 0, v217, vcc\n"
"buffer_load_dwordx4 v253, s[36:39], s64 offen offset:2048 lds\n"
"s_waitcnt lgkmcnt(5)\n"
"v_mfma_f32_32x32x16_bf16 v[60:75], a[72:75], a[40:43], v[60:75]\n"
"ds_read_b128 a[64:67], v49 offset:32768\n"
"ds_read_b128 a[24:27], v23\n"
"v_mfma_f32_32x32x16_bf16 v[0:15], a[72:75], a[48:51], v[0:15]\n"
"ds_read_b128 a[8:11], v23 offset:1024\n"
"v_mfma_f32_32x32x16_bf16 v[140:155], a[72:75], a[56:59], v[140:155]\n"
"ds_read_b128 a[200:203], v23 offset:2048\n"
"buffer_load_dwordx4 v[100:103], v157, s[48:51], 0 offen sc1\n"
"v_mfma_f32_32x32x16_bf16 v[160:175], a[72:75], a[60:63], v[160:175]\n"
"ds_read_b128 a[28:31], v23 offset:3072\n"
"s_waitcnt lgkmcnt(5)\n"
"v_mfma_f32_32x32x16_bf16 v[60:75], a[68:71], a[44:47], v[60:75]\n"
"ds_read_b128 a[60:63], v50 offset:32768\n"
"ds_read_b128 a[56:59], v24\n"
"buffer_load_dwordx4 v254, s[36:39], s64 offen offset:3072 lds\n"
"v_mfma_f32_32x32x16_bf16 v[0:15], a[68:71], a[52:55], v[0:15]\n"
"ds_read_b128 a[16:19], v24 offset:1024\n"
"v_mfma_f32_32x32x16_bf16 v[140:155], a[68:71], a[36:39], v[140:155]\n"
"s_add_u32 m0, s79, 8192\n"
"ds_read_b128 a[40:43], v24 offset:2048\n"
"buffer_load_dwordx4 v80, s[40:43], s64 offen lds\n"
"v_mfma_f32_32x32x16_bf16 v[160:175], a[68:71], a[12:15], v[160:175]\n"
"ds_read_b128 a[52:55], v24 offset:3072\n"
"s_waitcnt lgkmcnt(5)\n"
"v_mfma_f32_32x32x16_bf16 v[60:75], a[64:67], a[24:27], v[60:75]\n"
"ds_read_b128 a[48:51], v51 offset:32768\n"
"ds_read_b128 a[32:35], v25\n"
"buffer_load_dwordx4 v80, s[52:55], s64 offen offset:1024 lds\n"
"v_mfma_f32_32x32x16_bf16 v[0:15], a[64:67], a[8:11], v[0:15]\n"
"ds_read_b128 a[36:39], v25 offset:1024\n"
"v_mfma_f32_32x32x16_bf16 v[140:155], a[64:67], a[200:203], v[140:155]\n"
"ds_read_b128 a[44:47], v25 offset:2048\n"
"v_mfma_f32_32x32x16_bf16 v[160:175], a[64:67], a[28:31], v[160:175]\n"
"ds_read_b128 a[12:15], v25 offset:3072\n"
"buffer_load_dwordx4 v82, s[40:43], s64 offen offset:2048 lds\n"
"buffer_load_dwordx4 v82, s[52:55], s64 offen offset:3072 lds\n"
"s_add_u32 m0, s79, 28672\n"
"s_nop 0\n"
"buffer_load_dwordx4 v251, s[36:39], s65 offen lds\n"
"buffer_load_dwordx4 v252, s[36:39], s65 offen offset:1024 lds\n"
"s_waitcnt lgkmcnt(5)\n"
"v_mfma_f32_32x32x16_bf16 v[60:75], a[60:63], a[56:59], v[60:75]\n"
"s_waitcnt vmcnt(13)\n"
"s_barrier\n"
"buffer_load_dwordx4 v253, s[36:39], s65 offen offset:2048 lds\n"
"v_mfma_f32_32x32x16_bf16 v[0:15], a[60:63], a[16:19], v[0:15]\n"
"ds_read_b128 a[56:59], v48 offset:36864\n"
"ds_read_b128 a[168:171], v22 offset:4096\n"
"v_mfma_f32_32x32x16_bf16 v[140:155], a[60:63], a[40:43], v[140:155]\n"
"ds_read_b128 a[28:31], v22 offset:5120\n"
"ds_read_b128 a[16:19], v22 offset:6144\n"
"buffer_load_dwordx4 v254, s[36:39], s65 offen offset:3072 lds\n"
"v_mfma_f32_32x32x16_bf16 v[160:175], a[60:63], a[52:55], v[160:175]\n"
"ds_read_b128 a[24:27], v22 offset:7168\n"
"s_waitcnt lgkmcnt(5)\n"
"v_mfma_f32_32x32x16_bf16 v[60:75], a[48:51], a[32:35], v[60:75]\n"
"ds_read_b128 a[40:43], v49 offset:36864\n"
"s_add_u32 m0, s79, 12288\n"
"ds_read_b128 a[20:23], v23 offset:4096\n"
"buffer_load_dwordx4 v80, s[40:43], s65 offen lds\n"
"v_mfma_f32_32x32x16_bf16 v[0:15], a[48:51], a[36:39], v[0:15]\n"
"ds_read_b128 a[204:207], v23 offset:5120\n"
"v_mfma_f32_32x32x16_bf16 v[140:155], a[48:51], a[44:47], v[140:155]\n"
"ds_read_b128 a[8:11], v23 offset:6144\n"
"buffer_load_dwordx4 v80, s[52:55], s65 offen offset:1024 lds\n"
"v_mfma_f32_32x32x16_bf16 v[160:175], a[48:51], a[12:15], v[160:175]\n"
"ds_read_b128 a[200:203], v23 offset:7168\n"
"s_waitcnt lgkmcnt(5)\n"
"v_mfma_f32_32x32x16_bf16 v[60:75], a[56:59], a[168:171], v[60:75]\n"
"ds_read_b128 a[32:35], v50 offset:36864\n"
"ds_read_b128 a[52:55], v24 offset:4096\n"
"buffer_load_dwordx4 v82, s[40:43], s65 offen offset:2048 lds\n"
"v_mfma_f32_32x32x16_bf16 v[0:15], a[56:59], a[28:31], v[0:15]\n"
"ds_read_b128 a[12:15], v24 offset:5120\n"
"v_mfma_f32_32x32x16_bf16 v[140:155], a[56:59], a[16:19], v[140:155]\n"
"ds_read_b128 a[44:47], v24 offset:6144\n"
"v_mfma_f32_32x32x16_bf16 v[160:175], a[56:59], a[24:27], v[160:175]\n"
"ds_read_b128 a[36:39], v24 offset:7168\n"
"buffer_load_dwordx4 v82, s[52:55], s65 offen offset:3072 lds\n"
"s_waitcnt lgkmcnt(5)\n"
"v_mfma_f32_32x32x16_bf16 v[60:75], a[40:43], a[20:23], v[60:75]\n"
"ds_read_b128 a[16:19], v51 offset:36864\n"
"ds_read_b128 a[24:27], v25 offset:4096\n"
"v_mfma_f32_32x32x16_bf16 v[0:15], a[40:43], a[204:207], v[0:15]\n"
"ds_read_b128 a[28:31], v25 offset:5120\n"
"s_cmp_lt_u32 s76, 2\n"
"s_cselect_b32 s40, s40, s52\n"
"s_cselect_b32 s41, s41, s53\n"
"s_cselect_b32 s42, s42, s54\n"
"s_add_u32 m0, s79, 16384\n"
"s_nop 0\n"
"buffer_load_dwordx4 v84, s[40:43], s56 offen lds\n"
"v_mfma_f32_32x32x16_bf16 v[140:155], a[40:43], a[8:11], v[140:155]\n"
"ds_read_b128 a[20:23], v25 offset:6144\n"
"s_add_u32 s52, s34, 262144\n"
"s_addc_u32 s53, s35, 0\n"
"s_and_b32 s53, s53, 0xffff\n"
"v_mfma_f32_32x32x16_bf16 v[160:175], a[40:43], a[200:203], v[160:175]\n"
"s_add_u32 m0, s79, 17408\n"
"ds_read_b128 a[8:11], v25 offset:7168\n"
"buffer_load_dwordx4 v84, s[40:43], s57 offen lds\n"
"s_mov_b32 s54, 163840\n"
"v_xor_b32 v250, s8, v37\n"
"v_bitop3_b32 v250, v39, s9, v250 bitop3:0xbe\n"
"v_bitop3_b32 v250, v41, s8, v250 bitop3:0xbe\n"
"v_bitop3_b32 v250, v43, s9, v250 bitop3:0xbe\n"
"s_waitcnt vmcnt(20)\n"
"v_bitop3_b32 v250, v45, s8, v250 bitop3:0xbe\n"
"v_bitop3_b32 v250, v47, s9, v250 bitop3:0xbe\n"
"s_waitcnt vmcnt(17)\n"
"v_bitop3_b32 v250, v177, s8, v250 bitop3:0xbe\n"
"v_bitop3_b32 v250, v179, s9, v250 bitop3:0xbe\n"
"s_waitcnt vmcnt(15)\n"
"v_bitop3_b32 v250, v101, s8, v250 bitop3:0xbe\n"
"s_add_u32 m0, s79, 18432\n"
"v_bitop3_b32 v250, v103, s9, v250 bitop3:0xbe\n"
"buffer_load_dwordx4 v84, s[40:43], s58 offen lds\n"
"v_cmp_ne_u32 vcc, 0, v250\n"
"s_nop 1\n"
"s_cmp_lg_u64 vcc, 0\n"
"s_cbranch_scc1 SLOWBC_%=\n"
"BACKBC_%=:\n"
"s_add_u32 m0, s79, 19456\n"
"s_nop 0\n"
"buffer_load_dwordx4 v84, s[40:43], s59 offen lds\n"
"s_add_u32 m0, s79, 20480\n"
"s_nop 0\n"
"buffer_load_dwordx4 v84, s[40:43], s60 offen lds\n"
"s_waitcnt lgkmcnt(5)\n"
"v_mfma_f32_32x32x16_bf16 v[60:75], a[32:35], a[52:55], v[60:75]\n"
"s_waitcnt vmcnt(13)\n"
"s_barrier\n"
"s_add_u32 m0, s79, 21504\n"
"s_nop 0\n"
"buffer_load_dwordx4 v84, s[40:43], s61 offen lds\n"
"v_mfma_f32_32x32x16_bf16 v[0:15], a[32:35], a[12:15], v[0:15]\n"
"ds_read_b128 a[52:55], v48 offset:24576\n"
"ds_read_b128 a[212:215], v22 offset:8192\n"
"v_mfma_f32_32x32x16_bf16 v[140:155], a[32:35], a[44:47], v[140:155]\n"
"ds_read_b128 a[168:171], v22 offset:9216\n"
"s_add_u32 m0, s79, 22528\n"
"ds_read_b128 a[12:15], v22 offset:10240\n"
"buffer_load_dwordx4 v84, s[40:43], s62 offen lds\n"
"v_mfma_f32_32x32x16_bf16 v[160:175], a[32:35], a[36:39], v[160:175]\n"
"ds_read_b128 a[200:203], v22 offset:11264\n"
"s_waitcnt lgkmcnt(5)\n"
"v_mfma_f32_32x32x16_bf16 v[60:75], a[16:19], a[24:27], v[60:75]\n"
"ds_read_b128 a[36:39], v49 offset:24576\n"
"s_add_u32 m0, s79, 23552\n"
"ds_read_b128 a[204:207], v23 offset:8192\n"
"buffer_load_dwordx4 v84, s[40:43], s63 offen lds\n"
"v_mfma_f32_32x32x16_bf16 v[0:15], a[16:19], a[28:31], v[0:15]\n"
"ds_read_b128 a[208:211], v23 offset:9216\n"
"v_mfma_f32_32x32x16_bf16 v[140:155], a[16:19], a[20:23], v[140:155]\n"
"s_add_u32 m0, s79, 0\n"
"ds_read_b128 a[192:195], v23 offset:10240\n"
"buffer_load_dwordx4 v84, s[40:43], s64 offen lds\n"
"v_mfma_f32_32x32x16_bf16 v[160:175], a[16:19], a[8:11], v[160:175]\n"
"ds_read_b128 a[196:199], v23 offset:11264\n"
"s_waitcnt lgkmcnt(5)\n"
"v_mfma_f32_32x32x16_bf16 v[60:75], a[52:55], a[212:215], v[60:75]\n"
"ds_read_b128 a[28:31], v50 offset:24576\n"
"s_add_u32 m0, s79, 1024\n"
"ds_read_b128 a[44:47], v24 offset:8192\n"
"buffer_load_dwordx4 v84, s[40:43], s65 offen lds\n"
"v_mfma_f32_32x32x16_bf16 v[0:15], a[52:55], a[168:171], v[0:15]\n"
"ds_read_b128 a[24:27], v24 offset:9216\n"
"v_xor_b32 v80, s80, v77\n"
"v_bitop3_b32 v80, v79, s81, v80 bitop3:0xbe\n"
"v_cmp_ne_u32 vcc, 0, v80\n"
"s_nop 1\n"
"s_cmp_lg_u64 vcc, 0\n"
"s_cbranch_scc1 SLOWBS_%=\n"
"BACKBS_%=:\n"
"v_mfma_f32_32x32x16_bf16 v[140:155], a[52:55], a[12:15], v[140:155]\n"
"ds_read_b128 a[8:11], v24 offset:10240\n"
"v_and_b32 v80, s82, v76\n"
"v_lshlrev_b32 v82, 16, v76\n"
"v_rcp_f32 v81, v80\n"
"v_mfma_f32_32x32x16_bf16 v[160:175], a[52:55], a[200:203], v[160:175]\n"
"ds_read_b128 a[12:15], v24 offset:11264\n"
"ds_write_b32 v200, v76 offset:40704\n"
"v_lshrrev_b32 v158, 16, v82\n"
"s_waitcnt lgkmcnt(6)\n"
"v_mfma_f32_32x32x16_bf16 v[60:75], a[36:39], a[204:207], v[60:75]\n"
"ds_read_b128 a[20:23], v51 offset:24576\n"
"ds_read_b128 a[200:203], v25 offset:8192\n"
"v_mul_f32 v250, v80, v80\n"
"ds_write_b32 v200, v81 offset:40448\n"
"v_mfma_f32_32x32x16_bf16 v[0:15], a[36:39], a[208:211], v[0:15]\n"
"ds_read_b128 a[204:207], v25 offset:9216\n"
"v_or_b32 v156, v82, v158\n"
"v_mov_b32 v157, 0\n"
"v_mov_b32 v159, 0\n"
"v_mfma_f32_32x32x16_bf16 v[140:155], a[36:39], a[192:195], v[140:155]\n"
"ds_read_b128 a[208:211], v25 offset:10240\n"
"v_mul_f32 v80, v81, v81\n"
"v_mul_f32 v252, 0xc0135761, v81\n"
"v_mul_f32 v80, v80, v252\n"
"v_mul_f32 v200, 0x3d372713, v80\n"
"v_mfma_f32_32x32x16_bf16 v[160:175], a[36:39], a[196:199], v[160:175]\n"
"ds_read_b128 a[192:195], v25 offset:11264\n"
"ds_read_b128 v[76:79], v52 offset:40448\n"
"ds_read_b128 v[80:83], v52 offset:40464\n"
"ds_read_b128 v[84:87], v52 offset:40480\n"
"ds_read_b128 v[88:91], v52 offset:40496\n"
"v_mov_b32 v105, s80\n"
"v_mov_b32 v107, s81\n"
"v_add_u32 v92, 10240, v233\n"
"v_cndmask_b32 v92, v92, -16, s[18:19]  \n"
"s_waitcnt lgkmcnt(11)\n"
"v_mfma_f32_32x32x16_bf16 v[60:75], a[28:31], a[44:47], v[60:75]\n"
"s_waitcnt vmcnt(10)\n"
"s_barrier\n"
"v_mfma_f32_32x32x16_bf16 v[0:15], a[28:31], a[24:27], v[0:15]\n"
"ds_read_b128 a[44:47], v48 offset:28672\n"
"s_add_u32 m0, s79, 4096\n"
"ds_read_b128 a[212:215], v22 offset:12288\n"
"buffer_load_dwordx4 v225, s[44:47], s66 offen lds\n"
"v_mfma_f32_32x32x16_bf16 v[140:155], a[28:31], a[8:11], v[140:155]\n"
"ds_read_b128 a[172:175], v22 offset:13312\n"
"ds_read_b128 a[168:171], v22 offset:14336\n"
"buffer_load_dwordx4 v226, s[44:47], s66 offen offset:1024 lds\n"
"v_mfma_f32_32x32x16_bf16 v[160:175], a[28:31], a[12:15], v[160:175]\n"
"ds_read_b128 a[8:11], v22 offset:15360\n"
"s_waitcnt lgkmcnt(9)\n"
"v_mfma_f32_32x32x16_bf16 v[60:75], a[20:23], a[200:203], v[60:75]\n"
"ds_read_b128 a[24:27], v49 offset:28672\n"
"ds_read_b128 a[184:187], v23 offset:12288\n"
"buffer_load_dwordx4 v227, s[44:47], s66 offen offset:2048 lds\n"
"v_mfma_f32_32x32x16_bf16 v[0:15], a[20:23], a[204:207], v[0:15]\n"
"ds_read_b128 a[188:191], v23 offset:13312\n"
"v_mfma_f32_32x32x16_bf16 v[140:155], a[20:23], a[208:211], v[140:155]\n"
"ds_read_b128 a[204:207], v23 offset:14336\n"
"v_mfma_f32_32x32x16_bf16 v[160:175], a[20:23], a[192:195], v[160:175]\n"
"ds_read_b128 a[208:211], v23 offset:15360\n"
"buffer_load_dwordx4 v228, s[44:47], s66 offen offset:3072 lds\n"
"s_waitcnt lgkmcnt(5)\n"
"v_mfma_f32_32x32x16_bf16 v[60:75], a[44:47], a[212:215], v[60:75]\n"
"ds_read_b128 a[12:15], v50 offset:28672\n"
"ds_read_b128 a[196:199], v24 offset:12288\n"
"v_mfma_f32_32x32x16_bf16 v[0:15], a[44:47], a[172:175], v[0:15]\n"
"s_add_u32 m0, s79, 24576\n"
"ds_read_b128 a[192:195], v24 offset:13312\n"
"global_load_lds_dwordx4 v53, s[30:31]\n"
"v_add_u32 v53, 9216, v53\n"
"v_mfma_f32_32x32x16_bf16 v[140:155], a[44:47], a[168:171], v[140:155]\n"
"ds_read_b128 a[200:203], v24 offset:14336\n"
"v_mfma_f32_32x32x16_bf16 v[160:175], a[44:47], a[8:11], v[160:175]\n"
"ds_read_b128 a[168:171], v24 offset:15360\n"
"global_load_lds_dwordx4 v53, s[30:31] offset:1024\n"
"v_add_u32 v53, 9216, v53\n"
"s_waitcnt lgkmcnt(5)\n"
"v_mfma_f32_32x32x16_bf16 v[60:75], a[24:27], a[184:187], v[60:75]\n"
"ds_read_b128 a[8:11], v51 offset:28672\n"
"ds_read_b128 a[212:215], v25 offset:12288\n"
"v_mfma_f32_32x32x16_bf16 v[0:15], a[24:27], a[188:191], v[0:15]\n"
"ds_read_b128 a[172:175], v25 offset:13312\n"
"global_load_lds_dwordx4 v53, s[30:31] offset:2048\n"
"v_add_u32 v53, 9216, v53\n"
"v_mfma_f32_32x32x16_bf16 v[140:155], a[24:27], a[204:207], v[140:155]\n"
"ds_read_b128 a[176:179], v25 offset:14336\n"
"v_mfma_f32_32x32x16_bf16 v[160:175], a[24:27], a[208:211], v[160:175]\n"
"ds_read_b128 a[180:183], v25 offset:15360\n"
"global_load_lds_dwordx4 v53, s[30:31] offset:3072\n"
"s_waitcnt lgkmcnt(5)\n"
"v_mfma_f32_32x32x16_bf16 v[60:75], a[12:15], a[196:199], v[60:75]\n"
"s_waitcnt vmcnt(14)\n"
"s_barrier\n"
"v_mfma_f32_32x32x16_bf16 v[0:15], a[12:15], a[192:195], v[0:15]\n"
"ds_read_b128 a[184:187], v22 offset:16384\n"
"s_add_u32 m0, s79, 8192\n"
"ds_read_b128 a[188:191], v23 offset:16384\n"
"buffer_load_dwordx4 v225, s[44:47], s67 offen lds\n"
"v_mfma_f32_32x32x16_bf16 v[140:155], a[12:15], a[200:203], v[140:155]\n"
"ds_read_b128 a[192:195], v24 offset:16384\n"
"v_mfma_f32_32x32x16_bf16 v[160:175], a[12:15], a[168:171], v[160:175]\n"
"ds_read_b128 a[196:199], v25 offset:16384\n"
"buffer_load_dwordx4 v226, s[44:47], s67 offen offset:1024 lds\n"
"s_waitcnt lgkmcnt(4)\n"
"v_mfma_f32_32x32x16_bf16 v[60:75], a[8:11], a[212:215], v[60:75]\n"
"ds_read_b128 a[200:203], v22 offset:17408\n"
"v_mfma_f32_32x32x16_bf16 v[0:15], a[8:11], a[172:175], v[0:15]\n"
"ds_read_b128 a[204:207], v23 offset:17408\n"
"buffer_load_dwordx4 v227, s[44:47], s67 offen offset:2048 lds\n"
"v_mfma_f32_32x32x16_bf16 v[140:155], a[8:11], a[176:179], v[140:155]\n"
"ds_read_b128 a[208:211], v24 offset:17408\n"
"v_mfma_f32_32x32x16_bf16 v[160:175], a[8:11], a[180:183], v[160:175]\n"
"ds_read_b128 a[168:171], v25 offset:17408\n"
"buffer_load_dwordx4 v228, s[44:47], s67 offen offset:3072 lds\n"
"v_mfma_f32_32x32x16_bf16 v[60:75], v[156:159], v[36:39], v[60:75]\n"
"v_mfma_f32_32x32x16_bf16 v[0:15], v[156:159], v[40:43], v[0:15]\n"
"v_mfma_f32_32x32x16_bf16 v[140:155], v[156:159], v[44:47], v[140:155]\n"
"v_mfma_f32_32x32x16_bf16 v[160:175], v[156:159], v[176:179], v[160:175]\n"
"s_waitcnt lgkmcnt(4)\n"
"v_mfma_f32_32x32x16_bf16 v[36:51], a[160:163], a[184:187], 0\n"
"ds_read_b128 a[212:215], v22 offset:18432\n"
"v_mfma_f32_32x32x16_bf16 v[36:51], a[128:131], a[188:191], v[36:51]\n"
"ds_read_b128 a[184:187], v23 offset:18432\n"
"s_nop 3\n"
"v_mul_f32 v60, v76, v60\n"
"s_nop 0\n"
"v_mul_f32 v140, v76, v140\n"
"v_mul_f32 v0, v76, v0\n"
"v_mul_f32 v160, v76, v160\n"
"v_mul_f32 v61, v77, v61\n"
"v_mul_f32 v141, v77, v141\n"
"v_mul_f32 v1, v77, v1\n"
"v_mul_f32 v161, v77, v161\n"
"v_fma_f32 v54, -v187, v0, v61\n"
"v_fma_f32 v110, -v203, v160, v141\n"
"v_fma_f32 v55, v187, v60, v1\n"
"v_fma_f32 v111, v203, v140, v161\n"
"v_mul_f32 v62, v78, v62\n"
"v_mul_f32 v142, v78, v142\n"
"v_mul_f32 v2, v78, v2\n"
"v_mfma_f32_32x32x16_bf16 v[36:51], a[116:119], a[192:195], v[36:51]\n"
"ds_read_b128 a[188:191], v24 offset:18432\n"
"v_mul_f32 v162, v78, v162\n"
"v_fmac_f32 v54, v186, v60\n"
"v_fmac_f32 v110, v202, v140\n"
"v_fmac_f32 v55, v186, v0\n"
"v_fmac_f32 v111, v202, v160\n"
"v_fma_f32 v52, -v187, v55, v62\n"
"v_fma_f32 v108, -v203, v111, v142\n"
"v_fma_f32 v53, v187, v54, v2\n"
"v_fma_f32 v109, v203, v110, v162\n"
"v_mul_f32 v63, v79, v63\n"
"v_mul_f32 v143, v79, v143\n"
"v_mul_f32 v3, v79, v3\n"
"v_mul_f32 v163, v79, v163\n"
"v_fmac_f32 v52, v186, v54\n"
"v_fmac_f32 v108, v202, v110\n"
"v_mfma_f32_32x32x16_bf16 v[36:51], a[108:111], a[196:199], v[36:51]\n"
"ds_read_b128 a[192:195], v25 offset:18432\n"
"v_fmac_f32 v53, v186, v55\n"
"v_fmac_f32 v109, v202, v111\n"
"v_fma_f32 v54, -v187, v53, v63\n"
"v_fma_f32 v110, -v203, v109, v143\n"
"v_fma_f32 v55, v187, v52, v3\n"
"v_fma_f32 v111, v203, v108, v163\n"
"v_mul_f32 v64, v80, v64\n"
"v_mul_f32 v144, v80, v144\n"
"v_mul_f32 v4, v80, v4\n"
"v_mul_f32 v164, v80, v164\n"
"v_fmac_f32 v54, v186, v52\n"
"v_fmac_f32 v110, v202, v108\n"
"v_fmac_f32 v55, v186, v53\n"
"v_fmac_f32 v111, v202, v109\n"
"v_fma_f32 v52, -v187, v55, v64\n"
"s_waitcnt lgkmcnt(4)\n"
"v_mfma_f32_32x32x16_bf16 v[36:51], a[136:139], a[200:203], v[36:51]\n"
"ds_read_b128 a[196:199], v22 offset:19456\n"
"v_fma_f32 v108, -v203, v111, v144\n"
"v_fma_f32 v53, v187, v54, v4\n"
"v_fma_f32 v109, v203, v110, v164\n"
"v_mul_f32 v65, v81, v65\n"
"v_mul_f32 v145, v81, v145\n"
"v_mul_f32 v5, v81, v5\n"
"v_mul_f32 v165, v81, v165\n"
"v_fmac_f32 v52, v186, v54\n"
"v_fmac_f32 v108, v202, v110\n"
"v_fmac_f32 v53, v186, v55\n"
"v_fmac_f32 v109, v202, v111\n"
"v_fma_f32 v54, -v187, v53, v65\n"
"v_fma_f32 v110, -v203, v109, v145\n"
"v_fma_f32 v55, v187, v52, v5\n"
"v_mfma_f32_32x32x16_bf16 v[36:51], a[140:143], a[204:207], v[36:51]\n"
"ds_read_b128 a[200:203], v23 offset:19456\n"
"v_fma_f32 v111, v203, v108, v165\n"
"v_mul_f32 v66, v82, v66\n"
"v_mul_f32 v146, v82, v146\n"
"v_mul_f32 v6, v82, v6\n"
"v_mul_f32 v166, v82, v166\n"
"v_fmac_f32 v54, v186, v52\n"
"v_fmac_f32 v110, v202, v108\n"
"v_fmac_f32 v55, v186, v53\n"
"v_fmac_f32 v111, v202, v109\n"
"v_fma_f32 v52, -v187, v55, v66\n"
"v_fma_f32 v108, -v203, v111, v146\n"
"v_fma_f32 v53, v187, v54, v6\n"
"v_fma_f32 v109, v203, v110, v166\n"
"v_mul_f32 v67, v83, v67\n"
"v_mul_f32 v147, v83, v147\n"
"v_mfma_f32_32x32x16_bf16 v[36:51], a[148:151], a[208:211], v[36:51]\n"
"ds_read_b128 a[204:207], v24 offset:19456\n"
"v_mul_f32 v7, v83, v7\n"
"v_mul_f32 v167, v83, v167\n"
"v_fmac_f32 v52, v186, v54\n"
"v_fmac_f32 v108, v202, v110\n"
"v_fmac_f32 v53, v186, v55\n"
"v_fmac_f32 v109, v202, v111\n"
"v_fma_f32 v54, -v187, v53, v67\n"
"v_fma_f32 v110, -v203, v109, v147\n"
"v_fma_f32 v55, v187, v52, v7\n"
"v_fma_f32 v111, v203, v108, v167\n"
"v_mul_f32 v68, v84, v68\n"
"v_mul_f32 v148, v84, v148\n"
"v_mul_f32 v8, v84, v8\n"
"v_mul_f32 v168, v84, v168\n"
"v_fmac_f32 v54, v186, v52\n"
"v_mfma_f32_32x32x16_bf16 v[36:51], a[152:155], a[168:171], v[36:51]\n"
"ds_read_b128 a[208:211], v25 offset:19456\n"
"v_fmac_f32 v110, v202, v108\n"
"v_fmac_f32 v55, v186, v53\n"
"v_fmac_f32 v111, v202, v109\n"
"v_fma_f32 v52, -v187, v55, v68\n"
"v_fma_f32 v108, -v203, v111, v148\n"
"s_waitcnt vmcnt(4)\n"
"ds_read_b64_tr_b16 a[168:169], v180 offset:24576\n"
"ds_read_b64_tr_b16 a[170:171], v180 offset:25600\n"
"ds_read_b64_tr_b16 a[172:173], v180 offset:24608\n"
"ds_read_b64_tr_b16 a[174:175], v180 offset:25632\n"
"ds_read_b64_tr_b16 a[176:177], v180 offset:24640\n"
"ds_read_b64_tr_b16 a[178:179], v180 offset:25664\n"
"ds_read_b64_tr_b16 a[180:181], v180 offset:24672\n"
"ds_read_b64_tr_b16 a[182:183], v180 offset:25696\n"
"s_waitcnt lgkmcnt(12)\n"
"v_mfma_f32_32x32x16_bf16 v[36:51], a[156:159], a[212:215], v[36:51]\n"
"s_barrier\n"
"v_fma_f32 v53, v187, v54, v8\n"
"v_fma_f32 v109, v203, v110, v168\n"
"v_mul_f32 v69, v85, v69\n"
"v_mul_f32 v149, v85, v149\n"
"v_mul_f32 v9, v85, v9\n"
"v_mul_f32 v169, v85, v169\n"
"v_fmac_f32 v52, v186, v54\n"
"v_fmac_f32 v108, v202, v110\n"
"v_fmac_f32 v53, v186, v55\n"
"v_fmac_f32 v109, v202, v111\n"
"v_fma_f32 v54, -v187, v53, v69\n"
"v_mfma_f32_32x32x16_bf16 v[36:51], a[164:167], a[184:187], v[36:51]\n"
"ds_read_b128 a[216:219], v22 offset:20480\n"
"s_add_u32 m0, s79, 12288\n"
"ds_read_b128 a[212:215], v23 offset:20480\n"
"buffer_load_dwordx4 v225, s[44:47], s68 offen lds\n"
"v_fma_f32 v110, -v203, v109, v149\n"
"v_fma_f32 v55, v187, v52, v9\n"
"v_fma_f32 v111, v203, v108, v169\n"
"v_mul_f32 v70, v86, v70\n"
"buffer_load_dwordx4 v226, s[44:47], s68 offen offset:1024 lds\n"
"v_mul_f32 v150, v86, v150\n"
"v_mul_f32 v10, v86, v10\n"
"v_mul_f32 v170, v86, v170\n"
"v_mfma_f32_32x32x16_bf16 v[36:51], a[144:147], a[188:191], v[36:51]\n"
"ds_read_b128 a[184:187], v24 offset:20480\n"
"v_fmac_f32 v54, v186, v52\n"
"v_fmac_f32 v110, v202, v108\n"
"v_fmac_f32 v55, v186, v53\n"
"v_fmac_f32 v111, v202, v109\n"
"v_fma_f32 v52, -v187, v55, v70\n"
"v_fma_f32 v108, -v203, v111, v150\n"
"v_fma_f32 v53, v187, v54, v10\n"
"buffer_load_dwordx4 v227, s[44:47], s68 offen offset:2048 lds\n"
"v_fma_f32 v109, v203, v110, v170\n"
"v_mul_f32 v71, v87, v71\n"
"v_mul_f32 v151, v87, v151\n"
"v_mul_f32 v11, v87, v11\n"
"v_mul_f32 v171, v87, v171\n"
"v_mfma_f32_32x32x16_bf16 v[36:51], a[124:127], a[192:195], v[36:51]\n"
"ds_read_b128 a[188:191], v25 offset:20480\n"
"v_fmac_f32 v52, v186, v54\n"
"v_fmac_f32 v108, v202, v110\n"
"v_fmac_f32 v53, v186, v55\n"
"v_fmac_f32 v109, v202, v111\n"
"buffer_load_dwordx4 v228, s[44:47], s68 offen offset:3072 lds\n"
"v_fma_f32 v54, -v187, v53, v71\n"
"v_fma_f32 v110, -v203, v109, v151\n"
"v_fma_f32 v55, v187, v52, v11\n"
"v_fma_f32 v111, v203, v108, v171\n"
"v_mul_f32 v72, v88, v72\n"
"v_mul_f32 v152, v88, v152\n"
"v_mul_f32 v12, v88, v12\n"
"v_mul_f32 v172, v88, v172\n"
"s_waitcnt lgkmcnt(12)\n"
"v_mfma_f32_32x32x16_bf16 v[36:51], a[132:135], a[196:199], v[36:51]\n"
"ds_read_b128 a[192:195], v22 offset:21504\n"
"v_fmac_f32 v54, v186, v52\n"
"v_fmac_f32 v110, v202, v108\n"
"v_fmac_f32 v55, v186, v53\n"
"v_fmac_f32 v111, v202, v109\n"
"v_fma_f32 v52, -v187, v55, v72\n"
"v_fma_f32 v108, -v203, v111, v152\n"
"v_fma_f32 v53, v187, v54, v12\n"
"v_fma_f32 v109, v203, v110, v172\n"
"v_mul_f32 v73, v89, v73\n"
"v_mul_f32 v153, v89, v153\n"
"v_mul_f32 v13, v89, v13\n"
"v_mul_f32 v173, v89, v173\n"
"v_fmac_f32 v52, v186, v54\n"
"v_fmac_f32 v108, v202, v110\n"
"v_mfma_f32_32x32x16_bf16 v[36:51], a[120:123], a[200:203], v[36:51]\n"
"ds_read_b128 a[196:199], v23 offset:21504\n"
"v_fmac_f32 v53, v186, v55\n"
"v_fmac_f32 v109, v202, v111\n"
"v_fma_f32 v54, -v187, v53, v73\n"
"v_fma_f32 v110, -v203, v109, v153\n"
"v_fma_f32 v55, v187, v52, v13\n"
"v_fma_f32 v111, v203, v108, v173\n"
"v_mul_f32 v74, v90, v74\n"
"v_mul_f32 v154, v90, v154\n"
"v_mul_f32 v14, v90, v14\n"
"v_mul_f32 v174, v90, v174\n"
"v_fmac_f32 v54, v186, v52\n"
"v_fmac_f32 v110, v202, v108\n"
"v_fmac_f32 v55, v186, v53\n"
"v_fmac_f32 v111, v202, v109\n"
"v_fma_f32 v52, -v187, v55, v74\n"
"v_mfma_f32_32x32x16_bf16 v[36:51], a[112:115], a[204:207], v[36:51]\n"
"ds_read_b128 a[220:223], v24 offset:21504\n"
"v_fma_f32 v108, -v203, v111, v154\n"
"v_fma_f32 v53, v187, v54, v14\n"
"v_fma_f32 v109, v203, v110, v174\n"
"v_mul_f32 v75, v91, v75\n"
"v_mul_f32 v155, v91, v155\n"
"v_mul_f32 v15, v91, v15\n"
"v_mul_f32 v175, v91, v175\n"
"v_fmac_f32 v52, v186, v54\n"
"v_fmac_f32 v108, v202, v110\n"
"v_fmac_f32 v53, v186, v55\n"
"v_fmac_f32 v109, v202, v111\n"
"v_fma_f32 v54, -v187, v53, v75\n"
"v_fma_f32 v110, -v203, v109, v155\n"
"v_fma_f32 v55, v187, v52, v15\n"
"v_mfma_f32_32x32x16_bf16 v[36:51], a[104:107], a[208:211], v[36:51]\n"
"ds_read_b128 a[224:227], v25 offset:21504\n"
"v_fma_f32 v111, v203, v108, v175\n"
"v_fmac_f32 v54, v186, v52\n"
"v_fmac_f32 v110, v202, v108\n"
"v_fmac_f32 v55, v186, v53\n"
"v_fmac_f32 v111, v202, v109\n"
"ds_write_b64 v223, v[54:55] offset:35840\n"
"ds_write_b64 v223, v[110:111] offset:36352\n"
"ds_read_b128 v[176:179], v56 offset:40448\n"
"ds_read_b128 v[180:183], v56 offset:40464\n"
"s_waitcnt lgkmcnt(8)\n"
"v_mfma_f32_32x32x16_bf16 v[36:51], a[100:103], a[216:219], v[36:51]\n"
"ds_read_b128 a[228:231], v22 offset:22528\n"
"v_mfma_f32_32x32x16_bf16 v[36:51], a[96:99], a[212:215], v[36:51]\n"
"ds_read_b128 a[200:203], v23 offset:22528\n"
"s_waitcnt lgkmcnt(4)\n"
"s_barrier\n"
"ds_read_b128 v[54:57], v224\n"
"v_mfma_f32_32x32x16_bf16 v[36:51], a[92:95], a[184:187], v[36:51]\n"
"ds_read_b128 a[208:211], v24 offset:22528\n"
"v_mfma_f32_32x32x16_bf16 v[36:51], a[88:91], a[188:191], v[36:51]\n"
"ds_read_b128 a[204:207], v25 offset:22528\n"
"v_mfma_f32_32x32x16_bf16 v[36:51], a[84:87], a[192:195], v[36:51]\n"
"ds_read_b128 a[212:215], v22 offset:23552\n"
"v_mfma_f32_32x32x16_bf16 v[36:51], a[80:83], a[196:199], v[36:51]\n"
"ds_read_b128 a[216:219], v23 offset:23552\n"
"v_mfma_f32_32x32x16_bf16 v[36:51], a[76:79], a[220:223], v[36:51]\n"
"ds_read_b128 a[184:187], v24 offset:23552\n"
"s_waitcnt lgkmcnt(5)\n"
"v_fma_f32 v56, -v59, v55, v56\n"
"v_fmac_f32 v57, v59, v54\n"
"v_fmac_f32 v56, v58, v54\n"
"v_fmac_f32 v57, v58, v55\n"
"v_mov_b32 v110, v56\n"
"v_mov_b32 v111, v57\n"
"s_nop 0\n"
"v_permlane16_swap_b32 v56, v110\n"
"v_permlane16_swap_b32 v57, v111\n"
"v_fma_f32 v110, -v115, v57, v110\n"
"v_fmac_f32 v111, v115, v56\n"
"v_fmac_f32 v110, v114, v56\n"
"v_fmac_f32 v111, v114, v57\n"
"v_mov_b32 v104, v110\n"
"v_mfma_f32_32x32x16_bf16 v[36:51], a[72:75], a[224:227], v[36:51]\n"
"ds_read_b128 a[220:223], v25 offset:23552\n"
"v_mov_b32 v106, v111\n"
"s_nop 0\n"
"v_permlane32_swap_b32 v110, v104\n"
"v_permlane32_swap_b32 v111, v106\n"
"v_fma_f32 v104, -v113, v111, v104\n"
"v_fmac_f32 v106, v113, v110\n"
"v_fmac_f32 v104, v112, v110\n"
"v_fmac_f32 v106, v112, v111\n"
"buffer_store_dwordx4 v[104:107], v92, s[52:55], 0 offen sc1\n"
"s_waitcnt lgkmcnt(4)\n"
"v_mfma_f32_32x32x16_bf16 v[36:51], a[68:71], a[228:231], v[36:51]\n"
"s_barrier\n"
"buffer_load_dwordx2 v[92:93], v189, s[48:51], 0 offen sc1\n"
"buffer_store_dwordx4 a[168:171], v218, s[48:51], s10 offen sc1\n"
"buffer_store_dwordx4 a[172:175], v218, s[48:51], s11 offen sc1\n"
"buffer_store_dwordx4 a[176:179], v218, s[48:51], s12 offen sc1\n"
"buffer_store_dwordx4 a[180:183], v218, s[48:51], s13 offen sc1\n"
"v_mfma_f32_32x32x16_bf16 v[36:51], a[64:67], a[200:203], v[36:51]\n"
"ds_read_b128 a[172:175], v22\n"
"ds_read_b128 a[180:183], v23\n"
"v_cndmask_b32 v52, 0, v56, s[20:21]  \n"
"v_cndmask_b32 v53, 0, v57, s[20:21]  \n"
"v_fma_f32 v52, -v21, v111, v52\n"
"v_fmac_f32 v53, v21, v110\n"
"v_fmac_f32 v52, v20, v110\n"
"v_fmac_f32 v53, v20, v111\n"
"v_mfma_f32_32x32x16_bf16 v[36:51], a[60:63], a[208:211], v[36:51]\n"
"ds_read_b128 a[224:227], v24\n"
"v_mfma_f32_32x32x16_bf16 v[36:51], a[48:51], a[204:207], v[36:51]\n"
"s_add_u32 m0, s79, 16384\n"
"ds_read_b128 a[200:203], v25\n"
"buffer_load_dwordx4 v225, s[44:47], s69 offen lds\n"
"s_waitcnt lgkmcnt(4)\n"
"v_mfma_f32_32x32x16_bf16 v[36:51], a[56:59], a[212:215], v[36:51]\n"
"ds_read_b128 a[228:231], v22 offset:1024\n"
"v_mfma_f32_32x32x16_bf16 v[36:51], a[40:43], a[216:219], v[36:51]\n"
"ds_read_b128 a[208:211], v23 offset:1024\n"
"buffer_load_dwordx4 v226, s[44:47], s69 offen offset:1024 lds\n"
"v_mfma_f32_32x32x16_bf16 v[36:51], a[32:35], a[184:187], v[36:51]\n"
"ds_read_b128 a[216:219], v24 offset:1024\n"
"v_mfma_f32_32x32x16_bf16 v[36:51], a[16:19], a[220:223], v[36:51]\n"
"ds_read_b128 a[168:171], v25 offset:1024\n"
"buffer_load_dwordx4 v227, s[44:47], s69 offen offset:2048 lds\n"
"s_waitcnt lgkmcnt(4)\n"
"v_mfma_f32_32x32x16_bf16 v[36:51], a[52:55], a[172:175], v[36:51]\n"
"s_barrier\n"
"buffer_load_dwordx4 v228, s[44:47], s69 offen offset:3072 lds\n"
"v_mfma_f32_32x32x16_bf16 v[36:51], a[36:39], a[180:183], v[36:51]\n"
"ds_read_b64_tr_b16 a[172:173], v219 offset:4096\n"
"ds_read_b64_tr_b16 a[174:175], v219 offset:4608\n"
"ds_read_b64_tr_b16 a[180:181], v220 offset:4096\n"
"v_mfma_f32_32x32x16_bf16 v[36:51], a[28:31], a[224:227], v[36:51]\n"
"ds_read_b64_tr_b16 a[182:183], v220 offset:4608\n"
"ds_read_b64_tr_b16 a[176:177], v221 offset:4096\n"
"s_add_u32 m0, s79, 20480\n"
"ds_read_b64_tr_b16 a[178:179], v221 offset:4608\n"
"buffer_load_dwordx4 v225, s[44:47], s70 offen lds\n"
"v_mfma_f32_32x32x16_bf16 v[36:51], a[20:23], a[200:203], v[36:51]\n"
"ds_read_b64_tr_b16 a[204:205], v222 offset:4096\n"
"ds_read_b64_tr_b16 a[206:207], v222 offset:4608\n"
"s_waitcnt lgkmcnt(8)\n"
"v_mfma_f32_32x32x16_bf16 v[36:51], a[44:47], a[228:231], v[36:51]\n"
"ds_read_b64_tr_b16 a[184:185], v219 offset:5120\n"
"ds_read_b64_tr_b16 a[186:187], v219 offset:5632\n"
"buffer_load_dwordx4 v226, s[44:47], s70 offen offset:1024 lds\n"
"v_mfma_f32_32x32x16_bf16 v[36:51], a[24:27], a[208:211], v[36:51]\n"
"ds_read_b64_tr_b16 a[212:213], v220 offset:5120\n"
"ds_read_b64_tr_b16 a[214:215], v220 offset:5632\n"
"v_mfma_f32_32x32x16_bf16 v[36:51], a[12:15], a[216:219], v[36:51]\n"
"ds_read_b64_tr_b16 a[208:209], v221 offset:5120\n"
"ds_read_b64_tr_b16 a[210:211], v221 offset:5632\n"
"buffer_load_dwordx4 v227, s[44:47], s70 offen offset:2048 lds\n"
"v_mfma_f32_32x32x16_bf16 v[36:51], a[8:11], a[168:171], v[36:51]\n"
"ds_read_b64_tr_b16 a[216:217], v222 offset:5120\n"
"ds_read_b64_tr_b16 a[218:219], v222 offset:5632\n"
"v_mfma_f32_32x32x16_bf16 v[36:51], v[156:159], v[100:103], v[36:51]\n"
"s_waitcnt lgkmcnt(8)\n"
"v_mfma_f32_32x32x16_bf16 v[76:91], a[172:175], a[160:163], 0\n"
"ds_read_b64_tr_b16 a[168:169], v219 offset:6144\n"
"ds_read_b64_tr_b16 a[170:171], v219 offset:6656\n"
"buffer_load_dwordx4 v228, s[44:47], s70 offen offset:3072 lds\n"
"v_mfma_f32_32x32x16_bf16 v[100:115], a[180:183], a[160:163], 0\n"
"ds_read_b64_tr_b16 a[172:173], v220 offset:6144\n"
"ds_read_b64_tr_b16 a[174:175], v220 offset:6656\n"
"s_nop 3\n"
"v_permlane16_swap_b32 v36, v44\n"
"v_permlane16_swap_b32 v37, v45\n"
"v_permlane16_swap_b32 v38, v46\n"
"v_permlane16_swap_b32 v39, v47\n"
"v_permlane16_swap_b32 v40, v48\n"
"v_permlane16_swap_b32 v41, v49\n"
"v_permlane16_swap_b32 v42, v50\n"
"v_mfma_f32_32x32x16_bf16 v[20:35], a[176:179], a[160:163], 0\n"
"ds_read_b64_tr_b16 a[180:181], v221 offset:6144\n"
"ds_read_b64_tr_b16 a[182:183], v221 offset:6656\n"
"v_permlane16_swap_b32 v43, v51\n"
"v_mul_f32 v36, v176, v36\n"
"v_mul_f32 v44, v176, v44\n"
"v_mul_f32 v37, v177, v37\n"
"v_mul_f32 v45, v177, v45\n"
"v_fma_f32 v198, -v205, v44, v37\n"
"v_fma_f32 v199, v205, v36, v45\n"
"v_mul_f32 v38, v178, v38\n"
"v_mul_f32 v46, v178, v46\n"
"v_fmac_f32 v198, v204, v36\n"
"v_fmac_f32 v199, v204, v44\n"
"v_fma_f32 v196, -v205, v199, v38\n"
"v_mfma_f32_32x32x16_bf16 v[120:135], a[204:207], a[160:163], 0\n"
"ds_read_b64_tr_b16 a[176:177], v222 offset:6144\n"
"ds_read_b64_tr_b16 a[178:179], v222 offset:6656\n"
"v_fma_f32 v197, v205, v198, v46\n"
"v_mul_f32 v39, v179, v39\n"
"v_mul_f32 v47, v179, v47\n"
"v_fmac_f32 v196, v204, v198\n"
"v_fmac_f32 v197, v204, v199\n"
"v_fma_f32 v198, -v205, v197, v39\n"
"v_fma_f32 v199, v205, v196, v47\n"
"v_mul_f32 v40, v180, v40\n"
"v_mul_f32 v48, v180, v48\n"
"v_fmac_f32 v198, v204, v196\n"
"v_fmac_f32 v199, v204, v197\n"
"v_fma_f32 v196, -v205, v199, v40\n"
"v_fma_f32 v197, v205, v198, v48\n"
"s_waitcnt lgkmcnt(8)\n"
"v_mfma_f32_32x32x16_bf16 v[76:91], a[184:187], a[128:131], v[76:91]\n"
"ds_read_b64_tr_b16 a[160:161], v219 offset:7168\n"
"ds_read_b64_tr_b16 a[162:163], v219 offset:7680\n"
"v_mul_f32 v41, v181, v41\n"
"v_mul_f32 v49, v181, v49\n"
"v_fmac_f32 v196, v204, v198\n"
"v_fmac_f32 v197, v204, v199\n"
"v_fma_f32 v198, -v205, v197, v41\n"
"v_fma_f32 v199, v205, v196, v49\n"
"v_mul_f32 v42, v182, v42\n"
"s_waitcnt vmcnt(12)\n"
"v_xor_b32 v254, s80, v92\n"
"v_bitop3_b32 v254, v93, s81, v254 bitop3:0xbe\n"
"v_cmp_ne_u32 vcc, 0, v254\n"
"s_nop 1\n"
"s_cmp_lg_u64 vcc, 0\n"
"s_cbranch_scc1 SLOWBJC_%=\n"
"BACKBJC_%=:\n"
"s_add_u32 m0, s79, 28672\n"
"s_add_u32 s95, s96, 10240\n"
"buffer_load_dwordx4 v218, s[48:51], s95 offen sc1 lds\n"
"v_mfma_f32_32x32x16_bf16 v[100:115], a[212:215], a[128:131], v[100:115]\n"
"ds_read_b64_tr_b16 a[184:185], v220 offset:7168\n"
"ds_read_b64_tr_b16 a[186:187], v220 offset:7680\n"
"v_mul_f32 v50, v182, v50\n"
"v_fmac_f32 v198, v204, v196\n"
"buffer_load_dwordx4 v218, s[48:51], s95 offen offset:1024 sc1 lds\n"
"v_fmac_f32 v199, v204, v197\n"
"v_fma_f32 v196, -v205, v199, v42\n"
"v_fma_f32 v197, v205, v198, v50\n"
"v_mul_f32 v43, v183, v43\n"
"v_mul_f32 v51, v183, v51\n"
"v_fmac_f32 v196, v204, v198\n"
"v_fmac_f32 v197, v204, v199\n"
"v_fma_f32 v198, -v205, v197, v43\n"
"v_mfma_f32_32x32x16_bf16 v[20:35], a[208:211], a[128:131], v[20:35]\n"
"ds_read_b64_tr_b16 a[212:213], v221 offset:7168\n"
"ds_read_b64_tr_b16 a[214:215], v221 offset:7680\n"
"v_fma_f32 v199, v205, v196, v51\n"
"v_fmac_f32 v198, v204, v196\n"
"v_fmac_f32 v199, v204, v197\n"
"buffer_load_dwordx4 v218, s[48:51], s95 offen offset:2048 sc1 lds\n"
"ds_write_b64 v231, v[198:199] offset:36864\n"
"v_mov_b32 v93, s80\n"
"v_mov_b32 v95, s81\n"
"v_add_u32 v240, 10240, v232\n"
"v_mfma_f32_32x32x16_bf16 v[120:135], a[216:219], a[128:131], v[120:135]\n"
"ds_read_b64_tr_b16 a[208:209], v222 offset:7168\n"
"ds_read_b64_tr_b16 a[210:211], v222 offset:7680\n"
"s_cmp_eq_u32 s76, 0\n"
"s_cselect_b64 vcc, s[18:19], -1\n"
"v_cndmask_b32 v240, v240, -16, vcc  \n"
"v_subrev_u32 v254, 10240, v233\n"
"buffer_load_dwordx4 v218, s[48:51], s95 offen offset:3072 sc1 lds\n"
"v_subrev_u32 v234, 20480, v233\n"
"v_subrev_u32 v235, 30720, v233\n"
"buffer_load_dwordx4 v[196:199], v233, s[52:55], 0 offen sc1\n"
"s_waitcnt lgkmcnt(9)\n"
"v_mfma_f32_32x32x16_bf16 v[76:91], a[168:171], a[116:119], v[76:91]\n"
"s_barrier\n"
"s_waitcnt lgkmcnt(2)\n"
"s_barrier\n"
"ds_read_b128 v[178:181], v230\n"
"ds_read_b128 v[182:185], v230 offset:16\n"
"buffer_load_dwordx4 v[16:19], v254, s[52:55], 0 offen sc1\n"
"v_mfma_f32_32x32x16_bf16 v[100:115], a[172:175], a[116:119], v[100:115]\n"
"ds_read_b64_tr_b16 a[168:169], v219 offset:8192\n"
"ds_read_b64_tr_b16 a[170:171], v219 offset:8704\n"
"ds_read_b64_tr_b16 a[172:173], v220 offset:8192\n"
"buffer_load_dwordx4 v[192:195], v234, s[52:55], 0 offen sc1\n"
"buffer_load_dwordx4 v[188:191], v235, s[52:55], 0 offen sc1\n"
"s_waitcnt vmcnt(16)\n"
"v_mov_b32 v96, s80\n"
"v_mov_b32 v97, s81\n"
"s_mov_b64 exec, 1\n"
"buffer_store_dwordx2 v[96:97], off, s[48:51], s91 sc1\n"
"s_mov_b64 exec, -1\n"
"v_mfma_f32_32x32x16_bf16 v[20:35], a[180:183], a[116:119], v[20:35]\n"
"ds_read_b64_tr_b16 a[174:175], v220 offset:8704\n"
"ds_read_b64_tr_b16 a[128:129], v221 offset:8192\n"
"ds_read_b64_tr_b16 a[130:131], v221 offset:8704\n"
"v_ashrrev_i32 v236, 31, v233\n"
"s_waitcnt lgkmcnt(7)\n"
"v_fma_f32 v98, -v207, v179, v180\n"
"v_fma_f32 v99, v207, v178, v181\n"
"v_fmac_f32 v98, v206, v178\n"
"v_fmac_f32 v99, v206, v179\n"
"s_waitcnt lgkmcnt(6)\n"
"v_fma_f32 v96, -v207, v99, v182\n"
"v_fma_f32 v97, v207, v98, v183\n"
"v_fmac_f32 v96, v206, v98\n"
"v_fmac_f32 v97, v206, v99\n"
"v_fma_f32 v184, -v207, v97, v184\n"
"v_fmac_f32 v185, v207, v96\n"
"v_mfma_f32_32x32x16_bf16 v[120:135], a[176:179], a[116:119], v[120:135]\n"
"ds_read_b64_tr_b16 a[180:181], v222 offset:8192\n"
"ds_read_b64_tr_b16 a[182:183], v222 offset:8704\n"
"v_fmac_f32 v184, v206, v96\n"
"v_fmac_f32 v185, v206, v97\n"
"v_mov_b32 v98, v184\n"
"v_mov_b32 v99, v185\n"
"s_nop 0\n"
"v_permlane16_swap_b32 v184, v98\n"
"v_permlane16_swap_b32 v185, v99\n"
"v_fma_f32 v98, -v215, v185, v98\n"
"v_fmac_f32 v99, v215, v184\n"
"v_fmac_f32 v98, v214, v184\n"
"v_fmac_f32 v99, v214, v185\n"
"v_mov_b32 v92, v98\n"
"v_mfma_f32_32x32x16_bf16 v[76:91], a[160:163], a[108:111], v[76:91]\n"
"ds_read_b64_tr_b16 a[116:117], v219 offset:9216\n"
"ds_read_b64_tr_b16 a[118:119], v219 offset:9728\n"
"v_mov_b32 v94, v99\n"
"s_nop 0\n"
"v_permlane32_swap_b32 v98, v92\n"
"v_permlane32_swap_b32 v99, v94\n"
"v_fma_f32 v92, -v119, v99, v92\n"
"v_fmac_f32 v94, v119, v98\n"
"v_fmac_f32 v92, v118, v98\n"
"v_fmac_f32 v94, v118, v99\n"
"buffer_store_dwordx4 v[92:95], v240, s[52:55], 0 offen sc1\n"
"v_mfma_f32_32x32x16_bf16 v[100:115], a[184:187], a[108:111], v[100:115]\n"
"ds_read_b64_tr_b16 a[160:161], v220 offset:9216\n"
"ds_read_b64_tr_b16 a[162:163], v220 offset:9728\n"
"v_cndmask_b32 v176, 0, v184, s[20:21]  \n"
"v_cndmask_b32 v177, 0, v185, s[20:21]  \n"
"v_fma_f32 v176, -v217, v99, v176\n"
"v_fmac_f32 v177, v217, v98\n"
"v_fmac_f32 v176, v216, v98\n"
"v_fmac_f32 v177, v216, v99\n"
"v_ashrrev_i32 v237, 31, v254\n"
"v_ashrrev_i32 v238, 31, v234\n"
"v_ashrrev_i32 v239, 31, v235\n"
"v_mfma_f32_32x32x16_bf16 v[20:35], a[212:215], a[108:111], v[20:35]\n"
"ds_read_b64_tr_b16 a[184:185], v221 offset:9216\n"
"ds_read_b64_tr_b16 a[186:187], v221 offset:9728\n"
"v_mfma_f32_32x32x16_bf16 v[120:135], a[208:211], a[108:111], v[120:135]\n"
"ds_read_b64_tr_b16 a[212:213], v222 offset:9216\n"
"s_add_u32 m0, s79, 0\n"
"ds_read_b64_tr_b16 a[214:215], v222 offset:9728\n"
"buffer_load_dwordx4 v225, s[44:47], s71 offen lds\n"
"s_waitcnt lgkmcnt(8)\n"
"v_mfma_f32_32x32x16_bf16 v[76:91], a[168:171], a[136:139], v[76:91]\n"
"ds_read_b64_tr_b16 a[176:177], v219 offset:10240\n"
"ds_read_b64_tr_b16 a[178:179], v219 offset:10752\n"
"v_mfma_f32_32x32x16_bf16 v[100:115], a[172:175], a[136:139], v[100:115]\n"
"ds_read_b64_tr_b16 a[108:109], v220 offset:10240\n"
"ds_read_b64_tr_b16 a[110:111], v220 offset:10752\n"
"buffer_load_dwordx4 v226, s[44:47], s71 offen offset:1024 lds\n"
"v_mfma_f32_32x32x16_bf16 v[20:35], a[128:131], a[136:139], v[20:35]\n"
"ds_read_b64_tr_b16 a[208:209], v221 offset:10240\n"
"ds_read_b64_tr_b16 a[210:211], v221 offset:10752\n"
"v_mfma_f32_32x32x16_bf16 v[120:135], a[180:183], a[136:139], v[120:135]\n"
"ds_read_b64_tr_b16 a[172:173], v222 offset:10240\n"
"ds_read_b64_tr_b16 a[174:175], v222 offset:10752\n"
"s_waitcnt lgkmcnt(8)\n"
"v_mfma_f32_32x32x16_bf16 v[76:91], a[116:119], a[140:143], v[76:91]\n"
"ds_read_b64_tr_b16 a[136:137], v219 offset:11264\n"
"ds_read_b64_tr_b16 a[138:139], v219 offset:11776\n"
"buffer_load_dwordx4 v227, s[44:47], s71 offen offset:2048 lds\n"
"v_mfma_f32_32x32x16_bf16 v[100:115], a[160:163], a[140:143], v[100:115]\n"
"ds_read_b64_tr_b16 a[128:129], v220 offset:11264\n"
"ds_read_b64_tr_b16 a[130:131], v220 offset:11776\n"
"v_mfma_f32_32x32x16_bf16 v[20:35], a[184:187], a[140:143], v[20:35]\n"
"ds_read_b64_tr_b16 a[168:169], v221 offset:11264\n"
"ds_read_b64_tr_b16 a[170:171], v221 offset:11776\n"
"buffer_load_dwordx4 v228, s[44:47], s71 offen offset:3072 lds\n"
"v_mfma_f32_32x32x16_bf16 v[120:135], a[212:215], a[140:143], v[120:135]\n"
"ds_read_b64_tr_b16 a[180:181], v222 offset:11264\n"
"ds_read_b64_tr_b16 a[182:183], v222 offset:11776\n"
"s_waitcnt lgkmcnt(8)\n"
"v_mfma_f32_32x32x16_bf16 v[76:91], a[176:179], a[148:151], v[76:91]\n"
"s_barrier\n"
"s_add_u32 m0, s79, 32768\n"
"s_add_u32 s95, s96, 14336\n"
"buffer_load_dwordx4 v218, s[48:51], s95 offen sc1 lds\n"
"v_mfma_f32_32x32x16_bf16 v[100:115], a[108:111], a[148:151], v[100:115]\n"
"ds_read_b64_tr_b16 a[160:161], v219 offset:12288\n"
"ds_read_b64_tr_b16 a[162:163], v219 offset:12800\n"
"ds_read_b64_tr_b16 a[176:177], v220 offset:12288\n"
"v_mfma_f32_32x32x16_bf16 v[20:35], a[208:211], a[148:151], v[20:35]\n"
"ds_read_b64_tr_b16 a[178:179], v220 offset:12800\n"
"ds_read_b64_tr_b16 a[140:141], v221 offset:12288\n"
"s_add_u32 m0, s79, 4096\n"
"ds_read_b64_tr_b16 a[142:143], v221 offset:12800\n"
"buffer_load_dwordx4 v225, s[44:47], s72 offen lds\n"
"v_mfma_f32_32x32x16_bf16 v[120:135], a[172:175], a[148:151], v[120:135]\n"
"ds_read_b64_tr_b16 a[208:209], v222 offset:12288\n"
"ds_read_b64_tr_b16 a[210:211], v222 offset:12800\n"
"s_waitcnt lgkmcnt(8)\n"
"v_mfma_f32_32x32x16_bf16 v[76:91], a[136:139], a[152:155], v[76:91]\n"
"ds_read_b64_tr_b16 a[148:149], v219 offset:13312\n"
"ds_read_b64_tr_b16 a[150:151], v219 offset:13824\n"
"buffer_load_dwordx4 v226, s[44:47], s72 offen offset:1024 lds\n"
"v_mfma_f32_32x32x16_bf16 v[100:115], a[128:131], a[152:155], v[100:115]\n"
"ds_read_b64_tr_b16 a[212:213], v220 offset:13312\n"
"ds_read_b64_tr_b16 a[214:215], v220 offset:13824\n"
"v_mfma_f32_32x32x16_bf16 v[20:35], a[168:171], a[152:155], v[20:35]\n"
"ds_read_b64_tr_b16 a[184:185], v221 offset:13312\n"
"ds_read_b64_tr_b16 a[186:187], v221 offset:13824\n"
"v_mfma_f32_32x32x16_bf16 v[120:135], a[180:183], a[152:155], v[120:135]\n"
"ds_read_b64_tr_b16 a[108:109], v222 offset:13312\n"
"ds_read_b64_tr_b16 a[110:111], v222 offset:13824\n"
"buffer_load_dwordx4 v227, s[44:47], s72 offen offset:2048 lds\n"
"s_waitcnt lgkmcnt(8)\n"
"v_mfma_f32_32x32x16_bf16 v[76:91], a[160:163], a[156:159], v[76:91]\n"
"ds_read_b64_tr_b16 a[136:137], v219 offset:14336\n"
"ds_read_b64_tr_b16 a[138:139], v219 offset:14848\n"
"v_mfma_f32_32x32x16_bf16 v[100:115], a[176:179], a[156:159], v[100:115]\n"
"ds_read_b64_tr_b16 a[172:173], v220 offset:14336\n"
"ds_read_b64_tr_b16 a[174:175], v220 offset:14848\n"
"buffer_load_dwordx4 v228, s[44:47], s72 offen offset:3072 lds\n"
"v_mfma_f32_32x32x16_bf16 v[20:35], a[140:143], a[156:159], v[20:35]\n"
"ds_read_b64_tr_b16 a[168:169], v221 offset:14336\n"
"ds_read_b64_tr_b16 a[170:171], v221 offset:14848\n"
"v_mfma_f32_32x32x16_bf16 v[120:135], a[208:211], a[156:159], v[120:135]\n"
"ds_read_b64_tr_b16 a[128:129], v222 offset:14336\n"
"ds_read_b64_tr_b16 a[130:131], v222 offset:14848\n"
"s_waitcnt lgkmcnt(8)\n"
"v_mfma_f32_32x32x16_bf16 v[76:91], a[148:151], a[164:167], v[76:91]\n"
"ds_read_b64_tr_b16 a[140:141], v219 offset:15360\n"
"s_add_u32 m0, s79, 32768\n"
"ds_read_b64_tr_b16 a[142:143], v219 offset:15872\n"
"buffer_load_dwordx4 v218, s[48:51], s95 offen offset:1024 sc1 lds\n"
"v_mfma_f32_32x32x16_bf16 v[100:115], a[212:215], a[164:167], v[100:115]\n"
"ds_read_b64_tr_b16 a[148:149], v220 offset:15360\n"
"ds_read_b64_tr_b16 a[150:151], v220 offset:15872\n"
"v_mfma_f32_32x32x16_bf16 v[20:35], a[184:187], a[164:167], v[20:35]\n"
"ds_read_b64_tr_b16 a[152:153], v221 offset:15360\n"
"ds_read_b64_tr_b16 a[154:155], v221 offset:15872\n"
"buffer_load_dwordx4 v218, s[48:51], s95 offen offset:2048 sc1 lds\n"
"v_mfma_f32_32x32x16_bf16 v[120:135], a[108:111], a[164:167], v[120:135]\n"
"ds_read_b64_tr_b16 a[156:157], v222 offset:15360\n"
"ds_read_b64_tr_b16 a[158:159], v222 offset:15872\n"
"s_waitcnt vmcnt(16)\n"
"v_xor_b32 v93, s80, v197\n"
"v_bitop3_b32 v93, v199, s81, v93 bitop3:0xbe\n"
"v_bitop3_b32 v92, v93, v236, 0 bitop3:0xba\n"
"s_waitcnt vmcnt(15)\n"
"v_xor_b32 v93, s80, v17\n"
"v_bitop3_b32 v93, v19, s81, v93 bitop3:0xbe\n"
"buffer_load_dwordx4 a[184:187], v218, s[48:51], s95 offen offset:3072 sc1\n"
"v_bitop3_b32 v92, v93, v237, v92 bitop3:0xba\n"
"s_waitcnt vmcnt(15)\n"
"v_xor_b32 v93, s80, v193\n"
"v_bitop3_b32 v93, v195, s81, v93 bitop3:0xbe\n"
"v_bitop3_b32 v92, v93, v238, v92 bitop3:0xba\n"
"s_waitcnt lgkmcnt(8)\n"
"v_mfma_f32_32x32x16_bf16 v[76:91], a[136:139], a[144:147], v[76:91]\n"
"s_barrier\n"
"s_waitcnt vmcnt(14)\n"
"v_xor_b32 v93, s80, v189\n"
"v_bitop3_b32 v93, v191, s81, v93 bitop3:0xbe\n"
"v_bitop3_b32 v92, v93, v239, v92 bitop3:0xba\n"
"v_cmp_ne_u32 vcc, 0, v92\n"
"s_nop 1\n"
"s_cmp_lg_u64 vcc, 0\n"
"s_cbranch_scc1 SLOWBL0_%=\n"
"BACKBL0_%=:\n"
"s_add_u32 s95, s96, 18432\n"
"buffer_load_dwordx4 a[212:215], v218, s[48:51], s95 offen sc1\n"
"v_fma_f32 v192, -v139, v190, v192\n"
"v_mfma_f32_32x32x16_bf16 v[100:115], a[172:175], a[144:147], v[100:115]\n"
"ds_read_b64_tr_b16 a[136:137], v219 offset:16384\n"
"ds_read_b64_tr_b16 a[138:139], v219 offset:16896\n"
"ds_read_b64_tr_b16 a[164:165], v220 offset:16384\n"
"v_fmac_f32 v194, v139, v188\n"
"v_fmac_f32 v192, v138, v188\n"
"v_fmac_f32 v194, v138, v190\n"
"v_fma_f32 v16, -v139, v194, v16\n"
"v_fmac_f32 v18, v139, v192\n"
"s_add_u32 m0, s79, 8192\n"
"v_fmac_f32 v16, v138, v192\n"
"buffer_load_dwordx4 v225, s[44:47], s73 offen lds\n"
"v_fmac_f32 v18, v138, v194\n"
"v_fma_f32 v196, -v139, v18, v196\n"
"v_mfma_f32_32x32x16_bf16 v[20:35], a[168:171], a[144:147], v[20:35]\n"
"ds_read_b64_tr_b16 a[166:167], v220 offset:16896\n"
"ds_read_b64_tr_b16 a[172:173], v221 offset:16384\n"
"ds_read_b64_tr_b16 a[174:175], v221 offset:16896\n"
"v_fmac_f32 v198, v139, v16\n"
"v_fmac_f32 v196, v138, v16\n"
"v_fmac_f32 v198, v138, v18\n"
"v_mul_f32 v92, v116, v196\n"
"v_mul_f32 v93, v116, v198\n"
"v_fma_f32 v92, -v117, v198, v92\n"
"v_fmac_f32 v93, v117, v196\n"
"buffer_load_dwordx4 v226, s[44:47], s73 offen offset:1024 lds\n"
"v_mov_b32 v94, v92\n"
"v_mov_b32 v95, v93\n"
"v_mfma_f32_32x32x16_bf16 v[120:135], a[128:131], a[144:147], v[120:135]\n"
"ds_read_b64_tr_b16 a[168:169], v222 offset:16384\n"
"ds_read_b64_tr_b16 a[170:171], v222 offset:16896\n"
"s_nop 0\n"
"v_permlane16_swap_b32 v92, v94\n"
"v_permlane16_swap_b32 v93, v95\n"
"v_add_f32 v92, v92, v94\n"
"v_add_f32 v93, v93, v95\n"
"v_mov_b32 v94, v92\n"
"v_mov_b32 v95, v93\n"
"s_nop 0\n"
"buffer_load_dwordx4 v227, s[44:47], s73 offen offset:2048 lds\n"
"s_waitcnt lgkmcnt(8)\n"
"v_mfma_f32_32x32x16_bf16 v[76:91], a[140:143], a[124:127], v[76:91]\n"
"ds_read_b64_tr_b16 a[180:181], v219 offset:17408\n"
"ds_read_b64_tr_b16 a[182:183], v219 offset:17920\n"
"v_subrev_u32 v233, 10240, v232\n"
"v_subrev_u32 v254, 20480, v232\n"
"v_subrev_u32 v234, 30720, v232\n"
"buffer_load_dwordx4 v[196:199], v232, s[52:55], 0 offen sc1\n"
"buffer_load_dwordx4 v[16:19], v233, s[52:55], 0 offen sc1\n"
"buffer_load_dwordx4 v[192:195], v254, s[52:55], 0 offen sc1\n"
"v_mfma_f32_32x32x16_bf16 v[100:115], a[148:151], a[124:127], v[100:115]\n"
"ds_read_b64_tr_b16 a[108:109], v220 offset:17408\n"
"ds_read_b64_tr_b16 a[110:111], v220 offset:17920\n"
"buffer_load_dwordx4 v[188:191], v234, s[52:55], 0 offen sc1\n"
"v_ashrrev_i32 v235, 31, v232\n"
"v_ashrrev_i32 v236, 31, v233\n"
"v_ashrrev_i32 v237, 31, v254\n"
"v_ashrrev_i32 v238, 31, v234\n"
"v_permlane32_swap_b32 v92, v94\n"
"v_permlane32_swap_b32 v93, v95\n"
"v_add_f32 v92, v92, v94\n"
"v_add_f32 v93, v93, v95\n"
"v_fma_f32 v52, -v137, v93, v52\n"
"v_mfma_f32_32x32x16_bf16 v[20:35], a[152:155], a[124:127], v[20:35]\n"
"ds_read_b64_tr_b16 a[148:149], v221 offset:17408\n"
"ds_read_b64_tr_b16 a[150:151], v221 offset:17920\n"
"v_fmac_f32 v53, v137, v92\n"
"v_fmac_f32 v52, v136, v92\n"
"v_fmac_f32 v53, v136, v93\n"
"v_fma_f32 v54, -v59, v53, v54\n"
"v_fmac_f32 v55, v59, v52\n"
"v_fmac_f32 v54, v58, v52\n"
"v_fmac_f32 v55, v58, v53\n"
"ds_write_b128 v224, v[52:55]\n"
"v_mfma_f32_32x32x16_bf16 v[120:135], a[156:159], a[124:127], v[120:135]\n"
"ds_read_b64_tr_b16 a[128:129], v222 offset:17408\n"
"ds_read_b64_tr_b16 a[130:131], v222 offset:17920\n"
"s_waitcnt lgkmcnt(9)\n"
"v_mfma_f32_32x32x16_bf16 v[76:91], a[136:139], a[132:135], v[76:91]\n"
"ds_read_b64_tr_b16 a[140:141], v219 offset:18432\n"
"ds_read_b64_tr_b16 a[142:143], v219 offset:18944\n"
"s_waitcnt lgkmcnt(4)\n"
"s_barrier\n"
"ds_read_b64 v[52:53], v223 offset:35840\n"
"ds_read_b64 v[54:55], v223 offset:36352\n"
"v_mfma_f32_32x32x16_bf16 v[100:115], a[164:167], a[132:135], v[100:115]\n"
"ds_read_b64_tr_b16 a[156:157], v220 offset:18432\n"
"ds_read_b64_tr_b16 a[158:159], v220 offset:18944\n"
"v_mfma_f32_32x32x16_bf16 v[20:35], a[172:175], a[132:135], v[20:35]\n"
"ds_read_b64_tr_b16 a[144:145], v221 offset:18432\n"
"ds_read_b64_tr_b16 a[146:147], v221 offset:18944\n"
"buffer_load_dwordx2 v[216:217], v229, s[48:51], 0 offen sc1\n"
"v_mfma_f32_32x32x16_bf16 v[120:135], a[168:171], a[132:135], v[120:135]\n"
"ds_read_b64_tr_b16 a[164:165], v222 offset:18432\n"
"ds_read_b64_tr_b16 a[166:167], v222 offset:18944\n"
"s_waitcnt lgkmcnt(10)\n"
"v_mfma_f32_32x32x16_bf16 v[76:91], a[180:183], a[120:123], v[76:91]\n"
"ds_read_b64_tr_b16 a[132:133], v219 offset:19456\n"
"ds_read_b64_tr_b16 a[134:135], v219 offset:19968\n"
"s_waitcnt lgkmcnt(9)\n"
"v_fma_f32 v60, -v187, v53, v60\n"
"s_waitcnt lgkmcnt(8)\n"
"v_fma_f32 v140, -v203, v55, v140\n"
"buffer_load_dwordx4 v228, s[44:47], s73 offen offset:3072 lds\n"
"v_fmac_f32 v0, v187, v52\n"
"v_fmac_f32 v160, v203, v54\n"
"v_fmac_f32 v60, v186, v52\n"
"v_fmac_f32 v140, v202, v54\n"
"v_fmac_f32 v0, v186, v53\n"
"v_fmac_f32 v160, v202, v55\n"
"v_fma_f32 v61, -v187, v0, v61\n"
"v_mfma_f32_32x32x16_bf16 v[100:115], a[108:111], a[120:123], v[100:115]\n"
"ds_read_b64_tr_b16 a[124:125], v220 offset:19456\n"
"ds_read_b64_tr_b16 a[126:127], v220 offset:19968\n"
"v_fma_f32 v141, -v203, v160, v141\n"
"v_fmac_f32 v1, v187, v60\n"
"v_fmac_f32 v161, v203, v140\n"
"buffer_load_dwordx4 a[108:111], v218, s[48:51], s95 offen offset:1024 sc1\n"
"v_fmac_f32 v61, v186, v60\n"
"v_fmac_f32 v141, v202, v140\n"
"v_fmac_f32 v1, v186, v0\n"
"v_fmac_f32 v161, v202, v160\n"
"v_fma_f32 v62, -v187, v1, v62\n"
"v_fma_f32 v142, -v203, v161, v142\n"
"v_fmac_f32 v2, v187, v61\n"
"v_mfma_f32_32x32x16_bf16 v[20:35], a[148:151], a[120:123], v[20:35]\n"
"ds_read_b64_tr_b16 a[152:153], v221 offset:19456\n"
"ds_read_b64_tr_b16 a[154:155], v221 offset:19968\n"
"v_fmac_f32 v162, v203, v141\n"
"v_fmac_f32 v62, v186, v61\n"
"v_fmac_f32 v142, v202, v141\n"
"v_fmac_f32 v2, v186, v1\n"
"buffer_load_dwordx4 a[180:183], v218, s[48:51], s95 offen offset:2048 sc1\n"
"v_fmac_f32 v162, v202, v161\n"
"v_fma_f32 v63, -v187, v2, v63\n"
"v_fma_f32 v143, -v203, v162, v143\n"
"v_fmac_f32 v3, v187, v62\n"
"v_fmac_f32 v163, v203, v142\n"
"v_fmac_f32 v63, v186, v62\n"
"v_mfma_f32_32x32x16_bf16 v[120:135], a[128:131], a[120:123], v[120:135]\n"
"ds_read_b64_tr_b16 a[148:149], v222 offset:19456\n"
"ds_read_b64_tr_b16 a[150:151], v222 offset:19968\n"
"v_fmac_f32 v143, v202, v142\n"
"v_fmac_f32 v3, v186, v2\n"
"v_fmac_f32 v163, v202, v162\n"
"v_fma_f32 v64, -v187, v3, v64\n"
"v_fma_f32 v144, -v203, v163, v144\n"
"buffer_load_dwordx4 a[128:131], v218, s[48:51], s95 offen offset:3072 sc1\n"
"v_fmac_f32 v4, v187, v63\n"
"v_fmac_f32 v164, v203, v143\n"
"v_fmac_f32 v64, v186, v63\n"
"v_fmac_f32 v144, v202, v143\n"
"v_fmac_f32 v4, v186, v3\n"
"s_waitcnt lgkmcnt(8)\n"
"v_mfma_f32_32x32x16_bf16 v[76:91], a[140:143], a[112:115], v[76:91]\n"
"s_barrier\n"
"v_fmac_f32 v164, v202, v163\n"
"v_fma_f32 v65, -v187, v4, v65\n"
"v_fma_f32 v145, -v203, v164, v145\n"
"s_add_u32 s95, s96, 22528\n"
"buffer_load_dwordx4 a[168:171], v218, s[48:51], s95 offen sc1\n"
"v_fmac_f32 v5, v187, v64\n"
"v_fmac_f32 v165, v203, v144\n"
"v_fmac_f32 v65, v186, v64\n"
"v_fmac_f32 v145, v202, v144\n"
"v_mfma_f32_32x32x16_bf16 v[100:115], a[156:159], a[112:115], v[100:115]\n"
"ds_read_b64_tr_b16 a[136:137], v219 offset:20480\n"
"ds_read_b64_tr_b16 a[138:139], v219 offset:20992\n"
"ds_read_b64_tr_b16 a[140:141], v220 offset:20480\n"
"v_fmac_f32 v5, v186, v4\n"
"v_fmac_f32 v165, v202, v164\n"
"v_fma_f32 v66, -v187, v5, v66\n"
"v_fma_f32 v146, -v203, v165, v146\n"
"v_fmac_f32 v6, v187, v65\n"
"s_add_u32 m0, s79, 12288\n"
"v_fmac_f32 v166, v203, v145\n"
"buffer_load_dwordx4 v225, s[44:47], s74 offen lds\n"
"v_fmac_f32 v66, v186, v65\n"
"v_fmac_f32 v146, v202, v145\n"
"v_mfma_f32_32x32x16_bf16 v[20:35], a[144:147], a[112:115], v[20:35]\n"
"ds_read_b64_tr_b16 a[142:143], v220 offset:20992\n"
"ds_read_b64_tr_b16 a[156:157], v221 offset:20480\n"
"ds_read_b64_tr_b16 a[158:159], v221 offset:20992\n"
"v_fmac_f32 v6, v186, v5\n"
"v_fmac_f32 v166, v202, v165\n"
"v_fma_f32 v67, -v187, v6, v67\n"
"v_fma_f32 v147, -v203, v166, v147\n"
"v_fmac_f32 v7, v187, v66\n"
"v_fmac_f32 v167, v203, v146\n"
"buffer_load_dwordx4 v226, s[44:47], s74 offen offset:1024 lds\n"
"v_fmac_f32 v67, v186, v66\n"
"v_fmac_f32 v147, v202, v146\n"
"v_fmac_f32 v7, v186, v6\n"
"v_mfma_f32_32x32x16_bf16 v[120:135], a[164:167], a[112:115], v[120:135]\n"
"ds_read_b64_tr_b16 a[144:145], v222 offset:20480\n"
"ds_read_b64_tr_b16 a[146:147], v222 offset:20992\n"
"v_fmac_f32 v167, v202, v166\n"
"v_cvt_pk_bf16_f32 v52, v60, v61\n"
"v_cvt_pk_bf16_f32 v92, v140, v141\n"
"v_cvt_pk_bf16_f32 v53, v62, v63\n"
"v_cvt_pk_bf16_f32 v93, v142, v143\n"
"v_cvt_pk_bf16_f32 v54, v64, v65\n"
"v_cvt_pk_bf16_f32 v94, v144, v145\n"
"buffer_load_dwordx4 v227, s[44:47], s74 offen offset:2048 lds\n"
"v_cvt_pk_bf16_f32 v55, v66, v67\n"
"v_cvt_pk_bf16_f32 v95, v146, v147\n"
"s_waitcnt lgkmcnt(8)\n"
"v_mfma_f32_32x32x16_bf16 v[76:91], a[132:135], a[104:107], v[76:91]\n"
"ds_read_b64_tr_b16 a[164:165], v219 offset:21504\n"
"ds_read_b64_tr_b16 a[166:167], v219 offset:22016\n"
"v_cvt_pk_bf16_f32 v56, v0, v1\n"
"v_cvt_pk_bf16_f32 v96, v160, v161\n"
"v_cvt_pk_bf16_f32 v57, v2, v3\n"
"v_cvt_pk_bf16_f32 v97, v162, v163\n"
"v_cvt_pk_bf16_f32 v58, v4, v5\n"
"v_cvt_pk_bf16_f32 v98, v164, v165\n"
"v_accvgpr_read_b32 v223, a4\n"
"v_cvt_pk_bf16_f32 v59, v6, v7\n"
"buffer_load_dwordx4 v228, s[44:47], s74 offen offset:3072 lds\n"
"v_cvt_pk_bf16_f32 v99, v166, v167\n"
"v_mfma_f32_32x32x16_bf16 v[100:115], a[124:127], a[104:107], v[100:115]\n"
"ds_read_b64_tr_b16 a[132:133], v220 offset:21504\n"
"ds_read_b64_tr_b16 a[134:135], v220 offset:22016\n"
"v_fma_f32 v68, -v187, v7, v68\n"
"v_fma_f32 v148, -v203, v167, v148\n"
"s_add_u32 s14, s92, 598016\n"
"buffer_store_dwordx4 v[52:55], v223, s[48:51], s14 offen sc1\n"
"v_mfma_f32_32x32x16_bf16 v[20:35], a[152:155], a[104:107], v[20:35]\n"
"ds_read_b64_tr_b16 a[112:113], v221 offset:21504\n"
"ds_read_b64_tr_b16 a[114:115], v221 offset:22016\n"
"v_fmac_f32 v8, v187, v67\n"
"v_fmac_f32 v168, v203, v147\n"
"v_fmac_f32 v68, v186, v67\n"
"v_fmac_f32 v148, v202, v147\n"
"v_fmac_f32 v8, v186, v7\n"
"v_fmac_f32 v168, v202, v167\n"
"v_fma_f32 v69, -v187, v8, v69\n"
"buffer_load_dwordx4 a[152:155], v218, s[48:51], s95 offen offset:1024 sc1\n"
"v_fma_f32 v149, -v203, v168, v149\n"
"v_fmac_f32 v9, v187, v68\n"
"v_fmac_f32 v169, v203, v148\n"
"v_mfma_f32_32x32x16_bf16 v[120:135], a[148:151], a[104:107], v[120:135]\n"
"ds_read_b64_tr_b16 a[120:121], v222 offset:21504\n"
"ds_read_b64_tr_b16 a[122:123], v222 offset:22016\n"
"v_fmac_f32 v69, v186, v68\n"
"v_fmac_f32 v149, v202, v148\n"
"v_fmac_f32 v9, v186, v8\n"
"v_fmac_f32 v169, v202, v168\n"
"v_fma_f32 v70, -v187, v9, v70\n"
"v_fma_f32 v150, -v203, v169, v150\n"
"v_fmac_f32 v10, v187, v69\n"
"v_accvgpr_read_b32 v224, a3\n"
"buffer_load_dwordx4 a[124:127], v218, s[48:51], s95 offen offset:2048 sc1\n"
"v_fmac_f32 v170, v203, v149\n"
"v_fmac_f32 v70, v186, v69\n"
"v_fmac_f32 v150, v202, v149\n"
"s_waitcnt lgkmcnt(8)\n"
"v_mfma_f32_32x32x16_bf16 v[76:91], a[136:139], a[100:103], v[76:91]\n"
"ds_read_b64_tr_b16 a[104:105], v219 offset:22528\n"
"ds_read_b64_tr_b16 a[106:107], v219 offset:23040\n"
"buffer_load_dwordx4 v[136:139], v224, s[48:51], 0 offen sc1\n"
"s_waitcnt vmcnt(17)\n"
"v_xor_b32 v215, s80, v197\n"
"v_bitop3_b32 v215, v199, s81, v215 bitop3:0xbe\n"
"v_bitop3_b32 v214, v215, v235, 0 bitop3:0xba\n"
"s_waitcnt vmcnt(16)\n"
"v_xor_b32 v215, s80, v17\n"
"v_bitop3_b32 v215, v19, s81, v215 bitop3:0xbe\n"
"v_bitop3_b32 v214, v215, v236, v214 bitop3:0xba\n"
"s_waitcnt vmcnt(15)\n"
"v_xor_b32 v215, s80, v193\n"
"v_bitop3_b32 v215, v195, s81, v215 bitop3:0xbe\n"
"v_mfma_f32_32x32x16_bf16 v[100:115], a[140:143], a[100:103], v[100:115]\n"
"ds_read_b64_tr_b16 a[148:149], v220 offset:22528\n"
"ds_read_b64_tr_b16 a[150:151], v220 offset:23040\n"
"v_bitop3_b32 v214, v215, v237, v214 bitop3:0xba\n"
"s_waitcnt vmcnt(14)\n"
"v_xor_b32 v215, s80, v189\n"
"v_bitop3_b32 v215, v191, s81, v215 bitop3:0xbe\n"
"v_bitop3_b32 v214, v215, v238, v214 bitop3:0xba\n"
"buffer_load_dwordx4 v[116:119], v224, s[48:51], 0 offen offset:1024 sc1\n"
"v_cmp_ne_u32 vcc, 0, v214\n"
"s_nop 1\n"
"s_cmp_lg_u64 vcc, 0\n"
"s_cbranch_scc1 SLOWBL1_%=\n"
"BACKBL1_%=:\n"
"v_fma_f32 v192, -v213, v190, v192\n"
"v_fmac_f32 v194, v213, v188\n"
"v_fmac_f32 v192, v212, v188\n"
"v_mfma_f32_32x32x16_bf16 v[20:35], a[156:159], a[100:103], v[20:35]\n"
"ds_read_b64_tr_b16 a[140:141], v221 offset:22528\n"
"ds_read_b64_tr_b16 a[142:143], v221 offset:23040\n"
"v_fmac_f32 v194, v212, v190\n"
"v_fma_f32 v16, -v213, v194, v16\n"
"v_fmac_f32 v18, v213, v192\n"
"v_fmac_f32 v16, v212, v192\n"
"v_fmac_f32 v18, v212, v194\n"
"v_fma_f32 v196, -v213, v18, v196\n"
"v_fmac_f32 v198, v213, v16\n"
"buffer_load_dwordx4 v[192:195], v224, s[48:51], 0 offen offset:2048 sc1\n"
"v_fmac_f32 v196, v212, v16\n"
"v_fmac_f32 v198, v212, v18\n"
"v_mul_f32 v16, v210, v196\n"
"v_mfma_f32_32x32x16_bf16 v[120:135], a[144:147], a[100:103], v[120:135]\n"
"ds_read_b64_tr_b16 a[156:157], v222 offset:22528\n"
"ds_read_b64_tr_b16 a[158:159], v222 offset:23040\n"
"v_mul_f32 v17, v210, v198\n"
"v_fma_f32 v16, -v211, v198, v16\n"
"v_fmac_f32 v17, v211, v196\n"
"v_mov_b32 v18, v16\n"
"v_mov_b32 v19, v17\n"
"s_nop 0\n"
"v_permlane16_swap_b32 v16, v18\n"
"v_permlane16_swap_b32 v17, v19\n"
"buffer_load_dwordx4 v[196:199], v224, s[48:51], 0 offen offset:3072 sc1\n"
"v_add_f32 v16, v16, v18\n"
"s_waitcnt lgkmcnt(8)\n"
"v_mfma_f32_32x32x16_bf16 v[76:91], a[164:167], a[96:99], v[76:91]\n"
"ds_read_b64_tr_b16 a[100:101], v219 offset:23552\n"
"ds_read_b64_tr_b16 a[102:103], v219 offset:24064\n"
"v_add_f32 v17, v17, v19\n"
"v_mov_b32 v18, v16\n"
"v_mov_b32 v19, v17\n"
"s_nop 0\n"
"v_permlane32_swap_b32 v16, v18\n"
"v_permlane32_swap_b32 v17, v19\n"
"v_add_f32 v16, v16, v18\n"
"v_add_f32 v17, v17, v19\n"
"v_fma_f32 v176, -v209, v17, v176\n"
"buffer_load_dwordx4 a[144:147], v218, s[48:51], s95 offen offset:3072 sc1\n"
"v_mfma_f32_32x32x16_bf16 v[100:115], a[132:135], a[96:99], v[100:115]\n"
"ds_read_b64_tr_b16 a[164:165], v220 offset:23552\n"
"ds_read_b64_tr_b16 a[166:167], v220 offset:24064\n"
"v_fmac_f32 v177, v209, v16\n"
"v_fmac_f32 v176, v208, v16\n"
"v_fmac_f32 v177, v208, v17\n"
"v_fma_f32 v178, -v207, v177, v178\n"
"v_fmac_f32 v179, v207, v176\n"
"v_fmac_f32 v178, v206, v176\n"
"v_fmac_f32 v179, v206, v177\n"
"v_fma_f32 v180, -v207, v179, v180\n"
"v_fmac_f32 v181, v207, v178\n"
"v_fmac_f32 v180, v206, v178\n"
"v_fmac_f32 v181, v206, v179\n"
"s_add_u32 s95, s96, 26624\n"
"buffer_load_dwordx4 v[208:211], v218, s[48:51], s95 offen sc1\n"
"v_mfma_f32_32x32x16_bf16 v[20:35], a[112:115], a[96:99], v[20:35]\n"
"ds_read_b64_tr_b16 a[132:133], v221 offset:23552\n"
"ds_read_b64_tr_b16 a[134:135], v221 offset:24064\n"
"v_fma_f32 v182, -v207, v181, v182\n"
"v_fmac_f32 v183, v207, v180\n"
"v_fmac_f32 v182, v206, v180\n"
"v_fmac_f32 v183, v206, v181\n"
"ds_write_b128 v230, v[176:179]\n"
"buffer_load_dwordx4 v[212:215], v218, s[48:51], s95 offen offset:1024 sc1\n"
"v_mfma_f32_32x32x16_bf16 v[120:135], a[120:123], a[96:99], v[120:135]\n"
"ds_read_b64_tr_b16 a[112:113], v222 offset:23552\n"
"ds_read_b64_tr_b16 a[114:115], v222 offset:24064\n"
"ds_write_b128 v230, v[180:183] offset:16\n"
"buffer_load_dwordx4 a[120:123], v218, s[48:51], s95 offen offset:2048 sc1\n"
"ds_read_b64 v[16:17], v231 offset:36864\n"
"s_waitcnt lgkmcnt(11)\n"
"v_mfma_f32_32x32x16_bf16 v[76:91], a[104:107], a[92:95], v[76:91]\n"
"s_barrier\n"
"buffer_store_dwordx4 v[56:59], v223, s[48:51], s14 offen offset:512 sc1\n"
"v_mfma_f32_32x32x16_bf16 v[100:115], a[148:151], a[92:95], v[100:115]\n"
"ds_read_b64_tr_b16 a[96:97], v219\n"
"ds_read_b64_tr_b16 a[98:99], v219 offset:512\n"
"ds_read_b64_tr_b16 a[104:105], v220\n"
"s_waitcnt lgkmcnt(3)\n"
"v_fma_f32 v36, -v205, v17, v36\n"
"v_fmac_f32 v44, v205, v16\n"
"v_fmac_f32 v36, v204, v16\n"
"v_fmac_f32 v44, v204, v17\n"
"s_add_u32 m0, s79, 16384\n"
"v_fma_f32 v37, -v205, v44, v37\n"
"buffer_load_dwordx4 v225, s[44:47], s75 offen lds\n"
"v_fmac_f32 v45, v205, v36\n"
"v_fmac_f32 v37, v204, v36\n"
"v_fmac_f32 v45, v204, v44\n"
"v_mfma_f32_32x32x16_bf16 v[20:35], a[140:143], a[92:95], v[20:35]\n"
"ds_read_b64_tr_b16 a[106:107], v220 offset:512\n"
"ds_read_b64_tr_b16 a[148:149], v221\n"
"ds_read_b64_tr_b16 a[150:151], v221 offset:512\n"
"v_fma_f32 v38, -v205, v45, v38\n"
"v_fmac_f32 v46, v205, v37\n"
"v_fmac_f32 v38, v204, v37\n"
"v_fmac_f32 v46, v204, v45\n"
"v_fma_f32 v39, -v205, v46, v39\n"
"buffer_load_dwordx4 v226, s[44:47], s75 offen offset:1024 lds\n"
"v_fmac_f32 v47, v205, v38\n"
"v_fmac_f32 v39, v204, v38\n"
"v_fmac_f32 v47, v204, v46\n"
"v_fma_f32 v40, -v205, v47, v40\n"
"v_mfma_f32_32x32x16_bf16 v[120:135], a[156:159], a[92:95], v[120:135]\n"
"ds_read_b64_tr_b16 a[140:141], v222\n"
"ds_read_b64_tr_b16 a[142:143], v222 offset:512\n"
"v_fmac_f32 v48, v205, v39\n"
"v_fmac_f32 v40, v204, v39\n"
"v_fmac_f32 v48, v204, v47\n"
"v_fma_f32 v41, -v205, v48, v41\n"
"v_fmac_f32 v49, v205, v40\n"
"v_fmac_f32 v41, v204, v40\n"
"v_fmac_f32 v49, v204, v48\n"
"buffer_load_dwordx4 v227, s[44:47], s75 offen offset:2048 lds\n"
"v_fma_f32 v42, -v205, v49, v42\n"
"v_fmac_f32 v50, v205, v41\n"
"v_fmac_f32 v42, v204, v41\n"
"v_mfma_f32_32x32x16_bf16 v[76:91], a[100:103], a[88:91], v[76:91]\n"
"ds_read_b64_tr_b16 a[156:157], v219 offset:1024\n"
"ds_read_b64_tr_b16 a[158:159], v219 offset:1536\n"
"v_fmac_f32 v50, v204, v49\n"
"v_fma_f32 v43, -v205, v50, v43\n"
"v_fmac_f32 v51, v205, v42\n"
"v_fmac_f32 v43, v204, v42\n"
"v_fmac_f32 v51, v204, v50\n"
"v_cvt_pk_bf16_f32 v52, v36, v37\n"
"v_cvt_pk_bf16_f32 v53, v38, v39\n"
"buffer_load_dwordx4 v228, s[44:47], s75 offen offset:3072 lds\n"
"v_cvt_pk_bf16_f32 v54, v40, v41\n"
"v_accvgpr_read_b32 v180, a2\n"
"v_cvt_pk_bf16_f32 v55, v42, v43\n"
"v_mfma_f32_32x32x16_bf16 v[100:115], a[164:167], a[88:91], v[100:115]\n"
"ds_read_b64_tr_b16 a[100:101], v220 offset:1024\n"
"ds_read_b64_tr_b16 a[102:103], v220 offset:1536\n"
"v_cvt_pk_bf16_f32 v56, v44, v45\n"
"v_cvt_pk_bf16_f32 v57, v46, v47\n"
"v_cvt_pk_bf16_f32 v58, v48, v49\n"
"v_cvt_pk_bf16_f32 v59, v50, v51\n"
"s_add_u32 s14, s92, 614400\n"
"buffer_store_dwordx4 v[52:55], v180, s[48:51], s14 offen sc1\n"
"v_mfma_f32_32x32x16_bf16 v[20:35], a[132:135], a[88:91], v[20:35]\n"
"ds_read_b64_tr_b16 a[164:165], v221 offset:1024\n"
"ds_read_b64_tr_b16 a[166:167], v221 offset:1536\n"
"buffer_store_dwordx4 v[56:59], v180, s[48:51], s14 offen offset:512 sc1\n"
"v_mfma_f32_32x32x16_bf16 v[120:135], a[112:115], a[88:91], v[120:135]\n"
"ds_read_b64_tr_b16 a[92:93], v222 offset:1024\n"
"ds_read_b64_tr_b16 a[94:95], v222 offset:1536\n"
"v_fmac_f32 v10, v186, v9\n"
"v_fmac_f32 v170, v202, v169\n"
"v_fma_f32 v71, -v187, v10, v71\n"
"buffer_load_dwordx4 a[112:115], v218, s[48:51], s95 offen offset:3072 sc1\n"
"v_fma_f32 v151, -v203, v170, v151\n"
"v_fmac_f32 v11, v187, v70\n"
"v_fmac_f32 v171, v203, v150\n"
"v_fmac_f32 v71, v186, v70\n"
"v_fmac_f32 v151, v202, v150\n"
"v_fmac_f32 v11, v186, v10\n"
"v_fmac_f32 v171, v202, v170\n"
"s_waitcnt lgkmcnt(8)\n"
"v_mfma_f32_32x32x16_bf16 v[76:91], a[96:99], a[84:87], v[76:91]\n"
"ds_read_b64_tr_b16 a[88:89], v219 offset:2048\n"
"ds_read_b64_tr_b16 a[90:91], v219 offset:2560\n"
"v_fma_f32 v72, -v187, v11, v72\n"
"v_fma_f32 v152, -v203, v171, v152\n"
"v_fmac_f32 v12, v187, v71\n"
"s_add_u32 s95, s96, 30720\n"
"buffer_load_dwordx4 a[132:135], v218, s[48:51], s95 offen sc1\n"
"v_fmac_f32 v172, v203, v151\n"
"v_fmac_f32 v72, v186, v71\n"
"v_fmac_f32 v152, v202, v151\n"
"v_fmac_f32 v12, v186, v11\n"
"v_fmac_f32 v172, v202, v171\n"
"v_fma_f32 v73, -v187, v12, v73\n"
"v_mfma_f32_32x32x16_bf16 v[100:115], a[104:107], a[84:87], v[100:115]\n"
"ds_read_b64_tr_b16 a[96:97], v220 offset:2048\n"
"ds_read_b64_tr_b16 a[98:99], v220 offset:2560\n"
"v_fma_f32 v153, -v203, v172, v153\n"
"s_add_u32 s14, s92, 606208\n"
"buffer_store_dwordx4 v[92:95], v223, s[48:51], s14 offen sc1\n"
"v_mfma_f32_32x32x16_bf16 v[20:35], a[148:151], a[84:87], v[20:35]\n"
"ds_read_b64_tr_b16 a[104:105], v221 offset:2048\n"
"ds_read_b64_tr_b16 a[106:107], v221 offset:2560\n"
"s_waitcnt vmcnt(17)\n"
"v_xor_b32 v36, s8, v137\n"
"v_bitop3_b32 v36, v139, s9, v36 bitop3:0xbe\n"
"s_waitcnt vmcnt(16)\n"
"v_bitop3_b32 v36, v117, s8, v36 bitop3:0xbe\n"
"buffer_load_dwordx4 v[204:207], v218, s[48:51], s95 offen offset:1024 sc1\n"
"v_bitop3_b32 v36, v119, s9, v36 bitop3:0xbe\n"
"s_waitcnt vmcnt(16)\n"
"v_bitop3_b32 v36, v193, s8, v36 bitop3:0xbe\n"
"v_bitop3_b32 v36, v195, s9, v36 bitop3:0xbe\n"
"s_waitcnt vmcnt(15)\n"
"v_bitop3_b32 v36, v197, s8, v36 bitop3:0xbe\n"
"v_bitop3_b32 v36, v199, s9, v36 bitop3:0xbe\n"
"v_fmac_f32 v13, v187, v72\n"
"v_mfma_f32_32x32x16_bf16 v[120:135], a[140:143], a[84:87], v[120:135]\n"
"ds_read_b64_tr_b16 a[148:149], v222 offset:2048\n"
"ds_read_b64_tr_b16 a[150:151], v222 offset:2560\n"
"v_cmp_ne_u32 vcc, 0, v36\n"
"s_nop 1\n"
"s_cmp_lg_u64 vcc, 0\n"
"s_cbranch_scc1 SLOWBX0_%=\n"
"BACKBX0_%=:\n"
"v_fmac_f32 v173, v203, v152\n"
"buffer_load_dwordx4 a[140:143], v218, s[48:51], s95 offen offset:2048 sc1\n"
"v_fmac_f32 v73, v186, v72\n"
"v_fmac_f32 v153, v202, v152\n"
"v_fmac_f32 v13, v186, v12\n"
"v_fmac_f32 v173, v202, v172\n"
"v_fma_f32 v74, -v187, v13, v74\n"
"v_fma_f32 v154, -v203, v173, v154\n"
"s_waitcnt lgkmcnt(8)\n"
"v_mfma_f32_32x32x16_bf16 v[76:91], a[156:159], a[80:83], v[76:91]\n"
"ds_read_b64_tr_b16 a[84:85], v219 offset:3072\n"
"ds_read_b64_tr_b16 a[86:87], v219 offset:3584\n"
"v_fmac_f32 v14, v187, v73\n"
"v_fmac_f32 v174, v203, v153\n"
"v_fmac_f32 v74, v186, v73\n"
"v_fmac_f32 v154, v202, v153\n"
"buffer_load_dwordx4 v[232:235], v218, s[48:51], s95 offen offset:3072 sc1\n"
"v_fmac_f32 v14, v186, v13\n"
"v_fmac_f32 v174, v202, v173\n"
"v_fma_f32 v75, -v187, v14, v75\n"
"v_fma_f32 v155, -v203, v174, v155\n"
"v_fmac_f32 v15, v187, v74\n"
"v_mfma_f32_32x32x16_bf16 v[100:115], a[100:103], a[80:83], v[100:115]\n"
"ds_read_b64_tr_b16 a[156:157], v220 offset:3072\n"
"ds_read_b64_tr_b16 a[158:159], v220 offset:3584\n"
"v_fmac_f32 v175, v203, v154\n"
"v_fmac_f32 v75, v186, v74\n"
"v_fmac_f32 v155, v202, v154\n"
"buffer_store_dwordx4 v[96:99], v223, s[48:51], s14 offen offset:512 sc1\n"
"v_mfma_f32_32x32x16_bf16 v[20:35], a[164:167], a[80:83], v[20:35]\n"
"ds_read_b64_tr_b16 a[100:101], v221 offset:3072\n"
"ds_read_b64_tr_b16 a[102:103], v221 offset:3584\n"
"v_fmac_f32 v15, v186, v14\n"
"v_fmac_f32 v175, v202, v174\n"
"v_cvt_pk_bf16_f32 v92, v68, v69\n"
"v_cvt_pk_bf16_f32 v36, v148, v149\n"
"s_add_u32 s95, s96, 34816\n"
"buffer_load_dwordx4 a[136:139], v218, s[48:51], s95 offen sc1\n"
"v_xor_b32 v203, s80, v216\n"
"v_bitop3_b32 v203, v217, s81, v203 bitop3:0xbe\n"
"v_cmp_ne_u32 vcc, 0, v203\n"
"s_nop 1\n"
"s_cmp_lg_u64 vcc, 0\n"
"s_cbranch_scc1 SLOWBJW_%=\n"
"BACKBJW_%=:\n"
"v_cvt_pk_bf16_f32 v93, v70, v71\n"
"v_mfma_f32_32x32x16_bf16 v[120:135], a[92:95], a[80:83], v[120:135]\n"
"ds_read_b64_tr_b16 a[164:165], v222 offset:3072\n"
"ds_read_b64_tr_b16 a[166:167], v222 offset:3584\n"
"v_cvt_pk_bf16_f32 v37, v150, v151\n"
"v_cvt_pk_bf16_f32 v94, v72, v73\n"
"v_cvt_pk_bf16_f32 v38, v152, v153\n"
"v_cvt_pk_bf16_f32 v95, v74, v75\n"
"s_add_u32 s95, s96, 0\n"
"buffer_load_dwordx4 a[172:175], v218, s[48:51], s95 offen sc1\n"
"v_cvt_pk_bf16_f32 v39, v154, v155\n"
"v_cvt_pk_bf16_f32 v96, v8, v9\n"
"v_cvt_pk_bf16_f32 v40, v168, v169\n"
"v_cvt_pk_bf16_f32 v97, v10, v11\n"
"v_cvt_pk_bf16_f32 v41, v170, v171\n"
"s_waitcnt lgkmcnt(8)\n"
"v_mfma_f32_32x32x16_bf16 v[76:91], a[88:91], a[76:79], v[76:91]\n"
"s_barrier\n"
"v_cvt_pk_bf16_f32 v98, v12, v13\n"
"buffer_load_dwordx4 v[236:239], v218, s[48:51], s95 offen offset:1024 sc1\n"
"v_cvt_pk_bf16_f32 v42, v172, v173\n"
"v_cvt_pk_bf16_f32 v99, v14, v15\n"
"v_cvt_pk_bf16_f32 v43, v174, v175\n"
"buffer_store_dwordx4 v[36:39], v223, s[48:51], s14 offen offset:256 sc1\n"
"v_mfma_f32_32x32x16_bf16 v[100:115], a[96:99], a[76:79], v[100:115]\n"
"ds_read_b64_tr_b16 a[92:93], v219 offset:4096\n"
"ds_read_b64_tr_b16 a[94:95], v219 offset:4608\n"
"ds_read_b64_tr_b16 a[80:81], v220 offset:4096\n"
"buffer_store_dwordx4 v[40:43], v223, s[48:51], s14 offen offset:768 sc1\n"
"v_mfma_f32_32x32x16_bf16 v[20:35], a[104:107], a[76:79], v[20:35]\n"
"ds_read_b64_tr_b16 a[82:83], v220 offset:4608\n"
"ds_read_b64_tr_b16 a[88:89], v221 offset:4096\n"
"ds_read_b64_tr_b16 a[90:91], v221 offset:4608\n"
"s_add_u32 s14, s92, 598016\n"
"buffer_store_dwordx4 v[92:95], v223, s[48:51], s14 offen offset:256 sc1\n"
"v_mfma_f32_32x32x16_bf16 v[120:135], a[148:151], a[76:79], v[120:135]\n"
"ds_read_b64_tr_b16 a[96:97], v222 offset:4096\n"
"ds_read_b64_tr_b16 a[98:99], v222 offset:4608\n"
"buffer_store_dwordx4 v[96:99], v223, s[48:51], s14 offen offset:768 sc1\n"
"s_waitcnt lgkmcnt(8)\n"
"v_mfma_f32_32x32x16_bf16 v[76:91], a[84:87], a[72:75], v[76:91]\n"
"ds_read_b64_tr_b16 a[76:77], v219 offset:5120\n"
"ds_read_b64_tr_b16 a[78:79], v219 offset:5632\n"
"s_add_u32 s86, s76, 0\n"
"s_and_b32 s86, s86, 3\n"
"s_add_u32 s86, s86, 3\n"
"v_mfma_f32_32x32x16_bf16 v[100:115], a[156:159], a[72:75], v[100:115]\n"
"ds_read_b64_tr_b16 a[84:85], v220 offset:5120\n"
"ds_read_b64_tr_b16 a[86:87], v220 offset:5632\n"
"s_lshl_b32 s86, s86, 10\n"
"v_add_u32 v217, s86, v218\n"
"buffer_load_dwordx4 a[208:211], v218, s[48:51], s95 offen offset:2048 sc1\n"
"v_mfma_f32_32x32x16_bf16 v[20:35], a[100:103], a[72:75], v[20:35]\n"
"ds_read_b64_tr_b16 a[156:157], v221 offset:5120\n"
"ds_read_b64_tr_b16 a[158:159], v221 offset:5632\n"
"s_add_u32 s86, s76, 1\n"
"s_and_b32 s86, s86, 3\n"
"s_add_u32 s86, s86, 2\n"
"s_lshl_b32 s86, s86, 10\n"
"v_mfma_f32_32x32x16_bf16 v[120:135], a[164:167], a[72:75], v[120:135]\n"
"ds_read_b64_tr_b16 a[100:101], v222 offset:5120\n"
"ds_read_b64_tr_b16 a[102:103], v222 offset:5632\n"
"v_add_u32 v203, s86, v218\n"
"s_add_u32 s86, s76, 2\n"
"buffer_load_dwordx4 a[176:179], v218, s[48:51], s95 offen offset:3072 sc1\n"
"s_waitcnt lgkmcnt(8)\n"
"v_mfma_f32_32x32x16_bf16 v[76:91], a[92:95], a[68:71], v[76:91]\n"
"ds_read_b64_tr_b16 a[72:73], v219 offset:6144\n"
"ds_read_b64_tr_b16 a[74:75], v219 offset:6656\n"
"s_and_b32 s86, s86, 3\n"
"s_add_u32 s86, s86, 1\n"
"s_lshl_b32 s86, s86, 10\n"
"v_mfma_f32_32x32x16_bf16 v[100:115], a[80:83], a[68:71], v[100:115]\n"
"ds_read_b64_tr_b16 a[164:165], v220 offset:6144\n"
"ds_read_b64_tr_b16 a[166:167], v220 offset:6656\n"
"v_add_u32 v202, s86, v218\n"
"s_add_u32 s86, s76, 3\n"
"s_and_b32 s86, s86, 3\n"
"s_add_u32 s95, s96, 4096\n"
"buffer_load_dwordx4 a[80:83], v218, s[48:51], s95 offen sc1\n"
"v_mfma_f32_32x32x16_bf16 v[20:35], a[88:91], a[68:71], v[20:35]\n"
"ds_read_b64_tr_b16 a[148:149], v221 offset:6144\n"
"ds_read_b64_tr_b16 a[150:151], v221 offset:6656\n"
"s_add_u32 s86, s86, 0\n"
"s_lshl_b32 s86, s86, 10\n"
"v_add_u32 v229, s86, v218\n"
"s_lshl_b32 s86, s77, 1\n"
"v_mfma_f32_32x32x16_bf16 v[120:135], a[96:99], a[68:71], v[120:135]\n"
"ds_read_b64_tr_b16 a[104:105], v222 offset:6144\n"
"ds_read_b64_tr_b16 a[106:107], v222 offset:6656\n"
"s_add_u32 s86, s86, s27\n"
"s_lshl_b32 s86, s86, 2\n"
"buffer_load_dwordx4 a[96:99], v218, s[48:51], s95 offen offset:1024 sc1\n"
"s_waitcnt lgkmcnt(8)\n"
"v_mfma_f32_32x32x16_bf16 v[76:91], a[76:79], a[64:67], v[76:91]\n"
"ds_read_b64_tr_b16 a[68:69], v219 offset:7168\n"
"ds_read_b64_tr_b16 a[70:71], v219 offset:7680\n"
"s_add_u32 s86, s86, s76\n"
"s_mul_i32 s56, s86, 20480\n"
"s_mul_i32 s57, s26, 160\n"
"v_mfma_f32_32x32x16_bf16 v[100:115], a[84:87], a[64:67], v[100:115]\n"
"ds_read_b64_tr_b16 a[76:77], v220 offset:7168\n"
"ds_read_b64_tr_b16 a[78:79], v220 offset:7680\n"
"s_lshl_b32 s86, s27, 1\n"
"s_lshr_b32 s87, s76, 1\n"
"s_add_u32 s86, s86, s87\n"
"buffer_load_dwordx4 a[88:91], v218, s[48:51], s95 offen offset:2048 sc1\n"
"v_mfma_f32_32x32x16_bf16 v[20:35], a[156:159], a[64:67], v[20:35]\n"
"ds_read_b64_tr_b16 a[84:85], v221 offset:7168\n"
"ds_read_b64_tr_b16 a[86:87], v221 offset:7680\n"
"s_mul_i32 s86, s86, 40960\n"
"s_and_b32 s87, s76, 1\n"
"s_lshl_b32 s87, s87, 6\n"
"s_add_u32 s59, s86, s87\n"
"v_mfma_f32_32x32x16_bf16 v[120:135], a[100:103], a[64:67], v[120:135]\n"
"ds_read_b64_tr_b16 a[156:157], v222 offset:7168\n"
"ds_read_b64_tr_b16 a[158:159], v222 offset:7680\n"
"v_lshrrev_b32 v180, 4, v255\n"
"v_and_b32 v182, 15, v255\n"
"buffer_load_dwordx4 a[92:95], v218, s[48:51], s95 offen offset:3072 sc1\n"
"s_waitcnt lgkmcnt(8)\n"
"v_mfma_f32_32x32x16_bf16 v[76:91], a[72:75], a[60:63], v[76:91]\n"
"v_accvgpr_read_b32 v189, a1\n"
"s_barrier\n"
"v_mul_u32_u24 v181, 5120, v180\n"
"v_mfma_f32_32x32x16_bf16 v[100:115], a[164:167], a[60:63], v[100:115]\n"
"ds_read_b64_tr_b16 a[64:65], v219 offset:8192\n"
"ds_read_b64_tr_b16 a[66:67], v219 offset:8704\n"
"ds_read_b64_tr_b16 a[164:165], v220 offset:8192\n"
"v_add_u32 v181, s56, v181\n"
"buffer_load_dwordx2 v[60:61], v189, s[48:51], 0 offen sc1\n"
"v_mfma_f32_32x32x16_bf16 v[20:35], a[148:151], a[60:63], v[20:35]\n"
"ds_read_b64_tr_b16 a[166:167], v220 offset:8704\n"
"ds_read_b64_tr_b16 a[72:73], v221 offset:8192\n"
"ds_read_b64_tr_b16 a[74:75], v221 offset:8704\n"
"v_lshlrev_b32 v0, 1, v182\n"
"v_add_u32 v0, s57, v0\n"
"v_add_u32 v216, v181, v0\n"
"v_mfma_f32_32x32x16_bf16 v[120:135], a[104:107], a[60:63], v[120:135]\n"
"ds_read_b64_tr_b16 a[148:149], v222 offset:8192\n"
"ds_read_b64_tr_b16 a[150:151], v222 offset:8704\n"
"v_lshlrev_b32 v223, 1, v216\n"
"v_lshlrev_b32 v1, 4, v180\n"
"s_add_u32 s95, s96, 8192\n"
"buffer_load_dwordx4 a[160:163], v218, s[48:51], s95 offen sc1\n"
"s_waitcnt lgkmcnt(8)\n"
"v_mfma_f32_32x32x16_bf16 v[76:91], a[68:71], a[48:51], v[76:91]\n"
"ds_read_b64_tr_b16 a[60:61], v219 offset:9216\n"
"ds_read_b64_tr_b16 a[62:63], v219 offset:9728\n"
"v_add_u32 v1, s59, v1\n"
"ds_read_b128 v[40:43], v1 offset:40704\n"
"v_mfma_f32_32x32x16_bf16 v[100:115], a[76:79], a[48:51], v[100:115]\n"
"ds_read_b64_tr_b16 a[68:69], v220 offset:9216\n"
"ds_read_b64_tr_b16 a[70:71], v220 offset:9728\n"
"global_load_ushort v184, v0, s[24:25]\n"
"v_mfma_f32_32x32x16_bf16 v[20:35], a[84:87], a[48:51], v[20:35]\n"
"ds_read_b64_tr_b16 a[76:77], v221 offset:9216\n"
"ds_read_b64_tr_b16 a[78:79], v221 offset:9728\n"
"buffer_load_dwordx4 a[116:119], v218, s[48:51], s95 offen offset:1024 sc1\n"
"v_mfma_f32_32x32x16_bf16 v[120:135], a[156:159], a[48:51], v[120:135]\n"
"ds_read_b64_tr_b16 a[84:85], v222 offset:9216\n"
"ds_read_b64_tr_b16 a[86:87], v222 offset:9728\n"
"global_load_ushort v185, v0, s[24:25] offset:32\n"
"s_waitcnt lgkmcnt(9)\n"
"v_mfma_f32_32x32x16_bf16 v[76:91], a[64:67], a[56:59], v[76:91]\n"
"ds_read_b64_tr_b16 a[156:157], v219 offset:10240\n"
"ds_read_b64_tr_b16 a[158:159], v219 offset:10752\n"
"global_load_ushort v186, v0, s[24:25] offset:64\n"
"v_mfma_f32_32x32x16_bf16 v[100:115], a[164:167], a[56:59], v[100:115]\n"
"ds_read_b64_tr_b16 a[100:101], v220 offset:10240\n"
"ds_read_b64_tr_b16 a[102:103], v220 offset:10752\n"
"global_load_ushort v187, v0, s[24:25] offset:96\n"
"v_mfma_f32_32x32x16_bf16 v[20:35], a[72:75], a[56:59], v[20:35]\n"
"ds_read_b64_tr_b16 a[104:105], v221 offset:10240\n"
"ds_read_b64_tr_b16 a[106:107], v221 offset:10752\n"
"s_add_u32 s95, s96, 35840\n"
"buffer_load_dwordx4 a[216:219], v218, s[48:51], s95 offen sc1\n"
"v_mfma_f32_32x32x16_bf16 v[120:135], a[148:151], a[56:59], v[120:135]\n"
"ds_read_b64_tr_b16 a[72:73], v222 offset:10240\n"
"ds_read_b64_tr_b16 a[74:75], v222 offset:10752\n"
"global_load_ushort v188, v0, s[24:25] offset:128\n"
"s_waitcnt lgkmcnt(8)\n"
"v_mfma_f32_32x32x16_bf16 v[76:91], a[60:63], a[40:43], v[76:91]\n"
"ds_read_b64_tr_b16 a[48:49], v219 offset:11264\n"
"ds_read_b64_tr_b16 a[50:51], v219 offset:11776\n"
"buffer_load_ushort v0, v216, s[36:39], 0 offen\n"
"v_mfma_f32_32x32x16_bf16 v[100:115], a[68:71], a[40:43], v[100:115]\n"
"ds_read_b64_tr_b16 a[60:61], v220 offset:11264\n"
"ds_read_b64_tr_b16 a[62:63], v220 offset:11776\n"
"buffer_load_ushort v4, v216, s[36:39], 0 offen offset:32\n"
"v_mfma_f32_32x32x16_bf16 v[20:35], a[76:79], a[40:43], v[20:35]\n"
"ds_read_b64_tr_b16 a[64:65], v221 offset:11264\n"
"ds_read_b64_tr_b16 a[66:67], v221 offset:11776\n"
"buffer_load_ushort v8, v216, s[36:39], 0 offen offset:64\n"
"v_mfma_f32_32x32x16_bf16 v[120:135], a[84:87], a[40:43], v[120:135]\n"
"ds_read_b64_tr_b16 a[56:57], v222 offset:11264\n"
"ds_read_b64_tr_b16 a[58:59], v222 offset:11776\n"
"buffer_load_dwordx4 a[84:87], v218, s[48:51], s95 offen offset:1024 sc1\n"
"s_waitcnt lgkmcnt(8)\n"
"v_mfma_f32_32x32x16_bf16 v[76:91], a[156:159], a[32:35], v[76:91]\n"
"s_barrier\n"
"v_mfma_f32_32x32x16_bf16 v[100:115], a[100:103], a[32:35], v[100:115]\n"
"ds_read_b64_tr_b16 a[148:149], v219 offset:12288\n"
"ds_read_b64_tr_b16 a[150:151], v219 offset:12800\n"
"ds_read_b64_tr_b16 a[40:41], v220 offset:12288\n"
"buffer_load_ushort v12, v216, s[36:39], 0 offen offset:96\n"
"v_mfma_f32_32x32x16_bf16 v[20:35], a[104:107], a[32:35], v[20:35]\n"
"ds_read_b64_tr_b16 a[42:43], v220 offset:12800\n"
"ds_read_b64_tr_b16 a[164:165], v221 offset:12288\n"
"ds_read_b64_tr_b16 a[166:167], v221 offset:12800\n"
"buffer_load_dwordx4 a[100:103], v218, s[48:51], s95 offen offset:2048 sc1\n"
"v_mfma_f32_32x32x16_bf16 v[120:135], a[72:75], a[32:35], v[120:135]\n"
"ds_read_b64_tr_b16 a[156:157], v222 offset:12288\n"
"ds_read_b64_tr_b16 a[158:159], v222 offset:12800\n"
"buffer_load_ushort v16, v216, s[36:39], 0 offen offset:128\n"
"s_waitcnt lgkmcnt(8)\n"
"v_mfma_f32_32x32x16_bf16 v[76:91], a[48:51], a[16:19], v[76:91]\n"
"ds_read_b64_tr_b16 a[32:33], v219 offset:13312\n"
"ds_read_b64_tr_b16 a[34:35], v219 offset:13824\n"
"buffer_load_ushort v1, v216, s[36:39], 0 offen offset:1280\n"
"v_mfma_f32_32x32x16_bf16 v[100:115], a[60:63], a[16:19], v[100:115]\n"
"ds_read_b64_tr_b16 a[48:49], v220 offset:13312\n"
"ds_read_b64_tr_b16 a[50:51], v220 offset:13824\n"
"buffer_load_dwordx4 a[76:79], v218, s[48:51], s95 offen offset:3072 sc1\n"
"v_mfma_f32_32x32x16_bf16 v[20:35], a[64:67], a[16:19], v[20:35]\n"
"ds_read_b64_tr_b16 a[72:73], v221 offset:13312\n"
"ds_read_b64_tr_b16 a[74:75], v221 offset:13824\n"
"s_waitcnt vmcnt(24)\n"
"v_mov_b32 v62, s16\n"
"v_mov_b32 v63, s81\n"
"s_mov_b64 exec, 1\n"
"buffer_store_dwordx2 v[62:63], off, s[48:51], s28 sc1\n"
"s_mov_b64 exec, -1\n"
"v_mfma_f32_32x32x16_bf16 v[120:135], a[56:59], a[16:19], v[120:135]\n"
"ds_read_b64_tr_b16 a[60:61], v222 offset:13312\n"
"ds_read_b64_tr_b16 a[62:63], v222 offset:13824\n"
"buffer_load_ushort v5, v216, s[36:39], 0 offen offset:1312\n"
"s_waitcnt lgkmcnt(8)\n"
"v_mfma_f32_32x32x16_bf16 v[76:91], a[148:151], a[52:55], v[76:91]\n"
"ds_read_b64_tr_b16 a[56:57], v219 offset:14336\n"
"ds_read_b64_tr_b16 a[58:59], v219 offset:14848\n"
"buffer_load_ushort v9, v216, s[36:39], 0 offen offset:1344\n"
"v_mfma_f32_32x32x16_bf16 v[100:115], a[40:43], a[52:55], v[100:115]\n"
"ds_read_b64_tr_b16 a[104:105], v220 offset:14336\n"
"ds_read_b64_tr_b16 a[106:107], v220 offset:14848\n"
"buffer_load_ushort v13, v216, s[36:39], 0 offen offset:1376\n"
"v_mfma_f32_32x32x16_bf16 v[20:35], a[164:167], a[52:55], v[20:35]\n"
"ds_read_b64_tr_b16 a[64:65], v221 offset:14336\n"
"ds_read_b64_tr_b16 a[66:67], v221 offset:14848\n"
"buffer_load_ushort v17, v216, s[36:39], 0 offen offset:1408\n"
"v_mfma_f32_32x32x16_bf16 v[120:135], a[156:159], a[52:55], v[120:135]\n"
"ds_read_b64_tr_b16 a[148:149], v222 offset:14336\n"
"ds_read_b64_tr_b16 a[150:151], v222 offset:14848\n"
"buffer_load_ushort v2, v216, s[36:39], 0 offen offset:2560\n"
"s_waitcnt lgkmcnt(8)\n"
"v_mfma_f32_32x32x16_bf16 v[76:91], a[32:35], a[36:39], v[76:91]\n"
"ds_read_b64_tr_b16 a[156:157], v219 offset:15360\n"
"ds_read_b64_tr_b16 a[158:159], v219 offset:15872\n"
"buffer_load_ushort v6, v216, s[36:39], 0 offen offset:2592\n"
"v_mfma_f32_32x32x16_bf16 v[100:115], a[48:51], a[36:39], v[100:115]\n"
"ds_read_b64_tr_b16 a[52:53], v220 offset:15360\n"
"ds_read_b64_tr_b16 a[54:55], v220 offset:15872\n"
"s_add_u32 s95, s96, 39936\n"
"buffer_load_dwordx4 a[68:71], v218, s[48:51], s95 offen sc1\n"
"v_mfma_f32_32x32x16_bf16 v[20:35], a[72:75], a[36:39], v[20:35]\n"
"ds_read_b64_tr_b16 a[16:17], v221 offset:15360\n"
"ds_read_b64_tr_b16 a[18:19], v221 offset:15872\n"
"buffer_load_ushort v10, v216, s[36:39], 0 offen offset:2624\n"
"v_mfma_f32_32x32x16_bf16 v[120:135], a[60:63], a[36:39], v[120:135]\n"
"ds_read_b64_tr_b16 a[40:41], v222 offset:15360\n"
"ds_read_b64_tr_b16 a[42:43], v222 offset:15872\n"
"buffer_load_ushort v14, v216, s[36:39], 0 offen offset:2656\n"
"s_waitcnt lgkmcnt(8)\n"
"v_mfma_f32_32x32x16_bf16 v[76:91], a[56:59], a[28:31], v[76:91]\n"
"s_barrier\n"
"buffer_load_dwordx4 a[60:63], v218, s[48:51], s95 offen offset:1024 sc1\n"
"v_mfma_f32_32x32x16_bf16 v[100:115], a[104:107], a[28:31], v[100:115]\n"
"ds_read_b64_tr_b16 a[164:165], v219 offset:16384\n"
"ds_read_b64_tr_b16 a[166:167], v219 offset:16896\n"
"ds_read_b64_tr_b16 a[56:57], v220 offset:16384\n"
"buffer_load_ushort v18, v216, s[36:39], 0 offen offset:2688\n"
"v_mfma_f32_32x32x16_bf16 v[20:35], a[64:67], a[28:31], v[20:35]\n"
"ds_read_b64_tr_b16 a[58:59], v220 offset:16896\n"
"ds_read_b64_tr_b16 a[32:33], v221 offset:16384\n"
"ds_read_b64_tr_b16 a[34:35], v221 offset:16896\n"
"buffer_load_ushort v3, v216, s[36:39], 0 offen offset:3840\n"
"v_mfma_f32_32x32x16_bf16 v[120:135], a[148:151], a[28:31], v[120:135]\n"
"ds_read_b64_tr_b16 a[36:37], v222 offset:16384\n"
"ds_read_b64_tr_b16 a[38:39], v222 offset:16896\n"
"buffer_load_ushort v7, v216, s[36:39], 0 offen offset:3872\n"
"s_waitcnt lgkmcnt(8)\n"
"v_mfma_f32_32x32x16_bf16 v[76:91], a[156:159], a[20:23], v[76:91]\n"
"ds_read_b64_tr_b16 a[28:29], v219 offset:17408\n"
"ds_read_b64_tr_b16 a[30:31], v219 offset:17920\n"
"buffer_load_dwordx4 a[148:151], v218, s[48:51], s95 offen offset:2048 sc1\n"
"s_waitcnt vmcnt(32)\n"
"v_subrev_u32 v62, s16, v60\n"
"v_lshrrev_b32 v62, 1, v62\n"
"v_bitop3_b32 v62, v61, s81, v62 bitop3:0xbe\n"
"v_cmp_ne_u32 vcc, 0, v62\n"
"s_nop 1\n"
"s_cmp_lg_u32 vcc_lo, 0\n"
"s_cbranch_scc1 SLOWBH0_%=\n"
"BACKBH0_%=:\n"
"v_mfma_f32_32x32x16_bf16 v[100:115], a[52:55], a[20:23], v[100:115]\n"
"ds_read_b64_tr_b16 a[72:73], v220 offset:17408\n"
"ds_read_b64_tr_b16 a[74:75], v220 offset:17920\n"
"buffer_load_ushort v11, v216, s[36:39], 0 offen offset:3904\n"
"v_mfma_f32_32x32x16_bf16 v[20:35], a[16:19], a[20:23], v[20:35]\n"
"ds_read_b64_tr_b16 a[156:157], v221 offset:17408\n"
"ds_read_b64_tr_b16 a[158:159], v221 offset:17920\n"
"s_add_u32 s29, s93, 0\n"
"buffer_load_dwordx4 v[160:163], v217, s[48:51], s29 offen sc1\n"
"v_mfma_f32_32x32x16_bf16 v[120:135], a[40:43], a[20:23], v[120:135]\n"
"ds_read_b64_tr_b16 a[48:49], v222 offset:17408\n"
"ds_read_b64_tr_b16 a[50:51], v222 offset:17920\n"
"buffer_load_ushort v15, v216, s[36:39], 0 offen offset:3936\n"
"s_waitcnt lgkmcnt(8)\n"
"v_mfma_f32_32x32x16_bf16 v[76:91], a[164:167], a[44:47], v[76:91]\n"
"ds_read_b64_tr_b16 a[40:41], v219 offset:18432\n"
"ds_read_b64_tr_b16 a[42:43], v219 offset:18944\n"
"buffer_load_ushort v19, v216, s[36:39], 0 offen offset:3968\n"
"v_mfma_f32_32x32x16_bf16 v[100:115], a[56:59], a[44:47], v[100:115]\n"
"ds_read_b64_tr_b16 a[52:53], v220 offset:18432\n"
"ds_read_b64_tr_b16 a[54:55], v220 offset:18944\n"
"v_add_u32 v216, s79, v218\n"
"v_lshrrev_b32 v60, 5, v255\n"
"v_bfe_u32 v61, v255, 1, 1\n"
"v_mfma_f32_32x32x16_bf16 v[20:35], a[32:35], a[44:47], v[20:35]\n"
"ds_read_b64_tr_b16 a[16:17], v221 offset:18432\n"
"ds_read_b64_tr_b16 a[18:19], v221 offset:18944\n"
"buffer_load_dwordx4 a[56:59], v203, s[48:51], s29 offen offset:1024 sc1\n"
"v_lshl_add_u32 v60, v60, 1, v61\n"
"v_mfma_f32_32x32x16_bf16 v[120:135], a[36:39], a[44:47], v[120:135]\n"
"ds_read_b64_tr_b16 a[20:21], v222 offset:18432\n"
"ds_read_b64_tr_b16 a[22:23], v222 offset:18944\n"
"v_bfe_u32 v61, v255, 4, 1\n"
"v_lshl_add_u32 v60, v60, 1, v61\n"
"v_bfe_u32 v61, v255, 2, 2\n"
"s_waitcnt lgkmcnt(8)\n"
"v_mfma_f32_32x32x16_bf16 v[76:91], a[28:31], a[24:27], v[76:91]\n"
"ds_read_b64_tr_b16 a[36:37], v219 offset:19456\n"
"ds_read_b64_tr_b16 a[38:39], v219 offset:19968\n"
"buffer_load_dwordx4 v[36:39], v202, s[48:51], s29 offen offset:2048 sc1\n"
"v_mfma_f32_32x32x16_bf16 v[100:115], a[72:75], a[24:27], v[100:115]\n"
"ds_read_b64_tr_b16 a[28:29], v220 offset:19456\n"
"ds_read_b64_tr_b16 a[30:31], v220 offset:19968\n"
"v_lshl_add_u32 v60, v60, 3, v61\n"
"v_and_b32 v61, 1, v255\n"
"v_lshl_add_u32 v60, v60, 1, v61\n"
"v_mfma_f32_32x32x16_bf16 v[20:35], a[156:159], a[24:27], v[20:35]\n"
"ds_read_b64_tr_b16 a[44:45], v221 offset:19456\n"
"ds_read_b64_tr_b16 a[46:47], v221 offset:19968\n"
"buffer_load_dwordx4 v[92:95], v229, s[48:51], s29 offen offset:3072 sc1\n"
"v_lshlrev_b32 v60, 3, v60\n"
"v_mfma_f32_32x32x16_bf16 v[120:135], a[48:51], a[24:27], v[120:135]\n"
"ds_read_b64_tr_b16 a[32:33], v222 offset:19456\n"
"ds_read_b64_tr_b16 a[34:35], v222 offset:19968\n"
"v_add_u32 v224, s79, v60\n"
"s_mov_b32 s44, s32\n"
"s_and_b32 s45, s33, 0xffff\n"
"s_mov_b32 s46, 0x500000\n"
"s_waitcnt lgkmcnt(8)\n"
"v_mfma_f32_32x32x16_bf16 v[76:91], a[40:43], a[12:15], v[76:91]\n"
"s_mov_b32 s47, 0x20000\n"
"s_mov_b32 s88, 5120\n"
"s_sub_u32 s86, s76, 1\n"
"s_and_b32 s86, s86, 3\n"
"s_mul_i32 s86, s86, 40960\n"
"v_mfma_f32_32x32x16_bf16 v[100:115], a[52:55], a[12:15], v[100:115]\n"
"v_add_u32 v221, s86, v218\n"
"s_add_u32 s29, s93, 4096\n"
"buffer_load_dwordx4 a[24:27], v217, s[48:51], s29 offen sc1\n"
"s_sub_u32 s86, s76, 2\n"
"s_and_b32 s86, s86, 3\n"
"v_mfma_f32_32x32x16_bf16 v[20:35], a[16:19], a[12:15], v[20:35]\n"
"s_mul_i32 s86, s86, 40960\n"
"v_add_u32 v220, s86, v218\n"
"s_sub_u32 s86, s76, 3\n"
"s_and_b32 s86, s86, 3\n"
"s_mul_i32 s86, s86, 40960\n"
"buffer_load_dwordx4 a[16:19], v203, s[48:51], s29 offen offset:1024 sc1\n"
"v_mfma_f32_32x32x16_bf16 v[120:135], a[20:23], a[12:15], v[120:135]\n"
"v_add_u32 v219, s86, v218\n"
"s_lshl_b32 s86, s77, 1\n"
"s_add_u32 s86, s86, s27\n"
"s_lshl_b32 s86, s86, 3\n"
"s_add_u32 s86, s86, s26\n"
"s_lshl_b32 s86, s86, 2\n"
"s_waitcnt lgkmcnt(0)\n"
"v_mfma_f32_32x32x16_bf16 v[76:91], a[36:39], a[8:11], v[76:91]\n"
"s_add_u32 s86, s86, s76\n"
"s_xor_b32 s87, s86, 32\n"
"s_mul_i32 s60, s86, 5120\n"
"s_mul_i32 s61, s87, 5120\n"
"s_add_u32 s60, s60, 15728640\n"
"buffer_load_dwordx4 a[36:39], v202, s[48:51], s29 offen offset:2048 sc1\n"
"v_mfma_f32_32x32x16_bf16 v[100:115], a[28:31], a[8:11], v[100:115]\n"
"s_add_u32 s61, s61, 15728640\n"
"s_add_u32 s62, s60, 4096\n"
"s_add_u32 s63, s61, 4096\n"
"v_mfma_f32_32x32x16_bf16 v[20:35], a[44:47], a[8:11], v[20:35]\n"
"v_mfma_f32_32x32x16_bf16 v[120:135], a[32:35], a[8:11], v[120:135]\n"
"buffer_load_dwordx4 a[28:31], v229, s[48:51], s29 offen offset:3072 sc1\n"
"v_mfma_f32_32x32x16_bf16 v[76:91], v[136:139], v[156:159], v[76:91]\n"
"v_mfma_f32_32x32x16_bf16 v[100:115], v[116:119], v[156:159], v[100:115]\n"
"s_add_u32 s29, s97, 0\n"
"buffer_load_dwordx4 a[8:11], v217, s[48:51], s29 offen sc1\n"
"v_mfma_f32_32x32x16_bf16 v[20:35], v[192:195], v[156:159], v[20:35]\n"
"v_mfma_f32_32x32x16_bf16 v[120:135], v[196:199], v[156:159], v[120:135]\n"
"s_barrier\n"
"s_nop 5\n"
"v_pk_mul_f32 v[60:61], v[76:77], v[76:77]\n"
"v_pk_mul_f32 v[62:63], v[78:79], v[78:79]\n"
"buffer_load_dwordx4 a[12:15], v203, s[48:51], s29 offen offset:1024 sc1\n"
"v_pk_mul_f32 v[64:65], v[80:81], v[80:81]\n"
"v_pk_mul_f32 v[66:67], v[82:83], v[82:83]\n"
"v_pk_fma_f32 v[60:61], v[60:61], v[200:201], v[252:253] op_sel_hi:[1,0,0]\n"
"v_pk_fma_f32 v[62:63], v[62:63], v[200:201], v[252:253] op_sel_hi:[1,0,0]\n"
"v_pk_fma_f32 v[64:65], v[64:65], v[200:201], v[252:253] op_sel_hi:[1,0,0]\n"
"v_pk_fma_f32 v[66:67], v[66:67], v[200:201], v[252:253] op_sel_hi:[1,0,0]\n"
"v_pk_mul_f32 v[60:61], v[60:61], v[76:77]\n"
"v_pk_mul_f32 v[62:63], v[62:63], v[78:79]\n"
"v_pk_mul_f32 v[64:65], v[64:65], v[80:81]\n"
"v_pk_mul_f32 v[66:67], v[66:67], v[82:83]\n"
"buffer_load_dwordx4 a[32:35], v202, s[48:51], s29 offen offset:2048 sc1\n"
"v_exp_f32 v60, v60\n"
"v_exp_f32 v61, v61\n"
"v_pk_mul_f32 v[84:85], v[76:77], v[84:85]\n"
"v_exp_f32 v62, v62\n"
"v_exp_f32 v63, v63\n"
"v_pk_mul_f32 v[86:87], v[78:79], v[86:87]\n"
"v_exp_f32 v64, v64\n"
"v_exp_f32 v65, v65\n"
"v_pk_mul_f32 v[88:89], v[80:81], v[88:89]\n"
"v_exp_f32 v66, v66\n"
"v_exp_f32 v67, v67\n"
"v_pk_mul_f32 v[90:91], v[82:83], v[90:91]\n"
"v_pk_fma_f32 v[60:61], v[60:61], v[250:251], v[250:251] op_sel_hi:[1,0,0]\n"
"buffer_load_dwordx4 a[52:55], v229, s[48:51], s29 offen offset:3072 sc1\n"
"v_pk_fma_f32 v[62:63], v[62:63], v[250:251], v[250:251] op_sel_hi:[1,0,0]\n"
"v_pk_fma_f32 v[64:65], v[64:65], v[250:251], v[250:251] op_sel_hi:[1,0,0]\n"
"v_pk_fma_f32 v[66:67], v[66:67], v[250:251], v[250:251] op_sel_hi:[1,0,0]\n"
"v_rcp_f32 v60, v60\n"
"v_rcp_f32 v61, v61\n"
"v_rcp_f32 v62, v62\n"
"v_rcp_f32 v63, v63\n"
"v_rcp_f32 v64, v64\n"
"v_rcp_f32 v65, v65\n"
"v_rcp_f32 v66, v66\n"
"v_rcp_f32 v67, v67\n"
"v_pk_mul_f32 v[84:85], v[84:85], v[60:61]\n"
"v_accvgpr_read_b32 v68, a0\n"
"v_pk_mul_f32 v[86:87], v[86:87], v[62:63]\n"
"buffer_load_dwordx2 v[136:137], v189, s[48:51], 0 offen sc1\n"
"v_pk_mul_f32 v[88:89], v[88:89], v[64:65]\n"
"v_pk_mul_f32 v[90:91], v[90:91], v[66:67]\n"
"v_cvt_pk_bf16_f32 v156, v84, v85\n"
"v_cvt_pk_bf16_f32 v157, v86, v87\n"
"v_cvt_pk_bf16_f32 v158, v88, v89\n"
"v_cvt_pk_bf16_f32 v159, v90, v91\n"
"s_add_u32 s14, s92, 626688\n"
"buffer_store_dwordx4 v[156:159], v68, s[48:51], s14 offen sc1\n"
"v_pk_mul_f32 v[60:61], v[100:101], v[100:101]\n"
"v_pk_mul_f32 v[62:63], v[102:103], v[102:103]\n"
"v_pk_mul_f32 v[64:65], v[104:105], v[104:105]\n"
"v_pk_mul_f32 v[66:67], v[106:107], v[106:107]\n"
"v_pk_fma_f32 v[60:61], v[60:61], v[200:201], v[252:253] op_sel_hi:[1,0,0]\n"
"v_pk_fma_f32 v[62:63], v[62:63], v[200:201], v[252:253] op_sel_hi:[1,0,0]\n"
"v_pk_fma_f32 v[64:65], v[64:65], v[200:201], v[252:253] op_sel_hi:[1,0,0]\n"
"v_pk_fma_f32 v[66:67], v[66:67], v[200:201], v[252:253] op_sel_hi:[1,0,0]\n"
"v_pk_mul_f32 v[60:61], v[60:61], v[100:101]\n"
"v_pk_mul_f32 v[62:63], v[62:63], v[102:103]\n"
"v_pk_mul_f32 v[64:65], v[64:65], v[104:105]\n"
"v_pk_mul_f32 v[66:67], v[66:67], v[106:107]\n"
"v_exp_f32 v60, v60\n"
"v_exp_f32 v61, v61\n"
"v_pk_mul_f32 v[108:109], v[100:101], v[108:109]\n"
"v_exp_f32 v62, v62\n"
"v_exp_f32 v63, v63\n"
"s_add_u32 s29, s97, 4096\n"
"buffer_load_dwordx4 a[44:47], v217, s[48:51], s29 offen sc1\n"
"v_pk_mul_f32 v[110:111], v[102:103], v[110:111]\n"
"v_exp_f32 v64, v64\n"
"v_exp_f32 v65, v65\n"
"v_pk_mul_f32 v[112:113], v[104:105], v[112:113]\n"
"v_exp_f32 v66, v66\n"
"v_exp_f32 v67, v67\n"
"v_pk_mul_f32 v[114:115], v[106:107], v[114:115]\n"
"v_pk_fma_f32 v[60:61], v[60:61], v[250:251], v[250:251] op_sel_hi:[1,0,0]\n"
"v_pk_fma_f32 v[62:63], v[62:63], v[250:251], v[250:251] op_sel_hi:[1,0,0]\n"
"v_pk_fma_f32 v[64:65], v[64:65], v[250:251], v[250:251] op_sel_hi:[1,0,0]\n"
"v_pk_fma_f32 v[66:67], v[66:67], v[250:251], v[250:251] op_sel_hi:[1,0,0]\n"
"v_rcp_f32 v60, v60\n"
"v_rcp_f32 v61, v61\n"
"v_rcp_f32 v62, v62\n"
"buffer_load_dwordx4 a[40:43], v203, s[48:51], s29 offen offset:1024 sc1\n"
"v_rcp_f32 v63, v63\n"
"v_rcp_f32 v64, v64\n"
"v_rcp_f32 v65, v65\n"
"v_rcp_f32 v66, v66\n"
"v_rcp_f32 v67, v67\n"
"v_pk_mul_f32 v[108:109], v[108:109], v[60:61]\n"
"v_pk_mul_f32 v[110:111], v[110:111], v[62:63]\n"
"v_pk_mul_f32 v[112:113], v[112:113], v[64:65]\n"
"v_pk_mul_f32 v[114:115], v[114:115], v[66:67]\n"
"v_cvt_pk_bf16_f32 v76, v108, v109\n"
"v_cvt_pk_bf16_f32 v77, v110, v111\n"
"v_cvt_pk_bf16_f32 v78, v112, v113\n"
"v_cvt_pk_bf16_f32 v79, v114, v115\n"
"buffer_store_dwordx4 v[76:79], v68, s[48:51], s14 offen offset:512 sc1\n"
"v_pk_mul_f32 v[60:61], v[20:21], v[20:21]\n"
"v_pk_mul_f32 v[62:63], v[22:23], v[22:23]\n"
"v_pk_mul_f32 v[64:65], v[24:25], v[24:25]\n"
"v_pk_mul_f32 v[66:67], v[26:27], v[26:27]\n"
"v_pk_fma_f32 v[60:61], v[60:61], v[200:201], v[252:253] op_sel_hi:[1,0,0]\n"
"v_pk_fma_f32 v[62:63], v[62:63], v[200:201], v[252:253] op_sel_hi:[1,0,0]\n"
"v_pk_fma_f32 v[64:65], v[64:65], v[200:201], v[252:253] op_sel_hi:[1,0,0]\n"
"v_pk_fma_f32 v[66:67], v[66:67], v[200:201], v[252:253] op_sel_hi:[1,0,0]\n"
"v_pk_mul_f32 v[60:61], v[60:61], v[20:21]\n"
"v_pk_mul_f32 v[62:63], v[62:63], v[22:23]\n"
"buffer_load_dwordx4 a[20:23], v202, s[48:51], s29 offen offset:2048 sc1\n"
"v_pk_mul_f32 v[64:65], v[64:65], v[24:25]\n"
"v_pk_mul_f32 v[66:67], v[66:67], v[26:27]\n"
"v_exp_f32 v60, v60\n"
"v_exp_f32 v61, v61\n"
"v_pk_mul_f32 v[28:29], v[20:21], v[28:29]\n"
"v_exp_f32 v62, v62\n"
"v_exp_f32 v63, v63\n"
"v_pk_mul_f32 v[30:31], v[22:23], v[30:31]\n"
"v_exp_f32 v64, v64\n"
"v_exp_f32 v65, v65\n"
"v_pk_mul_f32 v[32:33], v[24:25], v[32:33]\n"
"v_exp_f32 v66, v66\n"
"v_exp_f32 v67, v67\n"
"buffer_load_dwordx4 a[48:51], v229, s[48:51], s29 offen offset:3072 sc1\n"
"v_pk_mul_f32 v[34:35], v[26:27], v[34:35]\n"
"v_pk_fma_f32 v[60:61], v[60:61], v[250:251], v[250:251] op_sel_hi:[1,0,0]\n"
"v_pk_fma_f32 v[62:63], v[62:63], v[250:251], v[250:251] op_sel_hi:[1,0,0]\n"
"v_pk_fma_f32 v[64:65], v[64:65], v[250:251], v[250:251] op_sel_hi:[1,0,0]\n"
"v_pk_fma_f32 v[66:67], v[66:67], v[250:251], v[250:251] op_sel_hi:[1,0,0]\n"
"v_rcp_f32 v60, v60\n"
"v_rcp_f32 v61, v61\n"
"v_rcp_f32 v62, v62\n"
"v_rcp_f32 v63, v63\n"
"v_rcp_f32 v64, v64\n"
"v_rcp_f32 v65, v65\n"
"v_rcp_f32 v66, v66\n"
"v_rcp_f32 v67, v67\n"
"buffer_load_dwordx4 v[196:199], v218, s[48:51], s95 offen offset:3072 sc1\n"
"v_pk_mul_f32 v[28:29], v[28:29], v[60:61]\n"
"v_pk_mul_f32 v[30:31], v[30:31], v[62:63]\n"
"v_pk_mul_f32 v[32:33], v[32:33], v[64:65]\n"
"v_pk_mul_f32 v[34:35], v[34:35], v[66:67]\n"
"v_cvt_pk_bf16_f32 v76, v28, v29\n"
"v_cvt_pk_bf16_f32 v77, v30, v31\n"
"v_cvt_pk_bf16_f32 v78, v32, v33\n"
"v_cvt_pk_bf16_f32 v79, v34, v35\n"
"s_add_u32 s14, s92, 630784\n"
"buffer_store_dwordx4 v[76:79], v68, s[48:51], s14 offen sc1\n"
"v_pk_mul_f32 v[60:61], v[120:121], v[120:121]\n"
"v_pk_mul_f32 v[62:63], v[122:123], v[122:123]\n"
"v_pk_mul_f32 v[64:65], v[124:125], v[124:125]\n"
"v_pk_mul_f32 v[66:67], v[126:127], v[126:127]\n"
"v_pk_fma_f32 v[60:61], v[60:61], v[200:201], v[252:253] op_sel_hi:[1,0,0]\n"
"v_pk_fma_f32 v[62:63], v[62:63], v[200:201], v[252:253] op_sel_hi:[1,0,0]\n"
"v_pk_fma_f32 v[64:65], v[64:65], v[200:201], v[252:253] op_sel_hi:[1,0,0]\n"
"v_pk_fma_f32 v[66:67], v[66:67], v[200:201], v[252:253] op_sel_hi:[1,0,0]\n"
"v_pk_mul_f32 v[60:61], v[60:61], v[120:121]\n"
"v_pk_mul_f32 v[62:63], v[62:63], v[122:123]\n"
"v_pk_mul_f32 v[64:65], v[64:65], v[124:125]\n"
"v_pk_mul_f32 v[66:67], v[66:67], v[126:127]\n"
"v_exp_f32 v60, v60\n"
"v_exp_f32 v61, v61\n"
"v_pk_mul_f32 v[128:129], v[120:121], v[128:129]\n"
"v_exp_f32 v62, v62\n"
"s_add_u32 s95, s96, 44032\n"
"buffer_load_dwordx4 v[192:195], v218, s[48:51], s95 offen sc1\n"
"v_exp_f32 v63, v63\n"
"v_pk_mul_f32 v[130:131], v[122:123], v[130:131]\n"
"v_exp_f32 v64, v64\n"
"v_exp_f32 v65, v65\n"
"v_pk_mul_f32 v[132:133], v[124:125], v[132:133]\n"
"v_exp_f32 v66, v66\n"
"v_exp_f32 v67, v67\n"
"v_pk_mul_f32 v[134:135], v[126:127], v[134:135]\n"
"v_pk_fma_f32 v[60:61], v[60:61], v[250:251], v[250:251] op_sel_hi:[1,0,0]\n"
"v_pk_fma_f32 v[62:63], v[62:63], v[250:251], v[250:251] op_sel_hi:[1,0,0]\n"
"v_pk_fma_f32 v[64:65], v[64:65], v[250:251], v[250:251] op_sel_hi:[1,0,0]\n"
"v_pk_fma_f32 v[66:67], v[66:67], v[250:251], v[250:251] op_sel_hi:[1,0,0]\n"
"v_rcp_f32 v60, v60\n"
"buffer_load_dwordx4 a[156:159], v218, s[48:51], s95 offen offset:1024 sc1\n"
"v_rcp_f32 v61, v61\n"
"v_rcp_f32 v62, v62\n"
"v_rcp_f32 v63, v63\n"
"v_rcp_f32 v64, v64\n"
"v_rcp_f32 v65, v65\n"
"v_rcp_f32 v66, v66\n"
"v_rcp_f32 v67, v67\n"
"v_pk_mul_f32 v[128:129], v[128:129], v[60:61]\n"
"v_pk_mul_f32 v[130:131], v[130:131], v[62:63]\n"
"v_pk_mul_f32 v[132:133], v[132:133], v[64:65]\n"
"v_pk_mul_f32 v[134:135], v[134:135], v[66:67]\n"
"v_cvt_pk_bf16_f32 v60, v128, v129\n"
"s_waitcnt vmcnt(10)\n"
"v_subrev_u32 v64, s16, v136\n"
"v_lshrrev_b32 v64, 1, v64\n"
"v_bitop3_b32 v64, v137, s81, v64 bitop3:0xbe\n"
"v_cmp_ne_u32 vcc, 0, v64\n"
"s_nop 1\n"
"s_cmp_lg_u32 vcc_hi, 0\n"
"s_cbranch_scc1 SLOWBH3_%=\n"
"BACKBH3_%=:\n"
"s_add_u32 m0, s79, 0\n"
"s_add_u32 s29, s93, 8192\n"
"buffer_load_dwordx4 v217, s[48:51], s29 offen sc1 lds\n"
"v_cvt_pk_bf16_f32 v61, v130, v131\n"
"v_cvt_pk_bf16_f32 v62, v132, v133\n"
"v_cvt_pk_bf16_f32 v63, v134, v135\n"
"buffer_store_dwordx4 v[60:63], v68, s[48:51], s14 offen offset:512 sc1\n"
"s_nop 1\n"
"v_mfma_f32_16x16x32_bf16 v[60:63], v[160:163], a[172:175], 0\n"
"v_lshlrev_b32 v184, 16, v184\n"
"v_mfma_f32_16x16x32_bf16 v[64:67], v[160:163], v[236:239], 0\n"
"v_lshlrev_b32 v185, 16, v185\n"
"v_lshlrev_b32 v186, 16, v186\n"
"v_mfma_f32_16x16x32_bf16 v[68:71], v[160:163], a[208:211], 0\n"
"v_lshlrev_b32 v187, 16, v187\n"
"v_lshlrev_b32 v188, 16, v188\n"
"v_mfma_f32_16x16x32_bf16 v[72:75], v[160:163], a[176:179], 0\n"
"v_and_b32 v180, s82, v40\n"
"v_mfma_f32_16x16x32_bf16 v[76:79], v[160:163], a[80:83], 0\n"
"v_rcp_f32 v180, v180\n"
"v_mfma_f32_16x16x32_bf16 v[160:163], a[56:59], a[172:175], 0\n"
"v_lshlrev_b32 v181, 16, v40\n"
"v_mfma_f32_16x16x32_bf16 v[164:167], a[56:59], v[236:239], 0\n"
"buffer_load_dwordx4 v203, s[48:51], s29 offen offset:1024 sc1 lds\n"
"v_mfma_f32_16x16x32_bf16 v[168:171], a[56:59], a[208:211], 0\n"
"v_lshlrev_b32 v0, 16, v0\n"
"v_lshlrev_b32 v4, 16, v4\n"
"v_mfma_f32_16x16x32_bf16 v[172:175], a[56:59], a[176:179], 0\n"
"v_lshlrev_b32 v8, 16, v8\n"
"v_lshlrev_b32 v12, 16, v12\n"
"v_mfma_f32_16x16x32_bf16 v[176:179], a[56:59], a[80:83], 0\n"
"v_lshlrev_b32 v16, 16, v16\n"
"v_add_f32 v182, v0, v181\n"
"v_mfma_f32_16x16x32_bf16 v[140:143], v[36:39], a[172:175], 0\n"
"buffer_load_dwordx4 v202, s[48:51], s29 offen offset:2048 sc1 lds\n"
"v_mfma_f32_16x16x32_bf16 v[144:147], v[36:39], v[236:239], 0\n"
"v_mul_f32 v182, v182, v180\n"
"v_fmac_f32 v0, v182, v184\n"
"v_mfma_f32_16x16x32_bf16 v[148:151], v[36:39], a[208:211], 0\n"
"v_add_f32 v183, v4, v181\n"
"v_mul_f32 v183, v183, v180\n"
"v_mfma_f32_16x16x32_bf16 v[152:155], v[36:39], a[176:179], 0\n"
"v_fmac_f32 v4, v183, v185\n"
"v_add_f32 v182, v8, v181\n"
"v_mfma_f32_16x16x32_bf16 v[156:159], v[36:39], a[80:83], 0\n"
"v_mul_f32 v182, v182, v180\n"
"buffer_load_dwordx4 v229, s[48:51], s29 offen offset:3072 sc1 lds\n"
"v_mfma_f32_16x16x32_bf16 v[120:123], v[92:95], a[172:175], 0\n"
"v_fmac_f32 v8, v182, v186\n"
"v_mfma_f32_16x16x32_bf16 v[124:127], v[92:95], v[236:239], 0\n"
"v_add_f32 v183, v12, v181\n"
"v_mul_f32 v183, v183, v180\n"
"v_mfma_f32_16x16x32_bf16 v[128:131], v[92:95], a[208:211], 0\n"
"v_fmac_f32 v12, v183, v187\n"
"v_add_f32 v182, v16, v181\n"
"v_mfma_f32_16x16x32_bf16 v[132:135], v[92:95], a[176:179], 0\n"
"v_mul_f32 v182, v182, v180\n"
"s_add_u32 m0, s79, 4096\n"
"s_add_u32 s29, s93, 12288\n"
"buffer_load_dwordx4 v217, s[48:51], s29 offen sc1 lds\n"
"v_mfma_f32_16x16x32_bf16 v[136:139], v[92:95], a[80:83], 0\n"
"v_fmac_f32 v16, v182, v188\n"
"v_and_b32 v180, s82, v41\n"
"v_mfma_f32_16x16x32_bf16 v[60:63], a[24:27], a[96:99], v[60:63]\n"
"v_mfma_f32_16x16x32_bf16 v[64:67], a[24:27], a[88:91], v[64:67]\n"
"v_rcp_f32 v180, v180\n"
"v_mfma_f32_16x16x32_bf16 v[68:71], a[24:27], a[92:95], v[68:71]\n"
"v_lshlrev_b32 v181, 16, v41\n"
"v_lshlrev_b32 v1, 16, v1\n"
"buffer_load_dwordx4 v203, s[48:51], s29 offen offset:1024 sc1 lds\n"
"v_mfma_f32_16x16x32_bf16 v[72:75], a[24:27], a[160:163], v[72:75]\n"
"v_lshlrev_b32 v5, 16, v5\n"
"v_lshlrev_b32 v9, 16, v9\n"
"v_mfma_f32_16x16x32_bf16 v[76:79], a[24:27], a[116:119], v[76:79]\n"
"v_lshlrev_b32 v13, 16, v13\n"
"v_lshlrev_b32 v17, 16, v17\n"
"v_mfma_f32_16x16x32_bf16 v[160:163], a[16:19], a[96:99], v[160:163]\n"
"v_add_f32 v182, v1, v181\n"
"v_mfma_f32_16x16x32_bf16 v[164:167], a[16:19], a[88:91], v[164:167]\n"
"v_mul_f32 v182, v182, v180\n"
"buffer_load_dwordx4 v202, s[48:51], s29 offen offset:2048 sc1 lds\n"
"v_mfma_f32_16x16x32_bf16 v[168:171], a[16:19], a[92:95], v[168:171]\n"
"v_fmac_f32 v1, v182, v184\n"
"v_add_f32 v183, v5, v181\n"
"v_mfma_f32_16x16x32_bf16 v[172:175], a[16:19], a[160:163], v[172:175]\n"
"v_mul_f32 v183, v183, v180\n"
"v_fmac_f32 v5, v183, v185\n"
"v_mfma_f32_16x16x32_bf16 v[176:179], a[16:19], a[116:119], v[176:179]\n"
"v_add_f32 v182, v9, v181\n"
"v_mul_f32 v182, v182, v180\n"
"v_mfma_f32_16x16x32_bf16 v[140:143], a[36:39], a[96:99], v[140:143]\n"
"v_fmac_f32 v9, v182, v186\n"
"buffer_load_dwordx4 v229, s[48:51], s29 offen offset:3072 sc1 lds\n"
"v_mfma_f32_16x16x32_bf16 v[144:147], a[36:39], a[88:91], v[144:147]\n"
"v_add_f32 v183, v13, v181\n"
"v_mul_f32 v183, v183, v180\n"
"v_mfma_f32_16x16x32_bf16 v[148:151], a[36:39], a[92:95], v[148:151]\n"
"v_fmac_f32 v13, v183, v187\n"
"v_add_f32 v182, v17, v181\n"
"v_mfma_f32_16x16x32_bf16 v[152:155], a[36:39], a[160:163], v[152:155]\n"
"v_mul_f32 v182, v182, v180\n"
"v_fmac_f32 v17, v182, v188\n"
"v_mfma_f32_16x16x32_bf16 v[156:159], a[36:39], a[116:119], v[156:159]\n"
"v_and_b32 v180, s82, v42\n"
"s_add_u32 m0, s79, 8192\n"
"s_add_u32 s29, s93, 16384\n"
"buffer_load_dwordx4 v217, s[48:51], s29 offen sc1 lds\n"
"v_mfma_f32_16x16x32_bf16 v[120:123], a[28:31], a[96:99], v[120:123]\n"
"v_mfma_f32_16x16x32_bf16 v[124:127], a[28:31], a[88:91], v[124:127]\n"
"v_rcp_f32 v180, v180\n"
"v_mfma_f32_16x16x32_bf16 v[128:131], a[28:31], a[92:95], v[128:131]\n"
"v_lshlrev_b32 v181, 16, v42\n"
"v_lshlrev_b32 v2, 16, v2\n"
"v_mfma_f32_16x16x32_bf16 v[132:135], a[28:31], a[160:163], v[132:135]\n"
"v_lshlrev_b32 v6, 16, v6\n"
"v_lshlrev_b32 v10, 16, v10\n"
"buffer_load_dwordx4 v203, s[48:51], s29 offen offset:1024 sc1 lds\n"
"v_mfma_f32_16x16x32_bf16 v[136:139], a[28:31], a[116:119], v[136:139]\n"
"v_lshlrev_b32 v14, 16, v14\n"
"v_lshlrev_b32 v18, 16, v18\n"
"v_mfma_f32_16x16x32_bf16 v[20:23], a[8:11], a[172:175], 0\n"
"v_add_f32 v182, v2, v181\n"
"v_mfma_f32_16x16x32_bf16 v[24:27], a[8:11], v[236:239], 0\n"
"v_mul_f32 v182, v182, v180\n"
"v_fmac_f32 v2, v182, v184\n"
"v_mfma_f32_16x16x32_bf16 v[28:31], a[8:11], a[208:211], 0\n"
"v_add_f32 v183, v6, v181\n"
"buffer_load_dwordx4 v202, s[48:51], s29 offen offset:2048 sc1 lds\n"
"v_mfma_f32_16x16x32_bf16 v[32:35], a[8:11], a[176:179], 0\n"
"v_mul_f32 v183, v183, v180\n"
"v_fmac_f32 v6, v183, v185\n"
"v_mfma_f32_16x16x32_bf16 v[36:39], a[8:11], a[80:83], 0\n"
"v_add_f32 v182, v10, v181\n"
"v_mul_f32 v182, v182, v180\n"
"s_waitcnt vmcnt(20)\n"
"v_mfma_f32_16x16x32_bf16 v[100:103], a[12:15], a[172:175], 0\n"
"v_fmac_f32 v10, v182, v186\n"
"v_mfma_f32_16x16x32_bf16 v[104:107], a[12:15], v[236:239], 0\n"
"v_add_f32 v183, v14, v181\n"
"v_mul_f32 v183, v183, v180\n"
"buffer_load_dwordx4 v229, s[48:51], s29 offen offset:3072 sc1 lds\n"
"v_mfma_f32_16x16x32_bf16 v[108:111], a[12:15], a[208:211], 0\n"
"v_fmac_f32 v14, v183, v187\n"
"v_add_f32 v182, v18, v181\n"
"v_mfma_f32_16x16x32_bf16 v[112:115], a[12:15], a[176:179], 0\n"
"v_mul_f32 v182, v182, v180\n"
"v_fmac_f32 v18, v182, v188\n"
"v_mfma_f32_16x16x32_bf16 v[116:119], a[12:15], a[80:83], 0\n"
"v_and_b32 v180, s82, v43\n"
"s_waitcnt vmcnt(20)\n"
"v_mfma_f32_16x16x32_bf16 v[80:83], a[32:35], a[172:175], 0\n"
"s_add_u32 m0, s79, 12288\n"
"s_add_u32 s29, s93, 20480\n"
"buffer_load_dwordx4 v217, s[48:51], s29 offen sc1 lds\n"
"v_mfma_f32_16x16x32_bf16 v[84:87], a[32:35], v[236:239], 0\n"
"v_rcp_f32 v180, v180\n"
"v_mfma_f32_16x16x32_bf16 v[88:91], a[32:35], a[208:211], 0\n"
"v_lshlrev_b32 v181, 16, v43\n"
"v_lshlrev_b32 v3, 16, v3\n"
"v_mfma_f32_16x16x32_bf16 v[92:95], a[32:35], a[176:179], 0\n"
"v_lshlrev_b32 v7, 16, v7\n"
"v_lshlrev_b32 v11, 16, v11\n"
"v_mfma_f32_16x16x32_bf16 v[96:99], a[32:35], a[80:83], 0\n"
"v_lshlrev_b32 v15, 16, v15\n"
"v_lshlrev_b32 v19, 16, v19\n"
"buffer_load_dwordx4 v203, s[48:51], s29 offen offset:1024 sc1 lds\n"
"s_waitcnt vmcnt(20)\n"
"v_mfma_f32_16x16x32_bf16 v[40:43], a[52:55], a[172:175], 0\n"
"v_add_f32 v182, v3, v181\n"
"v_mfma_f32_16x16x32_bf16 v[44:47], a[52:55], v[236:239], 0\n"
"v_mul_f32 v182, v182, v180\n"
"v_fmac_f32 v3, v182, v184\n"
"v_mfma_f32_16x16x32_bf16 v[48:51], a[52:55], a[208:211], 0\n"
"v_add_f32 v183, v7, v181\n"
"v_mul_f32 v183, v183, v180\n"
"v_mfma_f32_16x16x32_bf16 v[52:55], a[52:55], a[176:179], 0\n"
"v_fmac_f32 v7, v183, v185\n"
"s_add_u32 s89, s16, 1\n"
"s_waitcnt vmcnt(13)\n"
"v_mov_b32 v250, s89\n"
"v_mov_b32 v251, s81\n"
"s_mov_b64 exec, 1\n"
"buffer_store_dwordx2 v[250:251], off, s[48:51], s28 sc1\n"
"s_mov_b64 exec, -1\n"
"v_mfma_f32_16x16x32_bf16 v[56:59], a[52:55], a[80:83], 0\n"
"v_add_f32 v182, v11, v181\n"
"v_mul_f32 v182, v182, v180\n"
"v_fmac_f32 v11, v182, v186\n"
"v_add_f32 v183, v15, v181\n"
"v_mul_f32 v183, v183, v180\n"
"v_fmac_f32 v15, v183, v187\n"
"v_add_f32 v182, v19, v181\n"
"v_mul_f32 v182, v182, v180\n"
"v_fmac_f32 v19, v182, v188\n"
"buffer_load_dwordx4 v202, s[48:51], s29 offen offset:2048 sc1 lds\n"
"v_mfma_f32_16x16x32_bf16 v[20:23], a[44:47], a[96:99], v[20:23]\n"
"s_waitcnt vmcnt(12)\n"
"v_mfma_f32_16x16x32_bf16 v[24:27], a[44:47], a[88:91], v[24:27]\n"
"ds_read_b64_tr_b16 a[28:29], v224\n"
"v_mfma_f32_16x16x32_bf16 v[28:31], a[44:47], a[92:95], v[28:31]\n"
"ds_read_b128 a[8:11], v216 offset:28672\n"
"buffer_load_dwordx4 v229, s[48:51], s29 offen offset:3072 sc1 lds\n"
"v_mfma_f32_16x16x32_bf16 v[32:35], a[44:47], a[160:163], v[32:35]\n"
"ds_read_b64_tr_b16 a[30:31], v224 offset:64\n"
"v_mfma_f32_16x16x32_bf16 v[36:39], a[44:47], a[116:119], v[36:39]\n"
"ds_read_b128 a[12:15], v216 offset:29696\n"
"v_mfma_f32_16x16x32_bf16 v[100:103], a[40:43], a[96:99], v[100:103]\n"
"ds_read_b64_tr_b16 a[36:37], v224 offset:1024\n"
"v_mfma_f32_16x16x32_bf16 v[104:107], a[40:43], a[88:91], v[104:107]\n"
"ds_read_b128 a[24:27], v216 offset:30720\n"
"s_add_u32 m0, s79, 20480\n"
"s_add_u32 s29, s93, 24576\n"
"buffer_load_dwordx4 v217, s[48:51], s29 offen sc1 lds\n"
"v_mfma_f32_16x16x32_bf16 v[108:111], a[40:43], a[92:95], v[108:111]\n"
"ds_read_b64_tr_b16 a[38:39], v224 offset:1088\n"
"v_mfma_f32_16x16x32_bf16 v[112:115], a[40:43], a[160:163], v[112:115]\n"
"ds_read_b64_tr_b16 a[52:53], v224 offset:2048\n"
"v_mfma_f32_16x16x32_bf16 v[116:119], a[40:43], a[116:119], v[116:119]\n"
"ds_read_b128 a[16:19], v216 offset:31744\n"
"buffer_load_dwordx4 v203, s[48:51], s29 offen offset:1024 sc1 lds\n"
"v_mfma_f32_16x16x32_bf16 v[80:83], a[20:23], a[96:99], v[80:83]\n"
"ds_read_b64_tr_b16 a[54:55], v224 offset:2112\n"
"v_mfma_f32_16x16x32_bf16 v[84:87], a[20:23], a[88:91], v[84:87]\n"
"ds_read_b128 a[32:35], v216 offset:32768\n"
"v_mfma_f32_16x16x32_bf16 v[88:91], a[20:23], a[92:95], v[88:91]\n"
"ds_read_b64_tr_b16 a[40:41], v224 offset:3072\n"
"v_mfma_f32_16x16x32_bf16 v[92:95], a[20:23], a[160:163], v[92:95]\n"
"v_mfma_f32_16x16x32_bf16 v[96:99], a[20:23], a[116:119], v[96:99]\n"
"ds_read_b64_tr_b16 a[42:43], v224 offset:3136\n"
"buffer_load_dwordx4 v202, s[48:51], s29 offen offset:2048 sc1 lds\n"
"v_mfma_f32_16x16x32_bf16 v[40:43], a[48:51], a[96:99], v[40:43]\n"
"v_mfma_f32_16x16x32_bf16 v[44:47], a[48:51], a[88:91], v[44:47]\n"
"v_mfma_f32_16x16x32_bf16 v[48:51], a[48:51], a[92:95], v[48:51]\n"
"v_mfma_f32_16x16x32_bf16 v[52:55], a[48:51], a[160:163], v[52:55]\n"
"buffer_load_dwordx4 v229, s[48:51], s29 offen offset:3072 sc1 lds\n"
"v_mfma_f32_16x16x32_bf16 v[56:59], a[48:51], a[116:119], v[56:59]\n"
"s_waitcnt lgkmcnt(0)\n"
"v_mfma_f32_16x16x32_bf16 v[60:63], a[28:31], a[8:11], v[60:63]\n"
"s_waitcnt vmcnt(13)\n"
"v_mfma_f32_16x16x32_bf16 v[64:67], a[28:31], a[12:15], v[64:67]\n"
"ds_read_b64_tr_b16 a[56:57], v224 offset:4096\n"
"v_mfma_f32_16x16x32_bf16 v[68:71], a[28:31], a[24:27], v[68:71]\n"
"ds_read_b128 a[44:47], v216 offset:33792\n"
"buffer_load_dwordx4 a[116:119], v218, s[48:51], s95 offen offset:2048 sc1\n"
"v_mfma_f32_16x16x32_bf16 v[72:75], a[28:31], a[16:19], v[72:75]\n"
"ds_read_b64_tr_b16 a[58:59], v224 offset:4160\n"
"v_mfma_f32_16x16x32_bf16 v[76:79], a[28:31], a[32:35], v[76:79]\n"
"ds_read_b128 a[20:23], v216 offset:34816\n"
"v_mfma_f32_16x16x32_bf16 v[160:163], a[36:39], a[8:11], v[160:163]\n"
"ds_read_b64_tr_b16 a[48:49], v224 offset:5120\n"
"buffer_load_dwordx4 a[160:163], v218, s[48:51], s95 offen offset:3072 sc1\n"
"v_mfma_f32_16x16x32_bf16 v[164:167], a[36:39], a[12:15], v[164:167]\n"
"v_mfma_f32_16x16x32_bf16 v[168:171], a[36:39], a[24:27], v[168:171]\n"
"ds_read_b64_tr_b16 a[50:51], v224 offset:5184\n"
"v_mfma_f32_16x16x32_bf16 v[172:175], a[36:39], a[16:19], v[172:175]\n"
"ds_read_b64_tr_b16 a[28:29], v224 offset:6144\n"
"v_mfma_f32_16x16x32_bf16 v[176:179], a[36:39], a[32:35], v[176:179]\n"
"v_mfma_f32_16x16x32_bf16 v[140:143], a[52:55], a[8:11], v[140:143]\n"
"ds_read_b64_tr_b16 a[30:31], v224 offset:6208\n"
"s_add_u32 s95, s96, 48128\n"
"buffer_load_dwordx4 a[92:95], v218, s[48:51], s95 offen sc1\n"
"v_mfma_f32_16x16x32_bf16 v[144:147], a[52:55], a[12:15], v[144:147]\n"
"v_mfma_f32_16x16x32_bf16 v[148:151], a[52:55], a[24:27], v[148:151]\n"
"ds_read_b64_tr_b16 a[36:37], v224 offset:7168\n"
"v_mfma_f32_16x16x32_bf16 v[152:155], a[52:55], a[16:19], v[152:155]\n"
"v_mfma_f32_16x16x32_bf16 v[156:159], a[52:55], a[32:35], v[156:159]\n"
"ds_read_b64_tr_b16 a[38:39], v224 offset:7232\n"
"buffer_load_dwordx4 a[88:91], v218, s[48:51], s95 offen offset:1024 sc1\n"
"v_mfma_f32_16x16x32_bf16 v[120:123], a[40:43], a[8:11], v[120:123]\n"
"v_mfma_f32_16x16x32_bf16 v[124:127], a[40:43], a[12:15], v[124:127]\n"
"v_mfma_f32_16x16x32_bf16 v[128:131], a[40:43], a[24:27], v[128:131]\n"
"v_mfma_f32_16x16x32_bf16 v[132:135], a[40:43], a[16:19], v[132:135]\n"
"buffer_load_dwordx4 a[96:99], v218, s[48:51], s95 offen offset:2048 sc1\n"
"v_mfma_f32_16x16x32_bf16 v[136:139], a[40:43], a[32:35], v[136:139]\n"
"s_waitcnt lgkmcnt(0)\n"
"v_mfma_f32_16x16x32_bf16 v[60:63], a[56:59], a[44:47], v[60:63]\n"
"s_waitcnt vmcnt(14)\n"
"v_mfma_f32_16x16x32_bf16 v[64:67], a[56:59], a[20:23], v[64:67]\n"
"ds_read_b64_tr_b16 a[12:13], v224 offset:8192\n"
"v_mfma_f32_16x16x32_bf16 v[68:71], a[56:59], a[184:187], v[68:71]\n"
"buffer_load_dwordx4 a[80:83], v218, s[48:51], s95 offen offset:3072 sc1\n"
"v_mfma_f32_16x16x32_bf16 v[72:75], a[56:59], a[212:215], v[72:75]\n"
"ds_read_b64_tr_b16 a[14:15], v224 offset:8256\n"
"v_mfma_f32_16x16x32_bf16 v[76:79], a[56:59], a[108:111], v[76:79]\n"
"v_mfma_f32_16x16x32_bf16 v[160:163], a[48:51], a[44:47], v[160:163]\n"
"ds_read_b64_tr_b16 a[32:33], v224 offset:9216\n"
"v_mfma_f32_16x16x32_bf16 v[164:167], a[48:51], a[20:23], v[164:167]\n"
"s_add_u32 s95, s96, 52224\n"
"buffer_load_dwordx4 a[176:179], v218, s[48:51], s95 offen sc1\n"
"v_mfma_f32_16x16x32_bf16 v[168:171], a[48:51], a[184:187], v[168:171]\n"
"ds_read_b64_tr_b16 a[34:35], v224 offset:9280\n"
"v_mfma_f32_16x16x32_bf16 v[172:175], a[48:51], a[212:215], v[172:175]\n"
"ds_read_b64_tr_b16 a[16:17], v224 offset:10240\n"
"v_mfma_f32_16x16x32_bf16 v[176:179], a[48:51], a[108:111], v[176:179]\n"
"v_mfma_f32_16x16x32_bf16 v[140:143], a[28:31], a[44:47], v[140:143]\n"
"ds_read_b64_tr_b16 a[18:19], v224 offset:10304\n"
"buffer_load_dwordx4 a[208:211], v218, s[48:51], s95 offen offset:1024 sc1\n"
"v_mfma_f32_16x16x32_bf16 v[144:147], a[28:31], a[20:23], v[144:147]\n"
"v_mfma_f32_16x16x32_bf16 v[148:151], a[28:31], a[184:187], v[148:151]\n"
"ds_read_b64_tr_b16 a[24:25], v224 offset:11264\n"
"v_mfma_f32_16x16x32_bf16 v[152:155], a[28:31], a[212:215], v[152:155]\n"
"v_mfma_f32_16x16x32_bf16 v[156:159], a[28:31], a[108:111], v[156:159]\n"
"ds_read_b64_tr_b16 a[26:27], v224 offset:11328\n"
"v_mfma_f32_16x16x32_bf16 v[120:123], a[36:39], a[44:47], v[120:123]\n"
"buffer_load_dwordx4 v[236:239], v218, s[48:51], s95 offen offset:2048 sc1\n"
"v_mfma_f32_16x16x32_bf16 v[124:127], a[36:39], a[20:23], v[124:127]\n"
"v_mfma_f32_16x16x32_bf16 v[128:131], a[36:39], a[184:187], v[128:131]\n"
"v_mfma_f32_16x16x32_bf16 v[132:135], a[36:39], a[212:215], v[132:135]\n"
"v_mfma_f32_16x16x32_bf16 v[136:139], a[36:39], a[108:111], v[136:139]\n"
"buffer_load_dwordx4 a[172:175], v218, s[48:51], s95 offen offset:3072 sc1\n"
"s_waitcnt lgkmcnt(0)\n"
"v_mfma_f32_16x16x32_bf16 v[60:63], a[12:15], a[180:183], v[60:63]\n"
"s_waitcnt vmcnt(14)\n"
"v_mfma_f32_16x16x32_bf16 v[64:67], a[12:15], a[128:131], v[64:67]\n"
"ds_read_b64_tr_b16 a[28:29], v224 offset:12288\n"
"v_mfma_f32_16x16x32_bf16 v[68:71], a[12:15], a[168:171], v[68:71]\n"
"v_mfma_f32_16x16x32_bf16 v[72:75], a[12:15], a[152:155], v[72:75]\n"
"ds_read_b64_tr_b16 a[30:31], v224 offset:12352\n"
"s_add_u32 m0, s79, 0\n"
"s_add_u32 s29, s97, 8192\n"
"buffer_load_dwordx4 v217, s[48:51], s29 offen sc1 lds\n"
"v_mfma_f32_16x16x32_bf16 v[76:79], a[12:15], a[124:127], v[76:79]\n"
"v_mfma_f32_16x16x32_bf16 v[160:163], a[32:35], a[180:183], v[160:163]\n"
"ds_read_b64_tr_b16 a[36:37], v224 offset:13312\n"
"v_mfma_f32_16x16x32_bf16 v[164:167], a[32:35], a[128:131], v[164:167]\n"
"v_mfma_f32_16x16x32_bf16 v[168:171], a[32:35], a[168:171], v[168:171]\n"
"ds_read_b64_tr_b16 a[38:39], v224 offset:13376\n"
"buffer_load_dwordx4 v203, s[48:51], s29 offen offset:1024 sc1 lds\n"
"v_mfma_f32_16x16x32_bf16 v[172:175], a[32:35], a[152:155], v[172:175]\n"
"ds_read_b64_tr_b16 a[20:21], v224 offset:14336\n"
"v_mfma_f32_16x16x32_bf16 v[176:179], a[32:35], a[124:127], v[176:179]\n"
"v_mfma_f32_16x16x32_bf16 v[140:143], a[16:19], a[180:183], v[140:143]\n"
"ds_read_b64_tr_b16 a[22:23], v224 offset:14400\n"
"v_mfma_f32_16x16x32_bf16 v[144:147], a[16:19], a[128:131], v[144:147]\n"
"buffer_load_dwordx4 v202, s[48:51], s29 offen offset:2048 sc1 lds\n"
"v_mfma_f32_16x16x32_bf16 v[148:151], a[16:19], a[168:171], v[148:151]\n"
"ds_read_b64_tr_b16 a[32:33], v224 offset:15360\n"
"v_mfma_f32_16x16x32_bf16 v[152:155], a[16:19], a[152:155], v[152:155]\n"
"v_mfma_f32_16x16x32_bf16 v[156:159], a[16:19], a[124:127], v[156:159]\n"
"ds_read_b64_tr_b16 a[34:35], v224 offset:15424\n"
"v_mfma_f32_16x16x32_bf16 v[120:123], a[24:27], a[180:183], v[120:123]\n"
"v_mfma_f32_16x16x32_bf16 v[124:127], a[24:27], a[128:131], v[124:127]\n"
"buffer_load_dwordx4 v229, s[48:51], s29 offen offset:3072 sc1 lds\n"
"v_mfma_f32_16x16x32_bf16 v[128:131], a[24:27], a[168:171], v[128:131]\n"
"v_mfma_f32_16x16x32_bf16 v[132:135], a[24:27], a[152:155], v[132:135]\n"
"v_mfma_f32_16x16x32_bf16 v[136:139], a[24:27], a[124:127], v[136:139]\n"
"s_waitcnt lgkmcnt(0)\n"
"v_mfma_f32_16x16x32_bf16 v[60:63], a[28:31], a[144:147], v[60:63]\n"
"s_waitcnt vmcnt(14)\n"
"s_add_u32 m0, s79, 4096\n"
"s_add_u32 s29, s97, 12288\n"
"buffer_load_dwordx4 v217, s[48:51], s29 offen sc1 lds\n"
"v_mfma_f32_16x16x32_bf16 v[64:67], a[28:31], v[208:211], v[64:67]\n"
"ds_read_b64_tr_b16 a[44:45], v224 offset:20480\n"
"v_mfma_f32_16x16x32_bf16 v[68:71], a[28:31], v[212:215], v[68:71]\n"
"v_mfma_f32_16x16x32_bf16 v[72:75], a[28:31], a[120:123], v[72:75]\n"
"ds_read_b64_tr_b16 a[46:47], v224 offset:20544\n"
"v_mfma_f32_16x16x32_bf16 v[76:79], a[28:31], a[112:115], v[76:79]\n"
"buffer_load_dwordx2 v[180:181], v189, s[48:51], 0 offen sc1\n"
"v_mfma_f32_16x16x32_bf16 v[160:163], a[36:39], a[144:147], v[160:163]\n"
"ds_read_b64_tr_b16 a[16:17], v224 offset:21504\n"
"v_mfma_f32_16x16x32_bf16 v[164:167], a[36:39], v[208:211], v[164:167]\n"
"v_mfma_f32_16x16x32_bf16 v[168:171], a[36:39], v[212:215], v[168:171]\n"
"ds_read_b64_tr_b16 a[18:19], v224 offset:21568\n"
"v_mfma_f32_16x16x32_bf16 v[172:175], a[36:39], a[120:123], v[172:175]\n"
"ds_read_b64_tr_b16 a[24:25], v224 offset:22528\n"
"buffer_load_dwordx4 v203, s[48:51], s29 offen offset:1024 sc1 lds\n"
"v_mfma_f32_16x16x32_bf16 v[176:179], a[36:39], a[112:115], v[176:179]\n"
"v_mfma_f32_16x16x32_bf16 v[140:143], a[20:23], a[144:147], v[140:143]\n"
"ds_read_b64_tr_b16 a[26:27], v224 offset:22592\n"
"v_mfma_f32_16x16x32_bf16 v[144:147], a[20:23], v[208:211], v[144:147]\n"
"v_mfma_f32_16x16x32_bf16 v[148:151], a[20:23], v[212:215], v[148:151]\n"
"ds_read_b64_tr_b16 a[12:13], v224 offset:23552\n"
"buffer_load_dwordx4 v202, s[48:51], s29 offen offset:2048 sc1 lds\n"
"v_mfma_f32_16x16x32_bf16 v[152:155], a[20:23], a[120:123], v[152:155]\n"
"v_mfma_f32_16x16x32_bf16 v[156:159], a[20:23], a[112:115], v[156:159]\n"
"ds_read_b64_tr_b16 a[14:15], v224 offset:23616\n"
"v_mfma_f32_16x16x32_bf16 v[120:123], a[32:35], a[144:147], v[120:123]\n"
"v_mfma_f32_16x16x32_bf16 v[124:127], a[32:35], v[208:211], v[124:127]\n"
"buffer_load_dwordx4 v229, s[48:51], s29 offen offset:3072 sc1 lds\n"
"v_mfma_f32_16x16x32_bf16 v[128:131], a[32:35], v[212:215], v[128:131]\n"
"v_mfma_f32_16x16x32_bf16 v[132:135], a[32:35], a[120:123], v[132:135]\n"
"v_mfma_f32_16x16x32_bf16 v[136:139], a[32:35], a[112:115], v[136:139]\n"
"s_add_u32 m0, s79, 16384\n"
"s_add_u32 s29, s97, 16384\n"
"buffer_load_dwordx4 v217, s[48:51], s29 offen sc1 lds\n"
"buffer_load_dwordx4 v203, s[48:51], s29 offen offset:1024 sc1 lds\n"
"buffer_load_dwordx4 v202, s[48:51], s29 offen offset:2048 sc1 lds\n"
"buffer_load_dwordx4 v229, s[48:51], s29 offen offset:3072 sc1 lds\n"
"s_add_u32 m0, s79, 24576\n"
"s_add_u32 s29, s97, 20480\n"
"buffer_load_dwordx4 v217, s[48:51], s29 offen sc1 lds\n"
"buffer_load_dwordx4 v203, s[48:51], s29 offen offset:1024 sc1 lds\n"
"buffer_load_dwordx4 v202, s[48:51], s29 offen offset:2048 sc1 lds\n"
"buffer_load_dwordx4 v229, s[48:51], s29 offen offset:3072 sc1 lds\n"
"s_waitcnt lgkmcnt(0)\n"
"v_mfma_f32_16x16x32_bf16 v[60:63], a[44:47], a[132:135], v[60:63]\n"
"s_waitcnt vmcnt(13)\n"
"v_mfma_f32_16x16x32_bf16 v[64:67], a[44:47], v[204:207], v[64:67]\n"
"ds_read_b64_tr_b16 a[32:33], v224\n"
"s_add_u32 m0, s79, 35840\n"
"s_add_u32 s29, s97, 24576\n"
"buffer_load_dwordx4 v217, s[48:51], s29 offen sc1 lds\n"
"v_mfma_f32_16x16x32_bf16 v[68:71], a[44:47], a[140:143], v[68:71]\n"
"ds_read_b128 a[8:11], v216 offset:28672\n"
"v_mfma_f32_16x16x32_bf16 v[72:75], a[44:47], v[232:235], v[72:75]\n"
"ds_read_b64_tr_b16 a[34:35], v224 offset:64\n"
"v_mfma_f32_16x16x32_bf16 v[76:79], a[44:47], a[136:139], v[76:79]\n"
"ds_read_b128 a[28:31], v216 offset:29696\n"
"v_mfma_f32_16x16x32_bf16 v[160:163], a[16:19], a[132:135], v[160:163]\n"
"ds_read_b64_tr_b16 a[36:37], v224 offset:1024\n"
"buffer_load_dwordx4 v203, s[48:51], s29 offen offset:1024 sc1 lds\n"
"v_mfma_f32_16x16x32_bf16 v[164:167], a[16:19], v[204:207], v[164:167]\n"
"ds_read_b128 a[44:47], v216 offset:30720\n"
"v_mfma_f32_16x16x32_bf16 v[168:171], a[16:19], a[140:143], v[168:171]\n"
"ds_read_b64_tr_b16 a[38:39], v224 offset:1088\n"
"v_mfma_f32_16x16x32_bf16 v[172:175], a[16:19], v[232:235], v[172:175]\n"
"ds_read_b64_tr_b16 a[52:53], v224 offset:2048\n"
"v_mfma_f32_16x16x32_bf16 v[176:179], a[16:19], a[136:139], v[176:179]\n"
"ds_read_b128 a[20:23], v216 offset:31744\n"
"buffer_load_dwordx4 v202, s[48:51], s29 offen offset:2048 sc1 lds\n"
"v_mfma_f32_16x16x32_bf16 v[140:143], a[24:27], a[132:135], v[140:143]\n"
"ds_read_b64_tr_b16 a[54:55], v224 offset:2112\n"
"v_mfma_f32_16x16x32_bf16 v[144:147], a[24:27], v[204:207], v[144:147]\n"
"ds_read_b128 a[16:19], v216 offset:32768\n"
"v_mfma_f32_16x16x32_bf16 v[148:151], a[24:27], a[140:143], v[148:151]\n"
"ds_read_b64_tr_b16 a[40:41], v224 offset:3072\n"
"s_waitcnt vmcnt(14)\n"
"v_subrev_u32 v182, s16, v180\n"
"v_xor_b32 v182, 1, v182\n"
"v_bitop3_b32 v182, v181, s81, v182 bitop3:0xbe\n"
"v_cmp_ne_u32 vcc, 0, v182\n"
"s_nop 1\n"
"s_cmp_lg_u64 vcc, 0\n"
"s_cbranch_scc1 SLOWBH1_%=\n"
"BACKBH1_%=:\n"
"s_add_u32 s29, s93, 28672\n"
"buffer_load_dwordx4 v[180:183], v203, s[48:51], s29 offen offset:1024 sc1\n"
"v_mfma_f32_16x16x32_bf16 v[152:155], a[24:27], v[232:235], v[152:155]\n"
"v_mfma_f32_16x16x32_bf16 v[156:159], a[24:27], a[136:139], v[156:159]\n"
"ds_read_b64_tr_b16 a[42:43], v224 offset:3136\n"
"v_mfma_f32_16x16x32_bf16 v[120:123], a[12:15], a[132:135], v[120:123]\n"
"v_mfma_f32_16x16x32_bf16 v[124:127], a[12:15], v[204:207], v[124:127]\n"
"s_add_u32 s29, s93, 32768\n"
"buffer_load_dwordx4 v[184:187], v203, s[48:51], s29 offen offset:1024 sc1\n"
"v_mfma_f32_16x16x32_bf16 v[128:131], a[12:15], a[140:143], v[128:131]\n"
"v_mfma_f32_16x16x32_bf16 v[132:135], a[12:15], v[232:235], v[132:135]\n"
"v_mfma_f32_16x16x32_bf16 v[136:139], a[12:15], a[136:139], v[136:139]\n"
"s_waitcnt lgkmcnt(0)\n"
"v_mfma_f32_16x16x32_bf16 v[20:23], a[32:35], a[8:11], v[20:23]\n"
"s_waitcnt vmcnt(13)\n"
"s_add_u32 s29, s93, 36864\n"
"buffer_load_dwordx4 a[72:75], v203, s[48:51], s29 offen offset:1024 sc1\n"
"v_mfma_f32_16x16x32_bf16 v[24:27], a[32:35], a[28:31], v[24:27]\n"
"ds_read_b64_tr_b16 a[48:49], v224 offset:4096\n"
"v_mfma_f32_16x16x32_bf16 v[28:31], a[32:35], a[44:47], v[28:31]\n"
"ds_read_b128 a[24:27], v216 offset:33792\n"
"v_mfma_f32_16x16x32_bf16 v[32:35], a[32:35], a[20:23], v[32:35]\n"
"ds_read_b64_tr_b16 a[50:51], v224 offset:4160\n"
"v_mfma_f32_16x16x32_bf16 v[36:39], a[32:35], a[16:19], v[36:39]\n"
"ds_read_b128 a[12:15], v216 offset:34816\n"
"s_add_u32 s29, s93, 40960\n"
"buffer_load_dwordx4 a[164:167], v203, s[48:51], s29 offen offset:1024 sc1\n"
"v_mfma_f32_16x16x32_bf16 v[100:103], a[36:39], a[8:11], v[100:103]\n"
"ds_read_b64_tr_b16 a[56:57], v224 offset:5120\n"
"v_mfma_f32_16x16x32_bf16 v[104:107], a[36:39], a[28:31], v[104:107]\n"
"v_mfma_f32_16x16x32_bf16 v[108:111], a[36:39], a[44:47], v[108:111]\n"
"ds_read_b64_tr_b16 a[58:59], v224 offset:5184\n"
"s_add_u32 s29, s93, 28672\n"
"buffer_load_dwordx4 a[64:67], v202, s[48:51], s29 offen offset:2048 sc1\n"
"v_mfma_f32_16x16x32_bf16 v[112:115], a[36:39], a[20:23], v[112:115]\n"
"ds_read_b64_tr_b16 a[32:33], v224 offset:6144\n"
"v_mfma_f32_16x16x32_bf16 v[116:119], a[36:39], a[16:19], v[116:119]\n"
"v_mfma_f32_16x16x32_bf16 v[80:83], a[52:55], a[8:11], v[80:83]\n"
"ds_read_b64_tr_b16 a[34:35], v224 offset:6208\n"
"v_mfma_f32_16x16x32_bf16 v[84:87], a[52:55], a[28:31], v[84:87]\n"
"v_mfma_f32_16x16x32_bf16 v[88:91], a[52:55], a[44:47], v[88:91]\n"
"ds_read_b64_tr_b16 a[36:37], v224 offset:7168\n"
"s_add_u32 s29, s93, 32768\n"
"buffer_load_dwordx4 a[104:107], v202, s[48:51], s29 offen offset:2048 sc1\n"
"v_mfma_f32_16x16x32_bf16 v[92:95], a[52:55], a[20:23], v[92:95]\n"
"v_mfma_f32_16x16x32_bf16 v[96:99], a[52:55], a[16:19], v[96:99]\n"
"ds_read_b64_tr_b16 a[38:39], v224 offset:7232\n"
"v_mfma_f32_16x16x32_bf16 v[40:43], a[40:43], a[8:11], v[40:43]\n"
"v_mfma_f32_16x16x32_bf16 v[44:47], a[40:43], a[28:31], v[44:47]\n"
"s_add_u32 s29, s93, 36864\n"
"buffer_load_dwordx4 a[52:55], v202, s[48:51], s29 offen offset:2048 sc1\n"
"v_mfma_f32_16x16x32_bf16 v[48:51], a[40:43], a[44:47], v[48:51]\n"
"v_mfma_f32_16x16x32_bf16 v[52:55], a[40:43], a[20:23], v[52:55]\n"
"v_mfma_f32_16x16x32_bf16 v[56:59], a[40:43], a[16:19], v[56:59]\n"
"s_waitcnt lgkmcnt(0)\n"
"v_mfma_f32_16x16x32_bf16 v[20:23], a[48:51], a[24:27], v[20:23]\n"
"s_add_u32 s29, s93, 40960\n"
"buffer_load_dwordx4 a[20:23], v202, s[48:51], s29 offen offset:2048 sc1\n"
"v_mfma_f32_16x16x32_bf16 v[24:27], a[48:51], a[12:15], v[24:27]\n"
"v_mfma_f32_16x16x32_bf16 v[28:31], a[48:51], a[184:187], v[28:31]\n"
"v_mfma_f32_16x16x32_bf16 v[32:35], a[48:51], a[212:215], v[32:35]\n"
"v_mfma_f32_16x16x32_bf16 v[36:39], a[48:51], a[108:111], v[36:39]\n"
"s_add_u32 s29, s93, 28672\n"
"buffer_load_dwordx4 a[28:31], v229, s[48:51], s29 offen offset:3072 sc1\n"
"v_mfma_f32_16x16x32_bf16 v[100:103], a[56:59], a[24:27], v[100:103]\n"
"v_mfma_f32_16x16x32_bf16 v[104:107], a[56:59], a[12:15], v[104:107]\n"
"v_mfma_f32_16x16x32_bf16 v[108:111], a[56:59], a[184:187], v[108:111]\n"
"v_mfma_f32_16x16x32_bf16 v[112:115], a[56:59], a[212:215], v[112:115]\n"
"v_mfma_f32_16x16x32_bf16 v[116:119], a[56:59], a[108:111], v[116:119]\n"
"s_add_u32 s29, s93, 32768\n"
"buffer_load_dwordx4 a[8:11], v229, s[48:51], s29 offen offset:3072 sc1\n"
"v_mfma_f32_16x16x32_bf16 v[80:83], a[32:35], a[24:27], v[80:83]\n"
"v_mfma_f32_16x16x32_bf16 v[84:87], a[32:35], a[12:15], v[84:87]\n"
"v_mfma_f32_16x16x32_bf16 v[88:91], a[32:35], a[184:187], v[88:91]\n"
"v_mfma_f32_16x16x32_bf16 v[92:95], a[32:35], a[212:215], v[92:95]\n"
"s_add_u32 s29, s93, 36864\n"
"buffer_load_dwordx4 a[44:47], v229, s[48:51], s29 offen offset:3072 sc1\n"
"v_mfma_f32_16x16x32_bf16 v[96:99], a[32:35], a[108:111], v[96:99]\n"
"v_mfma_f32_16x16x32_bf16 v[40:43], a[36:39], a[24:27], v[40:43]\n"
"v_mfma_f32_16x16x32_bf16 v[44:47], a[36:39], a[12:15], v[44:47]\n"
"v_mfma_f32_16x16x32_bf16 v[48:51], a[36:39], a[184:187], v[48:51]\n"
"s_add_u32 s29, s93, 40960\n"
"buffer_load_dwordx4 a[24:27], v229, s[48:51], s29 offen offset:3072 sc1\n"
"v_mfma_f32_16x16x32_bf16 v[52:55], a[36:39], a[212:215], v[52:55]\n"
"v_mfma_f32_16x16x32_bf16 v[56:59], a[36:39], a[108:111], v[56:59]\n"
"s_add_u32 s29, s93, 28672\n"
"buffer_load_dwordx4 a[12:15], v217, s[48:51], s29 offen sc1\n"
"s_add_u32 s29, s93, 32768\n"
"buffer_load_dwordx4 a[212:215], v217, s[48:51], s29 offen sc1\n"
"s_waitcnt vmcnt(13)\n"
"v_mfma_f32_16x16x32_bf16 v[160:163], v[180:183], a[216:219], v[160:163]\n"
"v_mfma_f32_16x16x32_bf16 v[164:167], v[180:183], a[84:87], v[164:167]\n"
"v_mfma_f32_16x16x32_bf16 v[168:171], v[180:183], a[100:103], v[168:171]\n"
"s_add_u32 s29, s93, 36864\n"
"buffer_load_dwordx4 a[36:39], v217, s[48:51], s29 offen sc1\n"
"v_mfma_f32_16x16x32_bf16 v[172:175], v[180:183], a[76:79], v[172:175]\n"
"v_mfma_f32_16x16x32_bf16 v[176:179], v[180:183], a[68:71], v[176:179]\n"
"s_waitcnt vmcnt(13)\n"
"v_mfma_f32_16x16x32_bf16 v[160:163], v[184:187], a[60:63], v[160:163]\n"
"v_mfma_f32_16x16x32_bf16 v[164:167], v[184:187], a[148:151], v[164:167]\n"
"s_add_u32 s29, s93, 40960\n"
"buffer_load_dwordx4 v[180:183], v217, s[48:51], s29 offen sc1\n"
"v_mfma_f32_16x16x32_bf16 v[168:171], v[184:187], v[196:199], v[168:171]\n"
"v_mfma_f32_16x16x32_bf16 v[172:175], v[184:187], v[192:195], v[172:175]\n"
"v_mfma_f32_16x16x32_bf16 v[176:179], v[184:187], a[156:159], v[176:179]\n"
"s_waitcnt vmcnt(13)\n"
"v_mfma_f32_16x16x32_bf16 v[160:163], a[72:75], a[116:119], v[160:163]\n"
"v_mfma_f32_16x16x32_bf16 v[164:167], a[72:75], a[160:163], v[164:167]\n"
"s_add_u32 s29, s97, 24576\n"
"buffer_load_dwordx4 v229, s[48:51], s29 offen offset:3072 sc1 lds\n"
"v_mfma_f32_16x16x32_bf16 v[168:171], a[72:75], a[92:95], v[168:171]\n"
"v_mfma_f32_16x16x32_bf16 v[172:175], a[72:75], a[88:91], v[172:175]\n"
"v_mfma_f32_16x16x32_bf16 v[176:179], a[72:75], a[96:99], v[176:179]\n"
"s_waitcnt vmcnt(13)\n"
"v_mfma_f32_16x16x32_bf16 v[160:163], a[164:167], a[80:83], v[160:163]\n"
"s_add_u32 s29, s97, 28672\n"
"buffer_load_dwordx4 a[72:75], v203, s[48:51], s29 offen offset:1024 sc1\n"
"v_mfma_f32_16x16x32_bf16 v[164:167], a[164:167], a[176:179], v[164:167]\n"
"v_mfma_f32_16x16x32_bf16 v[168:171], a[164:167], a[208:211], v[168:171]\n"
"v_mfma_f32_16x16x32_bf16 v[172:175], a[164:167], v[236:239], v[172:175]\n"
"v_mfma_f32_16x16x32_bf16 v[176:179], a[164:167], a[172:175], v[176:179]\n"
"s_add_u32 s29, s97, 32768\n"
"buffer_load_dwordx4 a[108:111], v203, s[48:51], s29 offen offset:1024 sc1\n"
"s_waitcnt vmcnt(14)\n"
"v_mfma_f32_16x16x32_bf16 v[140:143], a[64:67], a[216:219], v[140:143]\n"
"v_mfma_f32_16x16x32_bf16 v[144:147], a[64:67], a[84:87], v[144:147]\n"
"v_mfma_f32_16x16x32_bf16 v[148:151], a[64:67], a[100:103], v[148:151]\n"
"ds_write_b128 v216, v[160:163]\n"
"s_add_u32 s29, s97, 36864\n"
"buffer_load_dwordx4 a[164:167], v203, s[48:51], s29 offen offset:1024 sc1\n"
"v_mfma_f32_16x16x32_bf16 v[152:155], a[64:67], a[76:79], v[152:155]\n"
"v_mfma_f32_16x16x32_bf16 v[156:159], a[64:67], a[68:71], v[156:159]\n"
"s_waitcnt vmcnt(14)\n"
"v_mfma_f32_16x16x32_bf16 v[140:143], a[104:107], a[60:63], v[140:143]\n"
"ds_write_b128 v216, v[164:167] offset:1024\n"
"s_add_u32 s29, s97, 40960\n"
"buffer_load_dwordx4 a[64:67], v203, s[48:51], s29 offen offset:1024 sc1\n"
"v_mfma_f32_16x16x32_bf16 v[144:147], a[104:107], a[148:151], v[144:147]\n"
"v_mfma_f32_16x16x32_bf16 v[148:151], a[104:107], v[196:199], v[148:151]\n"
"v_mfma_f32_16x16x32_bf16 v[152:155], a[104:107], v[192:195], v[152:155]\n"
"ds_write_b128 v216, v[168:171] offset:2048\n"
"s_add_u32 s29, s97, 28672\n"
"buffer_load_dwordx4 a[184:187], v202, s[48:51], s29 offen offset:2048 sc1\n"
"v_mfma_f32_16x16x32_bf16 v[156:159], a[104:107], a[156:159], v[156:159]\n"
"s_waitcnt vmcnt(15)\n"
"v_mfma_f32_16x16x32_bf16 v[140:143], a[52:55], a[116:119], v[140:143]\n"
"s_add_u32 s29, s97, 32768\n"
"buffer_load_dwordx4 a[104:107], v202, s[48:51], s29 offen offset:2048 sc1\n"
"v_mfma_f32_16x16x32_bf16 v[144:147], a[52:55], a[160:163], v[144:147]\n"
"ds_write_b128 v216, v[172:175] offset:3072\n"
"v_mfma_f32_16x16x32_bf16 v[148:151], a[52:55], a[92:95], v[148:151]\n"
"v_mfma_f32_16x16x32_bf16 v[152:155], a[52:55], a[88:91], v[152:155]\n"
"s_add_u32 s29, s97, 36864\n"
"buffer_load_dwordx4 a[32:35], v202, s[48:51], s29 offen offset:2048 sc1\n"
"v_mfma_f32_16x16x32_bf16 v[156:159], a[52:55], a[96:99], v[156:159]\n"
"ds_write_b128 v216, v[176:179] offset:4096\n"
"s_waitcnt vmcnt(16)\n"
"v_mfma_f32_16x16x32_bf16 v[140:143], a[20:23], a[80:83], v[140:143]\n"
"s_add_u32 s29, s97, 40960\n"
"buffer_load_dwordx4 a[52:55], v202, s[48:51], s29 offen offset:2048 sc1\n"
"v_mfma_f32_16x16x32_bf16 v[144:147], a[20:23], a[176:179], v[144:147]\n"
"v_mfma_f32_16x16x32_bf16 v[148:151], a[20:23], a[208:211], v[148:151]\n"
"v_mfma_f32_16x16x32_bf16 v[152:155], a[20:23], v[236:239], v[152:155]\n"
"v_mfma_f32_16x16x32_bf16 v[156:159], a[20:23], a[172:175], v[156:159]\n"
"s_add_u32 s29, s97, 28672\n"
"buffer_load_dwordx4 a[56:59], v229, s[48:51], s29 offen offset:3072 sc1\n"
"s_waitcnt vmcnt(17)\n"
"v_mfma_f32_16x16x32_bf16 v[120:123], a[28:31], a[216:219], v[120:123]\n"
"v_mfma_f32_16x16x32_bf16 v[124:127], a[28:31], a[84:87], v[124:127]\n"
"v_mfma_f32_16x16x32_bf16 v[128:131], a[28:31], a[100:103], v[128:131]\n"
"ds_write_b128 v216, v[140:143] offset:5120\n"
"s_add_u32 s29, s97, 32768\n"
"buffer_load_dwordx4 a[48:51], v229, s[48:51], s29 offen offset:3072 sc1\n"
"v_mfma_f32_16x16x32_bf16 v[132:135], a[28:31], a[76:79], v[132:135]\n"
"v_mfma_f32_16x16x32_bf16 v[136:139], a[28:31], a[68:71], v[136:139]\n"
"s_waitcnt vmcnt(17)\n"
"v_mfma_f32_16x16x32_bf16 v[120:123], a[8:11], a[60:63], v[120:123]\n"
"ds_write_b128 v216, v[144:147] offset:6144\n"
"s_add_u32 s29, s97, 36864\n"
"buffer_load_dwordx4 a[16:19], v229, s[48:51], s29 offen offset:3072 sc1\n"
"v_mfma_f32_16x16x32_bf16 v[124:127], a[8:11], a[148:151], v[124:127]\n"
"v_mfma_f32_16x16x32_bf16 v[128:131], a[8:11], v[196:199], v[128:131]\n"
"v_mfma_f32_16x16x32_bf16 v[132:135], a[8:11], v[192:195], v[132:135]\n"
"ds_write_b128 v216, v[148:151] offset:7168\n"
"s_add_u32 s29, s97, 40960\n"
"buffer_load_dwordx4 a[40:43], v229, s[48:51], s29 offen offset:3072 sc1\n"
"v_mfma_f32_16x16x32_bf16 v[136:139], a[8:11], a[156:159], v[136:139]\n"
"s_waitcnt vmcnt(17)\n"
"v_mfma_f32_16x16x32_bf16 v[120:123], a[44:47], a[116:119], v[120:123]\n"
"s_add_u32 s29, s97, 28672\n"
"buffer_load_dwordx4 v[200:203], v217, s[48:51], s29 offen sc1\n"
"v_mfma_f32_16x16x32_bf16 v[124:127], a[44:47], a[160:163], v[124:127]\n"
"ds_write_b128 v216, v[152:155] offset:8192\n"
"v_mfma_f32_16x16x32_bf16 v[128:131], a[44:47], a[92:95], v[128:131]\n"
"v_mfma_f32_16x16x32_bf16 v[132:135], a[44:47], a[88:91], v[132:135]\n"
"s_add_u32 s29, s97, 32768\n"
"buffer_load_dwordx4 v[228:231], v217, s[48:51], s29 offen sc1\n"
"v_mfma_f32_16x16x32_bf16 v[136:139], a[44:47], a[96:99], v[136:139]\n"
"ds_write_b128 v216, v[156:159] offset:9216\n"
"s_waitcnt vmcnt(16)\n"
"v_mfma_f32_16x16x32_bf16 v[120:123], a[24:27], a[80:83], v[120:123]\n"
"s_add_u32 s29, s97, 36864\n"
"buffer_load_dwordx4 v[188:191], v217, s[48:51], s29 offen sc1\n"
"v_mfma_f32_16x16x32_bf16 v[124:127], a[24:27], a[176:179], v[124:127]\n"
"v_mfma_f32_16x16x32_bf16 v[128:131], a[24:27], a[208:211], v[128:131]\n"
"v_mfma_f32_16x16x32_bf16 v[132:135], a[24:27], v[236:239], v[132:135]\n"
"v_mfma_f32_16x16x32_bf16 v[136:139], a[24:27], a[172:175], v[136:139]\n"
"s_add_u32 s29, s97, 40960\n"
"buffer_load_dwordx4 a[0:3], v217, s[48:51], s29 offen sc1\n"
"s_waitcnt vmcnt(17)\n"
"v_mfma_f32_16x16x32_bf16 v[60:63], a[12:15], a[216:219], v[60:63]\n"
"v_mfma_f32_16x16x32_bf16 v[64:67], a[12:15], a[84:87], v[64:67]\n"
"ds_read_b64_tr_b16 a[20:21], v224 offset:16384\n"
"v_mfma_f32_16x16x32_bf16 v[68:71], a[12:15], a[100:103], v[68:71]\n"
"ds_write_b128 v216, v[120:123] offset:10240\n"
"v_mfma_f32_16x16x32_bf16 v[72:75], a[12:15], a[76:79], v[72:75]\n"
"ds_read_b64_tr_b16 a[22:23], v224 offset:16448\n"
"v_mfma_f32_16x16x32_bf16 v[76:79], a[12:15], a[68:71], v[76:79]\n"
"ds_write_b128 v216, v[124:127] offset:11264\n"
"v_mfma_f32_16x16x32_bf16 v[60:63], a[212:215], a[60:63], v[60:63]\n"
"ds_read_b64_tr_b16 a[28:29], v224 offset:17408\n"
"v_mfma_f32_16x16x32_bf16 v[64:67], a[212:215], a[148:151], v[64:67]\n"
"ds_write_b128 v216, v[128:131] offset:12288\n"
"v_mfma_f32_16x16x32_bf16 v[68:71], a[212:215], v[196:199], v[68:71]\n"
"ds_read_b64_tr_b16 a[30:31], v224 offset:17472\n"
"v_mfma_f32_16x16x32_bf16 v[72:75], a[212:215], v[192:195], v[72:75]\n"
"ds_write_b128 v216, v[132:135] offset:13312\n"
"ds_read_b64_tr_b16 a[24:25], v224 offset:18432\n"
"v_mfma_f32_16x16x32_bf16 v[76:79], a[212:215], a[156:159], v[76:79]\n"
"v_mfma_f32_16x16x32_bf16 v[60:63], a[36:39], a[116:119], v[60:63]\n"
"ds_write_b128 v216, v[136:139] offset:14336\n"
"ds_read_b64_tr_b16 a[26:27], v224 offset:18496\n"
"v_mfma_f32_16x16x32_bf16 v[64:67], a[36:39], a[160:163], v[64:67]\n"
"v_mfma_f32_16x16x32_bf16 v[68:71], a[36:39], a[92:95], v[68:71]\n"
"ds_read_b64_tr_b16 a[8:9], v224 offset:19456\n"
"v_mfma_f32_16x16x32_bf16 v[72:75], a[36:39], a[88:91], v[72:75]\n"
"v_mfma_f32_16x16x32_bf16 v[76:79], a[36:39], a[96:99], v[76:79]\n"
"ds_read_b64_tr_b16 a[10:11], v224 offset:19520\n"
"v_mfma_f32_16x16x32_bf16 v[60:63], v[180:183], a[80:83], v[60:63]\n"
"v_mfma_f32_16x16x32_bf16 v[64:67], v[180:183], a[176:179], v[64:67]\n"
"v_mfma_f32_16x16x32_bf16 v[68:71], v[180:183], a[208:211], v[68:71]\n"
"v_mfma_f32_16x16x32_bf16 v[72:75], v[180:183], v[236:239], v[72:75]\n"
"v_mfma_f32_16x16x32_bf16 v[76:79], v[180:183], a[172:175], v[76:79]\n"
"s_waitcnt lgkmcnt(0)\n"
"v_mfma_f32_16x16x32_bf16 v[20:23], a[20:23], a[180:183], v[20:23]\n"
"s_barrier\n"
"ds_read_b128 v[120:123], v221\n"
"ds_read_b128 v[124:127], v221 offset:1024\n"
"ds_read_b128 v[128:131], v221 offset:2048\n"
"v_mfma_f32_16x16x32_bf16 v[24:27], a[20:23], a[128:131], v[24:27]\n"
"ds_read_b64_tr_b16 a[12:13], v224 offset:24576\n"
"ds_read_b128 v[132:135], v221 offset:3072\n"
"ds_read_b128 v[136:139], v221 offset:4096\n"
"ds_read_b128 v[140:143], v220 offset:5120\n"
"ds_read_b128 v[144:147], v220 offset:6144\n"
"ds_read_b128 v[148:151], v220 offset:7168\n"
"ds_read_b128 v[152:155], v220 offset:8192\n"
"ds_read_b128 v[156:159], v220 offset:9216\n"
"ds_read_b128 v[160:163], v219 offset:10240\n"
"v_mfma_f32_16x16x32_bf16 v[28:31], a[20:23], a[168:171], v[28:31]\n"
"ds_read_b128 v[164:167], v219 offset:11264\n"
"ds_read_b128 v[168:171], v219 offset:12288\n"
"ds_read_b128 v[172:175], v219 offset:13312\n"
"ds_read_b128 v[176:179], v219 offset:14336\n"
"v_mfma_f32_16x16x32_bf16 v[32:35], a[20:23], a[152:155], v[32:35]\n"
"ds_read_b64_tr_b16 a[14:15], v224 offset:24640\n"
"v_mfma_f32_16x16x32_bf16 v[36:39], a[20:23], a[124:127], v[36:39]\n"
"v_mfma_f32_16x16x32_bf16 v[100:103], a[28:31], a[180:183], v[100:103]\n"
"ds_read_b64_tr_b16 a[44:45], v224 offset:25600\n"
"v_mfma_f32_16x16x32_bf16 v[104:107], a[28:31], a[128:131], v[104:107]\n"
"v_mfma_f32_16x16x32_bf16 v[108:111], a[28:31], a[168:171], v[108:111]\n"
"ds_read_b64_tr_b16 a[46:47], v224 offset:25664\n"
"v_mov_b32 v181, s80\n"
"v_mov_b32 v183, s81\n"
"s_waitcnt lgkmcnt(12)\n"
"v_add_f32 v120, v120, v140\n"
"v_add_f32 v121, v121, v141\n"
"v_add_f32 v122, v122, v142\n"
"v_add_f32 v123, v123, v143\n"
"s_waitcnt lgkmcnt(7)\n"
"v_add_f32 v60, v60, v160\n"
"v_add_f32 v61, v61, v161\n"
"v_add_f32 v62, v62, v162\n"
"v_add_f32 v63, v63, v163\n"
"v_mfma_f32_16x16x32_bf16 v[112:115], a[28:31], a[152:155], v[112:115]\n"
"ds_read_b64_tr_b16 a[20:21], v224 offset:26624\n"
"v_add_f32 v60, v60, v120\n"
"v_add_f32 v61, v61, v121\n"
"v_add_f32 v62, v62, v122\n"
"v_add_f32 v63, v63, v123\n"
"v_cvt_pk_bf16_f32 v180, v60, v61\n"
"v_cvt_pk_bf16_f32 v182, v62, v63\n"
"buffer_store_dwordx4 v[180:183], v218, s[48:51], s61 offen sc1\n"
"v_mfma_f32_16x16x32_bf16 v[116:119], a[28:31], a[124:127], v[116:119]\n"
"v_mov_b32 v185, s80\n"
"v_mov_b32 v187, s81\n"
"v_add_f32 v124, v124, v144\n"
"v_add_f32 v125, v125, v145\n"
"v_add_f32 v126, v126, v146\n"
"v_add_f32 v127, v127, v147\n"
"s_waitcnt lgkmcnt(7)\n"
"v_add_f32 v64, v64, v164\n"
"v_add_f32 v65, v65, v165\n"
"v_add_f32 v66, v66, v166\n"
"v_add_f32 v67, v67, v167\n"
"v_add_f32 v64, v64, v124\n"
"v_mfma_f32_16x16x32_bf16 v[80:83], a[24:27], a[180:183], v[80:83]\n"
"ds_read_b64_tr_b16 a[22:23], v224 offset:26688\n"
"v_add_f32 v65, v65, v125\n"
"v_add_f32 v66, v66, v126\n"
"v_add_f32 v67, v67, v127\n"
"v_cvt_pk_bf16_f32 v184, v64, v65\n"
"v_cvt_pk_bf16_f32 v186, v66, v67\n"
"buffer_store_dwordx4 v[184:187], v218, s[48:51], s61 offen offset:1024 sc1\n"
"v_mfma_f32_16x16x32_bf16 v[84:87], a[24:27], a[128:131], v[84:87]\n"
"v_mov_b32 v181, s80\n"
"v_mov_b32 v183, s81\n"
"v_add_f32 v128, v128, v148\n"
"v_add_f32 v129, v129, v149\n"
"v_add_f32 v130, v130, v150\n"
"v_add_f32 v131, v131, v151\n"
"s_waitcnt lgkmcnt(7)\n"
"v_add_f32 v68, v68, v168\n"
"v_add_f32 v69, v69, v169\n"
"v_add_f32 v70, v70, v170\n"
"v_add_f32 v71, v71, v171\n"
"v_add_f32 v68, v68, v128\n"
"v_mfma_f32_16x16x32_bf16 v[88:91], a[24:27], a[168:171], v[88:91]\n"
"ds_read_b64_tr_b16 a[28:29], v224 offset:27648\n"
"v_add_f32 v69, v69, v129\n"
"v_add_f32 v70, v70, v130\n"
"v_add_f32 v71, v71, v131\n"
"v_cvt_pk_bf16_f32 v180, v68, v69\n"
"v_cvt_pk_bf16_f32 v182, v70, v71\n"
"buffer_store_dwordx4 v[180:183], v218, s[48:51], s61 offen offset:2048 sc1\n"
"v_mfma_f32_16x16x32_bf16 v[92:95], a[24:27], a[152:155], v[92:95]\n"
"v_mov_b32 v185, s80\n"
"v_mov_b32 v187, s81\n"
"v_add_f32 v132, v132, v152\n"
"v_add_f32 v133, v133, v153\n"
"v_add_f32 v134, v134, v154\n"
"v_add_f32 v135, v135, v155\n"
"s_waitcnt lgkmcnt(7)\n"
"v_add_f32 v72, v72, v172\n"
"v_add_f32 v73, v73, v173\n"
"v_add_f32 v74, v74, v174\n"
"v_add_f32 v75, v75, v175\n"
"v_add_f32 v72, v72, v132\n"
"v_mfma_f32_16x16x32_bf16 v[96:99], a[24:27], a[124:127], v[96:99]\n"
"ds_read_b64_tr_b16 a[30:31], v224 offset:27712\n"
"v_add_f32 v73, v73, v133\n"
"v_add_f32 v74, v74, v134\n"
"v_add_f32 v75, v75, v135\n"
"v_cvt_pk_bf16_f32 v184, v72, v73\n"
"v_cvt_pk_bf16_f32 v186, v74, v75\n"
"buffer_store_dwordx4 v[184:187], v218, s[48:51], s61 offen offset:3072 sc1\n"
"v_mfma_f32_16x16x32_bf16 v[40:43], a[8:11], a[180:183], v[40:43]\n"
"v_mov_b32 v181, s80\n"
"v_mov_b32 v183, s81\n"
"v_add_f32 v136, v136, v156\n"
"v_add_f32 v137, v137, v157\n"
"v_add_f32 v138, v138, v158\n"
"v_add_f32 v139, v139, v159\n"
"s_waitcnt lgkmcnt(7)\n"
"v_add_f32 v76, v76, v176\n"
"v_add_f32 v77, v77, v177\n"
"v_add_f32 v78, v78, v178\n"
"v_add_f32 v79, v79, v179\n"
"v_add_f32 v76, v76, v136\n"
"v_mfma_f32_16x16x32_bf16 v[44:47], a[8:11], a[128:131], v[44:47]\n"
"v_add_f32 v77, v77, v137\n"
"v_add_f32 v78, v78, v138\n"
"v_add_f32 v79, v79, v139\n"
"v_cvt_pk_bf16_f32 v180, v76, v77\n"
"v_cvt_pk_bf16_f32 v182, v78, v79\n"
"buffer_store_dwordx4 v[180:183], v218, s[48:51], s63 offen sc1\n"
"v_mfma_f32_16x16x32_bf16 v[48:51], a[8:11], a[168:171], v[48:51]\n"
"v_mfma_f32_16x16x32_bf16 v[52:55], a[8:11], a[152:155], v[52:55]\n"
"v_mfma_f32_16x16x32_bf16 v[56:59], a[8:11], a[124:127], v[56:59]\n"
"s_waitcnt lgkmcnt(0)\n"
"v_mfma_f32_16x16x32_bf16 v[20:23], a[12:15], a[144:147], v[20:23]\n"
"s_waitcnt vmcnt(21)\n"
"v_mfma_f32_16x16x32_bf16 v[24:27], a[12:15], v[208:211], v[24:27]\n"
"ds_read_b64_tr_b16 a[8:9], v224 offset:35840\n"
"v_mfma_f32_16x16x32_bf16 v[28:31], a[12:15], v[212:215], v[28:31]\n"
"v_mfma_f32_16x16x32_bf16 v[32:35], a[12:15], a[120:123], v[32:35]\n"
"ds_read_b64_tr_b16 a[10:11], v224 offset:35904\n"
"v_mfma_f32_16x16x32_bf16 v[36:39], a[12:15], a[112:115], v[36:39]\n"
"v_mfma_f32_16x16x32_bf16 v[100:103], a[44:47], a[144:147], v[100:103]\n"
"ds_read_b64_tr_b16 a[24:25], v224 offset:36864\n"
"v_mfma_f32_16x16x32_bf16 v[104:107], a[44:47], v[208:211], v[104:107]\n"
"v_mfma_f32_16x16x32_bf16 v[108:111], a[44:47], v[212:215], v[108:111]\n"
"ds_read_b64_tr_b16 a[26:27], v224 offset:36928\n"
"v_mfma_f32_16x16x32_bf16 v[112:115], a[44:47], a[120:123], v[112:115]\n"
"ds_read_b64_tr_b16 a[12:13], v224 offset:37888\n"
"v_mfma_f32_16x16x32_bf16 v[116:119], a[44:47], a[112:115], v[116:119]\n"
"v_mfma_f32_16x16x32_bf16 v[80:83], a[20:23], a[144:147], v[80:83]\n"
"ds_read_b64_tr_b16 a[14:15], v224 offset:37952\n"
"v_mfma_f32_16x16x32_bf16 v[84:87], a[20:23], v[208:211], v[84:87]\n"
"v_mfma_f32_16x16x32_bf16 v[88:91], a[20:23], v[212:215], v[88:91]\n"
"ds_read_b64_tr_b16 a[44:45], v224 offset:38912\n"
"v_mfma_f32_16x16x32_bf16 v[92:95], a[20:23], a[120:123], v[92:95]\n"
"v_mfma_f32_16x16x32_bf16 v[96:99], a[20:23], a[112:115], v[96:99]\n"
"ds_read_b64_tr_b16 a[46:47], v224 offset:38976\n"
"v_mfma_f32_16x16x32_bf16 v[40:43], a[28:31], a[144:147], v[40:43]\n"
"v_mfma_f32_16x16x32_bf16 v[44:47], a[28:31], v[208:211], v[44:47]\n"
"v_mfma_f32_16x16x32_bf16 v[48:51], a[28:31], v[212:215], v[48:51]\n"
"v_mfma_f32_16x16x32_bf16 v[52:55], a[28:31], a[120:123], v[52:55]\n"
"v_mfma_f32_16x16x32_bf16 v[56:59], a[28:31], a[112:115], v[56:59]\n"
"s_waitcnt lgkmcnt(0)\n"
"v_mfma_f32_16x16x32_bf16 v[20:23], a[8:11], a[132:135], v[20:23]\n"
"v_mfma_f32_16x16x32_bf16 v[24:27], a[8:11], v[204:207], v[24:27]\n"
"v_mfma_f32_16x16x32_bf16 v[28:31], a[8:11], a[140:143], v[28:31]\n"
"v_mfma_f32_16x16x32_bf16 v[32:35], a[8:11], v[232:235], v[32:35]\n"
"v_mfma_f32_16x16x32_bf16 v[36:39], a[8:11], a[136:139], v[36:39]\n"
"v_mfma_f32_16x16x32_bf16 v[100:103], a[24:27], a[132:135], v[100:103]\n"
"v_mfma_f32_16x16x32_bf16 v[104:107], a[24:27], v[204:207], v[104:107]\n"
"v_mfma_f32_16x16x32_bf16 v[108:111], a[24:27], a[140:143], v[108:111]\n"
"v_mfma_f32_16x16x32_bf16 v[112:115], a[24:27], v[232:235], v[112:115]\n"
"v_mfma_f32_16x16x32_bf16 v[116:119], a[24:27], a[136:139], v[116:119]\n"
"v_mfma_f32_16x16x32_bf16 v[80:83], a[12:15], a[132:135], v[80:83]\n"
"v_mfma_f32_16x16x32_bf16 v[84:87], a[12:15], v[204:207], v[84:87]\n"
"v_mfma_f32_16x16x32_bf16 v[88:91], a[12:15], a[140:143], v[88:91]\n"
"v_mfma_f32_16x16x32_bf16 v[92:95], a[12:15], v[232:235], v[92:95]\n"
"v_mfma_f32_16x16x32_bf16 v[96:99], a[12:15], a[136:139], v[96:99]\n"
"v_mfma_f32_16x16x32_bf16 v[40:43], a[44:47], a[132:135], v[40:43]\n"
"v_mfma_f32_16x16x32_bf16 v[44:47], a[44:47], v[204:207], v[44:47]\n"
"v_mfma_f32_16x16x32_bf16 v[48:51], a[44:47], a[140:143], v[48:51]\n"
"v_mfma_f32_16x16x32_bf16 v[52:55], a[44:47], v[232:235], v[52:55]\n"
"v_mfma_f32_16x16x32_bf16 v[56:59], a[44:47], a[136:139], v[56:59]\n"
"s_waitcnt vmcnt(17)\n"
"v_mfma_f32_16x16x32_bf16 v[100:103], a[72:75], a[216:219], v[100:103]\n"
"v_mfma_f32_16x16x32_bf16 v[104:107], a[72:75], a[84:87], v[104:107]\n"
"v_mfma_f32_16x16x32_bf16 v[108:111], a[72:75], a[100:103], v[108:111]\n"
"v_mfma_f32_16x16x32_bf16 v[112:115], a[72:75], a[76:79], v[112:115]\n"
"v_mfma_f32_16x16x32_bf16 v[116:119], a[72:75], a[68:71], v[116:119]\n"
"s_waitcnt vmcnt(16)\n"
"v_mfma_f32_16x16x32_bf16 v[100:103], a[108:111], a[60:63], v[100:103]\n"
"v_mfma_f32_16x16x32_bf16 v[104:107], a[108:111], a[148:151], v[104:107]\n"
"v_mfma_f32_16x16x32_bf16 v[108:111], a[108:111], v[196:199], v[108:111]\n"
"v_mfma_f32_16x16x32_bf16 v[112:115], a[108:111], v[192:195], v[112:115]\n"
"v_mfma_f32_16x16x32_bf16 v[116:119], a[108:111], a[156:159], v[116:119]\n"
"s_waitcnt vmcnt(15)\n"
"v_mfma_f32_16x16x32_bf16 v[100:103], a[164:167], a[116:119], v[100:103]\n"
"v_mfma_f32_16x16x32_bf16 v[104:107], a[164:167], a[160:163], v[104:107]\n"
"v_mfma_f32_16x16x32_bf16 v[108:111], a[164:167], a[92:95], v[108:111]\n"
"v_mfma_f32_16x16x32_bf16 v[112:115], a[164:167], a[88:91], v[112:115]\n"
"v_mfma_f32_16x16x32_bf16 v[116:119], a[164:167], a[96:99], v[116:119]\n"
"s_waitcnt vmcnt(14)\n"
"v_mfma_f32_16x16x32_bf16 v[100:103], a[64:67], a[80:83], v[100:103]\n"
"v_mfma_f32_16x16x32_bf16 v[104:107], a[64:67], a[176:179], v[104:107]\n"
"v_mfma_f32_16x16x32_bf16 v[108:111], a[64:67], a[208:211], v[108:111]\n"
"v_mfma_f32_16x16x32_bf16 v[112:115], a[64:67], v[236:239], v[112:115]\n"
"v_mfma_f32_16x16x32_bf16 v[116:119], a[64:67], a[172:175], v[116:119]\n"
"s_waitcnt vmcnt(13)\n"
"v_mfma_f32_16x16x32_bf16 v[80:83], a[184:187], a[216:219], v[80:83]\n"
"v_mfma_f32_16x16x32_bf16 v[84:87], a[184:187], a[84:87], v[84:87]\n"
"v_mfma_f32_16x16x32_bf16 v[88:91], a[184:187], a[100:103], v[88:91]\n"
"ds_write_b128 v216, v[100:103] offset:16384\n"
"v_mfma_f32_16x16x32_bf16 v[92:95], a[184:187], a[76:79], v[92:95]\n"
"v_mfma_f32_16x16x32_bf16 v[96:99], a[184:187], a[68:71], v[96:99]\n"
"s_waitcnt vmcnt(12)\n"
"v_mfma_f32_16x16x32_bf16 v[80:83], a[104:107], a[60:63], v[80:83]\n"
"ds_write_b128 v216, v[104:107] offset:17408\n"
"v_mfma_f32_16x16x32_bf16 v[84:87], a[104:107], a[148:151], v[84:87]\n"
"v_mfma_f32_16x16x32_bf16 v[88:91], a[104:107], v[196:199], v[88:91]\n"
"v_mfma_f32_16x16x32_bf16 v[92:95], a[104:107], v[192:195], v[92:95]\n"
"ds_write_b128 v216, v[108:111] offset:18432\n"
"v_mfma_f32_16x16x32_bf16 v[96:99], a[104:107], a[156:159], v[96:99]\n"
"s_waitcnt vmcnt(11)\n"
"v_mfma_f32_16x16x32_bf16 v[80:83], a[32:35], a[116:119], v[80:83]\n"
"v_mfma_f32_16x16x32_bf16 v[84:87], a[32:35], a[160:163], v[84:87]\n"
"ds_write_b128 v216, v[112:115] offset:19456\n"
"v_mfma_f32_16x16x32_bf16 v[88:91], a[32:35], a[92:95], v[88:91]\n"
"v_mfma_f32_16x16x32_bf16 v[92:95], a[32:35], a[88:91], v[92:95]\n"
"v_mfma_f32_16x16x32_bf16 v[96:99], a[32:35], a[96:99], v[96:99]\n"
"ds_write_b128 v216, v[116:119] offset:20480\n"
"s_waitcnt vmcnt(10)\n"
"v_mfma_f32_16x16x32_bf16 v[80:83], a[52:55], a[80:83], v[80:83]\n"
"v_mfma_f32_16x16x32_bf16 v[84:87], a[52:55], a[176:179], v[84:87]\n"
"v_mfma_f32_16x16x32_bf16 v[88:91], a[52:55], a[208:211], v[88:91]\n"
"v_mfma_f32_16x16x32_bf16 v[92:95], a[52:55], v[236:239], v[92:95]\n"
"v_mfma_f32_16x16x32_bf16 v[96:99], a[52:55], a[172:175], v[96:99]\n"
"s_waitcnt vmcnt(9)\n"
"v_mfma_f32_16x16x32_bf16 v[40:43], a[56:59], a[216:219], v[40:43]\n"
"v_mfma_f32_16x16x32_bf16 v[44:47], a[56:59], a[84:87], v[44:47]\n"
"v_mfma_f32_16x16x32_bf16 v[48:51], a[56:59], a[100:103], v[48:51]\n"
"ds_write_b128 v216, v[80:83] offset:21504\n"
"v_mfma_f32_16x16x32_bf16 v[52:55], a[56:59], a[76:79], v[52:55]\n"
"v_mfma_f32_16x16x32_bf16 v[56:59], a[56:59], a[68:71], v[56:59]\n"
"s_waitcnt vmcnt(8)\n"
"v_mfma_f32_16x16x32_bf16 v[40:43], a[48:51], a[60:63], v[40:43]\n"
"ds_write_b128 v216, v[84:87] offset:22528\n"
"v_mfma_f32_16x16x32_bf16 v[44:47], a[48:51], a[148:151], v[44:47]\n"
"v_mfma_f32_16x16x32_bf16 v[48:51], a[48:51], v[196:199], v[48:51]\n"
"v_mfma_f32_16x16x32_bf16 v[52:55], a[48:51], v[192:195], v[52:55]\n"
"ds_write_b128 v216, v[88:91] offset:23552\n"
"v_mfma_f32_16x16x32_bf16 v[56:59], a[48:51], a[156:159], v[56:59]\n"
"s_waitcnt vmcnt(7)\n"
"v_mfma_f32_16x16x32_bf16 v[40:43], a[16:19], a[116:119], v[40:43]\n"
"v_mfma_f32_16x16x32_bf16 v[44:47], a[16:19], a[160:163], v[44:47]\n"
"ds_write_b128 v216, v[92:95] offset:24576\n"
"v_mfma_f32_16x16x32_bf16 v[48:51], a[16:19], a[92:95], v[48:51]\n"
"v_mfma_f32_16x16x32_bf16 v[52:55], a[16:19], a[88:91], v[52:55]\n"
"v_mfma_f32_16x16x32_bf16 v[56:59], a[16:19], a[96:99], v[56:59]\n"
"ds_write_b128 v216, v[96:99] offset:25600\n"
"s_waitcnt vmcnt(6)\n"
"v_mfma_f32_16x16x32_bf16 v[40:43], a[40:43], a[80:83], v[40:43]\n"
"v_mfma_f32_16x16x32_bf16 v[44:47], a[40:43], a[176:179], v[44:47]\n"
"v_mfma_f32_16x16x32_bf16 v[48:51], a[40:43], a[208:211], v[48:51]\n"
"v_mfma_f32_16x16x32_bf16 v[52:55], a[40:43], v[236:239], v[52:55]\n"
"v_mfma_f32_16x16x32_bf16 v[56:59], a[40:43], a[172:175], v[56:59]\n"
"s_waitcnt vmcnt(5)\n"
"v_mfma_f32_16x16x32_bf16 v[20:23], v[200:203], a[216:219], v[20:23]\n"
"v_mfma_f32_16x16x32_bf16 v[24:27], v[200:203], a[84:87], v[24:27]\n"
"v_mfma_f32_16x16x32_bf16 v[28:31], v[200:203], a[100:103], v[28:31]\n"
"ds_write_b128 v216, v[40:43] offset:26624\n"
"v_mfma_f32_16x16x32_bf16 v[32:35], v[200:203], a[76:79], v[32:35]\n"
"v_mfma_f32_16x16x32_bf16 v[36:39], v[200:203], a[68:71], v[36:39]\n"
"ds_write_b128 v216, v[44:47] offset:27648\n"
"v_mfma_f32_16x16x32_bf16 v[20:23], v[228:231], a[60:63], v[20:23]\n"
"v_mfma_f32_16x16x32_bf16 v[24:27], v[228:231], a[148:151], v[24:27]\n"
"ds_write_b128 v216, v[48:51] offset:28672\n"
"v_mfma_f32_16x16x32_bf16 v[28:31], v[228:231], v[196:199], v[28:31]\n"
"v_mfma_f32_16x16x32_bf16 v[32:35], v[228:231], v[192:195], v[32:35]\n"
"ds_write_b128 v216, v[52:55] offset:29696\n"
"buffer_load_dwordx4 v[76:79], v218, s[48:51], s60 offen sc1\n"
"v_mfma_f32_16x16x32_bf16 v[36:39], v[228:231], a[156:159], v[36:39]\n"
"v_mfma_f32_16x16x32_bf16 v[20:23], v[188:191], a[116:119], v[20:23]\n"
"ds_write_b128 v216, v[56:59] offset:30720\n"
"buffer_load_dwordx4 v[72:75], v218, s[48:51], s60 offen offset:1024 sc1\n"
"v_mfma_f32_16x16x32_bf16 v[24:27], v[188:191], a[160:163], v[24:27]\n"
"v_mfma_f32_16x16x32_bf16 v[28:31], v[188:191], a[92:95], v[28:31]\n"
"v_mfma_f32_16x16x32_bf16 v[32:35], v[188:191], a[88:91], v[32:35]\n"
"buffer_load_dwordx4 v[64:67], v218, s[48:51], s60 offen offset:2048 sc1\n"
"v_mfma_f32_16x16x32_bf16 v[36:39], v[188:191], a[96:99], v[36:39]\n"
"s_waitcnt lgkmcnt(0)\n"
"s_barrier\n"
"v_mfma_f32_16x16x32_bf16 v[20:23], a[0:3], a[80:83], v[20:23]\n"
"ds_read_b128 v[40:43], v221 offset:16384\n"
"ds_read_b128 v[44:47], v221 offset:17408\n"
"ds_read_b128 v[48:51], v221 offset:18432\n"
"buffer_load_dwordx4 v[60:63], v218, s[48:51], s60 offen offset:3072 sc1\n"
"v_mfma_f32_16x16x32_bf16 v[24:27], a[0:3], a[176:179], v[24:27]\n"
"ds_read_b128 v[52:55], v221 offset:19456\n"
"ds_read_b128 v[56:59], v221 offset:20480\n"
"ds_read_b128 v[100:103], v220 offset:21504\n"
"v_mfma_f32_16x16x32_bf16 v[28:31], a[0:3], a[208:211], v[28:31]\n"
"ds_read_b128 v[104:107], v220 offset:22528\n"
"ds_read_b128 v[108:111], v220 offset:23552\n"
"ds_read_b128 v[112:115], v220 offset:24576\n"
"v_mfma_f32_16x16x32_bf16 v[32:35], a[0:3], v[236:239], v[32:35]\n"
"ds_read_b128 v[116:119], v220 offset:25600\n"
"ds_read_b128 v[80:83], v219 offset:26624\n"
"ds_read_b128 v[84:87], v219 offset:27648\n"
"buffer_load_dwordx4 v[68:71], v218, s[48:51], s62 offen sc1\n"
"v_mfma_f32_16x16x32_bf16 v[36:39], a[0:3], a[172:175], v[36:39]\n"
"ds_read_b128 v[88:91], v219 offset:28672\n"
"ds_read_b128 v[92:95], v219 offset:29696\n"
"ds_read_b128 v[96:99], v219 offset:30720\n"
"s_waitcnt lgkmcnt(9)\n"
"v_pk_add_f32 v[40:41], v[40:41], v[100:101]\n"
"v_pk_add_f32 v[42:43], v[42:43], v[102:103]\n"
"s_waitcnt lgkmcnt(8)\n"
"v_pk_add_f32 v[44:45], v[44:45], v[104:105]\n"
"v_pk_add_f32 v[46:47], v[46:47], v[106:107]\n"
"s_waitcnt lgkmcnt(7)\n"
"v_pk_add_f32 v[48:49], v[48:49], v[108:109]\n"
"v_pk_add_f32 v[50:51], v[50:51], v[110:111]\n"
"s_waitcnt lgkmcnt(6)\n"
"v_pk_add_f32 v[52:53], v[52:53], v[112:113]\n"
"v_pk_add_f32 v[54:55], v[54:55], v[114:115]\n"
"s_waitcnt lgkmcnt(5)\n"
"v_pk_add_f32 v[56:57], v[56:57], v[116:117]\n"
"v_pk_add_f32 v[58:59], v[58:59], v[118:119]\n"
"s_waitcnt lgkmcnt(4)\n"
"v_pk_add_f32 v[0:1], v[0:1], v[80:81]\n"
"v_pk_add_f32 v[2:3], v[2:3], v[82:83]\n"
"s_waitcnt lgkmcnt(3)\n"
"v_pk_add_f32 v[4:5], v[4:5], v[84:85]\n"
"v_pk_add_f32 v[6:7], v[6:7], v[86:87]\n"
"s_waitcnt lgkmcnt(2)\n"
"v_pk_add_f32 v[8:9], v[8:9], v[88:89]\n"
"v_pk_add_f32 v[10:11], v[10:11], v[90:91]\n"
"s_waitcnt lgkmcnt(1)\n"
"v_pk_add_f32 v[12:13], v[12:13], v[92:93]\n"
"v_pk_add_f32 v[14:15], v[14:15], v[94:95]\n"
"s_waitcnt lgkmcnt(0)\n"
"v_pk_add_f32 v[16:17], v[16:17], v[96:97]\n"
"v_pk_add_f32 v[18:19], v[18:19], v[98:99]\n"
"v_pk_add_f32 v[20:21], v[20:21], v[40:41]\n"
"v_pk_add_f32 v[22:23], v[22:23], v[42:43]\n"
"v_pk_add_f32 v[24:25], v[24:25], v[44:45]\n"
"v_pk_add_f32 v[26:27], v[26:27], v[46:47]\n"
"v_pk_add_f32 v[28:29], v[28:29], v[48:49]\n"
"v_pk_add_f32 v[30:31], v[30:31], v[50:51]\n"
"v_pk_add_f32 v[32:33], v[32:33], v[52:53]\n"
"v_pk_add_f32 v[34:35], v[34:35], v[54:55]\n"
"v_pk_add_f32 v[36:37], v[36:37], v[56:57]\n"
"v_pk_add_f32 v[38:39], v[38:39], v[58:59]\n"
"v_pk_add_f32 v[20:21], v[20:21], v[0:1]\n"
"v_pk_add_f32 v[22:23], v[22:23], v[2:3]\n"
"v_pk_add_f32 v[24:25], v[24:25], v[4:5]\n"
"v_pk_add_f32 v[26:27], v[26:27], v[6:7]\n"
"v_pk_add_f32 v[28:29], v[28:29], v[8:9]\n"
"v_pk_add_f32 v[30:31], v[30:31], v[10:11]\n"
"v_pk_add_f32 v[32:33], v[32:33], v[12:13]\n"
"v_pk_add_f32 v[34:35], v[34:35], v[14:15]\n"
"v_pk_add_f32 v[36:37], v[36:37], v[16:17]\n"
"v_pk_add_f32 v[38:39], v[38:39], v[18:19]\n"
"s_waitcnt vmcnt(4)\n"
"v_xor_b32 v0, s80, v77\n"
"v_bitop3_b32 v0, v79, s81, v0 bitop3:0xbe\n"
"s_waitcnt vmcnt(3)\n"
"v_bitop3_b32 v0, v73, s80, v0 bitop3:0xbe\n"
"v_bitop3_b32 v0, v75, s81, v0 bitop3:0xbe\n"
"s_waitcnt vmcnt(2)\n"
"v_bitop3_b32 v0, v65, s80, v0 bitop3:0xbe\n"
"v_bitop3_b32 v0, v67, s81, v0 bitop3:0xbe\n"
"s_waitcnt vmcnt(1)\n"
"v_bitop3_b32 v0, v61, s80, v0 bitop3:0xbe\n"
"v_bitop3_b32 v0, v63, s81, v0 bitop3:0xbe\n"
"s_waitcnt vmcnt(0)\n"
"v_bitop3_b32 v0, v69, s80, v0 bitop3:0xbe\n"
"v_bitop3_b32 v0, v71, s81, v0 bitop3:0xbe\n"
"v_cmp_ne_u32 vcc, 0, v0\n"
"s_nop 1\n"
"s_cmp_lg_u64 vcc, 0\n"
"s_cbranch_scc1 SLOWBY_%=\n"
"BACKBY_%=:\n"
"v_and_b32 v77, s82, v76\n"
"v_lshlrev_b32 v76, 16, v76\n"
"v_pk_add_f32 v[20:21], v[20:21], v[76:77]\n"
"v_and_b32 v79, s82, v78\n"
"v_lshlrev_b32 v78, 16, v78\n"
"v_pk_add_f32 v[22:23], v[22:23], v[78:79]\n"
"buffer_store_dword v20, v223, s[44:47], 0 offen sc1\n"
"buffer_store_dword v21, v223, s[44:47], 0 offen offset:2560 sc1\n"
"v_and_b32 v73, s82, v72\n"
"v_lshlrev_b32 v72, 16, v72\n"
"v_pk_add_f32 v[24:25], v[24:25], v[72:73]\n"
"buffer_store_dword v22, v223, s[44:47], s88 offen sc1\n"
"buffer_store_dword v23, v223, s[44:47], s88 offen offset:2560 sc1\n"
"v_and_b32 v75, s82, v74\n"
"v_lshlrev_b32 v74, 16, v74\n"
"v_pk_add_f32 v[26:27], v[26:27], v[74:75]\n"
"buffer_store_dword v24, v223, s[44:47], 0 offen offset:64 sc1\n"
"buffer_store_dword v25, v223, s[44:47], 0 offen offset:2624 sc1\n"
"v_and_b32 v65, s82, v64\n"
"v_lshlrev_b32 v64, 16, v64\n"
"v_pk_add_f32 v[28:29], v[28:29], v[64:65]\n"
"buffer_store_dword v26, v223, s[44:47], s88 offen offset:64 sc1\n"
"buffer_store_dword v27, v223, s[44:47], s88 offen offset:2624 sc1\n"
"v_and_b32 v67, s82, v66\n"
"v_lshlrev_b32 v66, 16, v66\n"
"v_pk_add_f32 v[30:31], v[30:31], v[66:67]\n"
"buffer_store_dword v28, v223, s[44:47], 0 offen offset:128 sc1\n"
"buffer_store_dword v29, v223, s[44:47], 0 offen offset:2688 sc1\n"
"v_and_b32 v61, s82, v60\n"
"v_lshlrev_b32 v60, 16, v60\n"
"v_pk_add_f32 v[32:33], v[32:33], v[60:61]\n"
"buffer_store_dword v30, v223, s[44:47], s88 offen offset:128 sc1\n"
"buffer_store_dword v31, v223, s[44:47], s88 offen offset:2688 sc1\n"
"v_and_b32 v63, s82, v62\n"
"v_lshlrev_b32 v62, 16, v62\n"
"v_pk_add_f32 v[34:35], v[34:35], v[62:63]\n"
"buffer_store_dword v32, v223, s[44:47], 0 offen offset:192 sc1\n"
"buffer_store_dword v33, v223, s[44:47], 0 offen offset:2752 sc1\n"
"v_and_b32 v69, s82, v68\n"
"v_lshlrev_b32 v68, 16, v68\n"
"v_pk_add_f32 v[36:37], v[36:37], v[68:69]\n"
"buffer_store_dword v34, v223, s[44:47], s88 offen offset:192 sc1\n"
"buffer_store_dword v35, v223, s[44:47], s88 offen offset:2752 sc1\n"
"v_and_b32 v71, s82, v70\n"
"v_lshlrev_b32 v70, 16, v70\n"
"v_pk_add_f32 v[38:39], v[38:39], v[70:71]\n"
"buffer_store_dword v36, v223, s[44:47], 0 offen offset:256 sc1\n"
"buffer_store_dword v37, v223, s[44:47], 0 offen offset:2816 sc1\n"
"buffer_store_dword v38, v223, s[44:47], s88 offen offset:256 sc1\n"
"buffer_store_dword v39, v223, s[44:47], s88 offen offset:2816 sc1\n"
"s_branch KEND_%=\n"
"SLOWBS_%=:\n"
"s_nop 7\n"
"s_nop 7\n"
"s_add_u32 s17, s17, 1\n"
"s_cmp_gt_u32 s17, 100000\n"
"s_cbranch_scc1 BACKBS_%=\n"
"s_sleep 4\n"
"buffer_load_dwordx4 v[76:79], v158, s[48:51], 0 offen sc1\n"
"s_waitcnt vmcnt(0)\n"
"v_xor_b32 v80, s80, v77\n"
"v_bitop3_b32 v80, v79, s81, v80 bitop3:0xbe\n"
"v_cmp_ne_u32 vcc, 0, v80\n"
"s_nop 1\n"
"s_cmp_lg_u64 vcc, 0\n"
"s_cbranch_scc1 SLOWBS_%=\n"
"s_branch BACKBS_%=\n"
"SLOWBC_%=:\n"
"s_nop 7\n"
"s_nop 7\n"
"s_add_u32 s17, s17, 1\n"
"s_cmp_gt_u32 s17, 100000\n"
"s_cbranch_scc1 BACKBC_%=\n"
"s_sleep 4\n"
"buffer_load_dwordx4 v[36:39], v156, s[48:51], 0 offen sc1\n"
"buffer_load_dwordx4 v[40:43], v156, s[48:51], s90 offen sc1\n"
"buffer_load_dwordx4 v[44:47], v156, s[48:51], 0 offen offset:512 sc1\n"
"buffer_load_dwordx4 v[176:179], v156, s[48:51], s90 offen offset:512 sc1\n"
"buffer_load_dwordx4 v[100:103], v157, s[48:51], 0 offen sc1\n"
"s_waitcnt vmcnt(0)\n"
"v_xor_b32 v250, s8, v37\n"
"v_bitop3_b32 v250, v39, s9, v250 bitop3:0xbe\n"
"v_bitop3_b32 v250, v41, s8, v250 bitop3:0xbe\n"
"v_bitop3_b32 v250, v43, s9, v250 bitop3:0xbe\n"
"v_bitop3_b32 v250, v45, s8, v250 bitop3:0xbe\n"
"v_bitop3_b32 v250, v47, s9, v250 bitop3:0xbe\n"
"v_bitop3_b32 v250, v177, s8, v250 bitop3:0xbe\n"
"v_bitop3_b32 v250, v179, s9, v250 bitop3:0xbe\n"
"v_bitop3_b32 v250, v101, s8, v250 bitop3:0xbe\n"
"v_bitop3_b32 v250, v103, s9, v250 bitop3:0xbe\n"
"v_cmp_ne_u32 vcc, 0, v250\n"
"s_nop 1\n"
"s_cmp_lg_u64 vcc, 0\n"
"s_cbranch_scc1 SLOWBC_%=\n"
"s_branch BACKBC_%=\n"
"SLOWBL1_%=:\n"
"s_nop 7\n"
"s_nop 7\n"
"s_add_u32 s17, s17, 1\n"
"s_cmp_gt_u32 s17, 100000\n"
"s_cbranch_scc1 BACKBL1_%=\n"
"s_sleep 4\n"
"buffer_load_dwordx4 v[196:199], v232, s[52:55], 0 offen sc1\n"
"buffer_load_dwordx4 v[16:19], v233, s[52:55], 0 offen sc1\n"
"buffer_load_dwordx4 v[192:195], v254, s[52:55], 0 offen sc1\n"
"buffer_load_dwordx4 v[188:191], v234, s[52:55], 0 offen sc1\n"
"s_waitcnt vmcnt(0)\n"
"v_xor_b32 v215, s80, v197\n"
"v_bitop3_b32 v215, v199, s81, v215 bitop3:0xbe\n"
"v_bitop3_b32 v214, v215, v235, 0 bitop3:0xba\n"
"v_xor_b32 v215, s80, v17\n"
"v_bitop3_b32 v215, v19, s81, v215 bitop3:0xbe\n"
"v_bitop3_b32 v214, v215, v236, v214 bitop3:0xba\n"
"v_xor_b32 v215, s80, v193\n"
"v_bitop3_b32 v215, v195, s81, v215 bitop3:0xbe\n"
"v_bitop3_b32 v214, v215, v237, v214 bitop3:0xba\n"
"v_xor_b32 v215, s80, v189\n"
"v_bitop3_b32 v215, v191, s81, v215 bitop3:0xbe\n"
"v_bitop3_b32 v214, v215, v238, v214 bitop3:0xba\n"
"v_cmp_ne_u32 vcc, 0, v214\n"
"s_nop 1\n"
"s_cmp_lg_u64 vcc, 0\n"
"s_cbranch_scc1 SLOWBL1_%=\n"
"s_branch BACKBL1_%=\n"
"SLOWBL0_%=:\n"
"s_nop 7\n"
"s_nop 7\n"
"s_add_u32 s17, s17, 1\n"
"s_cmp_gt_u32 s17, 100000\n"
"s_cbranch_scc1 BACKBL0_%=\n"
"s_sleep 4\n"
"buffer_load_dwordx4 v[196:199], v233, s[52:55], 0 offen sc1\n"
"buffer_load_dwordx4 v[16:19], v254, s[52:55], 0 offen sc1\n"
"buffer_load_dwordx4 v[192:195], v234, s[52:55], 0 offen sc1\n"
"buffer_load_dwordx4 v[188:191], v235, s[52:55], 0 offen sc1\n"
"s_waitcnt vmcnt(0)\n"
"v_xor_b32 v93, s80, v197\n"
"v_bitop3_b32 v93, v199, s81, v93 bitop3:0xbe\n"
"v_bitop3_b32 v92, v93, v236, 0 bitop3:0xba\n"
"v_xor_b32 v93, s80, v17\n"
"v_bitop3_b32 v93, v19, s81, v93 bitop3:0xbe\n"
"v_bitop3_b32 v92, v93, v237, v92 bitop3:0xba\n"
"v_xor_b32 v93, s80, v193\n"
"v_bitop3_b32 v93, v195, s81, v93 bitop3:0xbe\n"
"v_bitop3_b32 v92, v93, v238, v92 bitop3:0xba\n"
"v_xor_b32 v93, s80, v189\n"
"v_bitop3_b32 v93, v191, s81, v93 bitop3:0xbe\n"
"v_bitop3_b32 v92, v93, v239, v92 bitop3:0xba\n"
"v_cmp_ne_u32 vcc, 0, v92\n"
"s_nop 1\n"
"s_cmp_lg_u64 vcc, 0\n"
"s_cbranch_scc1 SLOWBL0_%=\n"
"s_branch BACKBL0_%=\n"
"SLOWBX0_%=:\n"
"s_nop 7\n"
"s_nop 7\n"
"s_add_u32 s17, s17, 1\n"
"s_cmp_gt_u32 s17, 100000\n"
"s_cbranch_scc1 BACKBX0_%=\n"
"s_sleep 4\n"
"buffer_load_dwordx4 v[136:139], v224, s[48:51], 0 offen sc1\n"
"buffer_load_dwordx4 v[116:119], v224, s[48:51], 0 offen offset:1024 sc1\n"
"buffer_load_dwordx4 v[192:195], v224, s[48:51], 0 offen offset:2048 sc1\n"
"buffer_load_dwordx4 v[196:199], v224, s[48:51], 0 offen offset:3072 sc1\n"
"s_waitcnt vmcnt(0)\n"
"v_xor_b32 v36, s8, v137\n"
"v_bitop3_b32 v36, v139, s9, v36 bitop3:0xbe\n"
"v_bitop3_b32 v36, v117, s8, v36 bitop3:0xbe\n"
"v_bitop3_b32 v36, v119, s9, v36 bitop3:0xbe\n"
"v_bitop3_b32 v36, v193, s8, v36 bitop3:0xbe\n"
"v_bitop3_b32 v36, v195, s9, v36 bitop3:0xbe\n"
"v_bitop3_b32 v36, v197, s8, v36 bitop3:0xbe\n"
"v_bitop3_b32 v36, v199, s9, v36 bitop3:0xbe\n"
"v_cmp_ne_u32 vcc, 0, v36\n"
"s_nop 1\n"
"s_cmp_lg_u64 vcc, 0\n"
"s_cbranch_scc1 SLOWBX0_%=\n"
"s_branch BACKBX0_%=\n"
"SLOWBJW_%=:\n"
"s_nop 7\n"
"s_nop 7\n"
"s_add_u32 s17, s17, 1\n"
"s_cmp_gt_u32 s17, 100000\n"
"s_cbranch_scc1 BACKBJW_%=\n"
"s_sleep 4\n"
"buffer_load_dwordx2 v[216:217], v229, s[48:51], 0 offen sc1\n"
"s_waitcnt vmcnt(0)\n"
"v_xor_b32 v203, s80, v216\n"
"v_bitop3_b32 v203, v217, s81, v203 bitop3:0xbe\n"
"v_cmp_ne_u32 vcc, 0, v203\n"
"s_nop 1\n"
"s_cmp_lg_u64 vcc, 0\n"
"s_cbranch_scc1 SLOWBJW_%=\n"
"s_branch BACKBJW_%=\n"
"SLOWBJC_%=:\n"
"s_nop 7\n"
"s_nop 7\n"
"s_add_u32 s17, s17, 1\n"
"s_cmp_gt_u32 s17, 100000\n"
"s_cbranch_scc1 BACKBJC_%=\n"
"s_sleep 4\n"
"buffer_load_dwordx2 v[92:93], v189, s[48:51], 0 offen sc1\n"
"s_waitcnt vmcnt(0)\n"
"v_xor_b32 v254, s80, v92\n"
"v_bitop3_b32 v254, v93, s81, v254 bitop3:0xbe\n"
"v_cmp_ne_u32 vcc, 0, v254\n"
"s_nop 1\n"
"s_cmp_lg_u64 vcc, 0\n"
"s_cbranch_scc1 SLOWBJC_%=\n"
"s_branch BACKBJC_%=\n"
"SLOWBH0_%=:\n"
"s_nop 7\n"
"s_nop 7\n"
"s_add_u32 s17, s17, 1\n"
"s_cmp_gt_u32 s17, 100000\n"
"s_cbranch_scc1 BACKBH0_%=\n"
"s_sleep 4\n"
"buffer_load_dwordx2 v[60:61], v189, s[48:51], 0 offen sc1\n"
"s_waitcnt vmcnt(0)\n"
"v_subrev_u32 v62, s16, v60\n"
"v_lshrrev_b32 v62, 1, v62\n"
"v_bitop3_b32 v62, v61, s81, v62 bitop3:0xbe\n"
"v_cmp_ne_u32 vcc, 0, v62\n"
"s_nop 1\n"
"s_cmp_lg_u32 vcc_lo, 0\n"
"s_cbranch_scc1 SLOWBH0_%=\n"
"s_branch BACKBH0_%=\n"
"SLOWBH3_%=:\n"
"s_nop 7\n"
"s_nop 7\n"
"s_add_u32 s17, s17, 1\n"
"s_cmp_gt_u32 s17, 100000\n"
"s_cbranch_scc1 BACKBH3_%=\n"
"s_sleep 4\n"
"buffer_load_dwordx2 v[136:137], v189, s[48:51], 0 offen sc1\n"
"s_waitcnt vmcnt(0)\n"
"v_subrev_u32 v64, s16, v136\n"
"v_lshrrev_b32 v64, 1, v64\n"
"v_bitop3_b32 v64, v137, s81, v64 bitop3:0xbe\n"
"v_cmp_ne_u32 vcc, 0, v64\n"
"s_nop 1\n"
"s_cmp_lg_u32 vcc_hi, 0\n"
"s_cbranch_scc1 SLOWBH3_%=\n"
"s_branch BACKBH3_%=\n"
"SLOWBH1_%=:\n"
"s_nop 7\n"
"s_nop 7\n"
"s_add_u32 s17, s17, 1\n"
"s_cmp_gt_u32 s17, 100000\n"
"s_cbranch_scc1 BACKBH1_%=\n"
"s_sleep 4\n"
"buffer_load_dwordx2 v[180:181], v189, s[48:51], 0 offen sc1\n"
"s_waitcnt vmcnt(0)\n"
"v_subrev_u32 v182, s16, v180\n"
"v_xor_b32 v182, 1, v182\n"
"v_bitop3_b32 v182, v181, s81, v182 bitop3:0xbe\n"
"v_cmp_ne_u32 vcc, 0, v182\n"
"s_nop 1\n"
"s_cmp_lg_u64 vcc, 0\n"
"s_cbranch_scc1 SLOWBH1_%=\n"
"s_branch BACKBH1_%=\n"
"SLOWBY_%=:\n"
"s_nop 7\n"
"s_nop 7\n"
"s_add_u32 s17, s17, 1\n"
"s_cmp_gt_u32 s17, 100000\n"
"s_cbranch_scc1 BACKBY_%=\n"
"s_sleep 4\n"
"buffer_load_dwordx4 v[76:79], v218, s[48:51], s60 offen sc1\n"
"buffer_load_dwordx4 v[72:75], v218, s[48:51], s60 offen offset:1024 sc1\n"
"buffer_load_dwordx4 v[64:67], v218, s[48:51], s60 offen offset:2048 sc1\n"
"buffer_load_dwordx4 v[60:63], v218, s[48:51], s60 offen offset:3072 sc1\n"
"buffer_load_dwordx4 v[68:71], v218, s[48:51], s62 offen sc1\n"
"s_waitcnt vmcnt(0)\n"
"v_xor_b32 v0, s80, v77\n"
"v_bitop3_b32 v0, v79, s81, v0 bitop3:0xbe\n"
"v_bitop3_b32 v0, v73, s80, v0 bitop3:0xbe\n"
"v_bitop3_b32 v0, v75, s81, v0 bitop3:0xbe\n"
"v_bitop3_b32 v0, v65, s80, v0 bitop3:0xbe\n"
"v_bitop3_b32 v0, v67, s81, v0 bitop3:0xbe\n"
"v_bitop3_b32 v0, v61, s80, v0 bitop3:0xbe\n"
"v_bitop3_b32 v0, v63, s81, v0 bitop3:0xbe\n"
"v_bitop3_b32 v0, v69, s80, v0 bitop3:0xbe\n"
"v_bitop3_b32 v0, v71, s81, v0 bitop3:0xbe\n"
"v_cmp_ne_u32 vcc, 0, v0\n"
"s_nop 1\n"
"s_cmp_lg_u64 vcc, 0\n"
"s_cbranch_scc1 SLOWBY_%=\n"
"s_branch BACKBY_%=\n"
"KEND_%=:\n"
  :: "s"(__builtin_amdgcn_kernarg_segment_ptr()), "s"(blockIdx.x), "s"(wid), "s"((unsigned)(size_t)lds) : "v0","v1","v2","v3","v4","v5","v6","v7","v8","v9","v10","v11","v12","v13","v14","v15","v16","v17","v18","v19","v20","v21","v22","v23","v24","v25","v26","v27","v28","v29","v30","v31","v32","v33","v34","v35","v36","v37","v38","v39","v40","v41","v42","v43","v44","v45","v46","v47","v48","v49","v50","v51","v52","v53","v54","v55","v56","v57","v58","v59","v60","v61","v62","v63","v64","v65","v66","v67","v68","v69","v70","v71","v72","v73","v74","v75","v76","v77","v78","v79","v80","v81","v82","v83","v84","v85","v86","v87","v88","v89","v90","v91","v92","v93","v94","v95","v96","v97","v98","v99","v100","v101","v102","v103","v104","v105","v106","v107","v108","v109","v110","v111","v112","v113","v114","v115","v116","v117","v118","v119","v120","v121","v122","v123","v124","v125","v126","v127","v128","v129","v130","v131","v132","v133","v134","v135","v136","v137","v138","v139","v140","v141","v142","v143","v144","v145","v146","v147","v148","v149","v150","v151","v152","v153","v154","v155","v156","v157","v158","v159","v160","v161","v162","v163","v164","v165","v166","v167","v168","v169","v170","v171","v172","v173","v174","v175","v176","v177","v178","v179","v180","v181","v182","v183","v184","v185","v186","v187","v188","v189","v190","v191","v192","v193","v194","v195","v196","v197","v198","v199","v200","v201","v202","v203","v204","v205","v206","v207","v208","v209","v210","v211","v212","v213","v214","v215","v216","v217","v218","v219","v220","v221","v222","v223","v224","v225","v226","v227","v228","v229","v230","v231","v232","v233","v234","v235","v236","v237","v238","v239","v240","v241","v242","v243","v244","v245","v246","v247","v248","v249","v250","v251","v252","v253","v254","v255","a0","a1","a2","a3","a4","a5","a6","a7","a8","a9","a10","a11","a12","a13","a14","a15","a16","a17","a18","a19","a20","a21","a22","a23","a24","a25","a26","a27","a28","a29","a30","a31","a32","a33","a34","a35","a36","a37","a38","a39","a40","a41","a42","a43","a44","a45","a46","a47","a48","a49","a50","a51","a52","a53","a54","a55","a56","a57","a58","a59","a60","a61","a62","a63","a64","a65","a66","a67","a68","a69","a70","a71","a72","a73","a74","a75","a76","a77","a78","a79","a80","a81","a82","a83","a84","a85","a86","a87","a88","a89","a90","a91","a92","a93","a94","a95","a96","a97","a98","a99","a100","a101","a102","a103","a104","a105","a106","a107","a108","a109","a110","a111","a112","a113","a114","a115","a116","a117","a118","a119","a120","a121","a122","a123","a124","a125","a126","a127","a128","a129","a130","a131","a132","a133","a134","a135","a136","a137","a138","a139","a140","a141","a142","a143","a144","a145","a146","a147","a148","a149","a150","a151","a152","a153","a154","a155","a156","a157","a158","a159","a160","a161","a162","a163","a164","a165","a166","a167","a168","a169","a170","a171","a172","a173","a174","a175","a176","a177","a178","a179","a180","a181","a182","a183","a184","a185","a186","a187","a188","a189","a190","a191","a192","a193","a194","a195","a196","a197","a198","a199","a200","a201","a202","a203","a204","a205","a206","a207","a208","a209","a210","a211","a212","a213","a214","a215","a216","a217","a218","a219","a220","a221","a222","a223","a224","a225","a226","a227","a228","a229","a230","a231","a232","a233","a234","a235","a236","a237","a238","a239","a240","a241","a242","a243","a244","a245","a246","a247","a248","a249","a250","a251","a252","a253","a254","a255","s8","s9","s10","s11","s12","s13","s14","s15","s16","s17","s18","s19","s20","s21","s22","s23","s24","s25","s26","s27","s28","s29","s30","s31","s33","s34","s35","s36","s37","s38","s39","s40","s41","s42","s43","s44","s45","s46","s47","s48","s49","s50","s51","s52","s53","s54","s55","s56","s57","s58","s59","s60","s61","s62","s63","s64","s65","s66","s67","s68","s69","s70","s71","s72","s73","s74","s75","s76","s77","s78","s79","s80","s81","s82","s83","s84","s85","s86","s87","s88","s89","s90","s91","s92","s93","s94","s95","s96","s97","s98","s99","vcc","scc","memory");
}
extern "C" void kernel_launch(void* const* d_in, const int* in_sizes, int n_in, void* d_out, int out_size, void* d_ws, size_t ws_size, hipStream_t stream) {
  KArgs a;
  a.p[0]=d_in[0]; a.p[1]=d_in[1]; a.p[2]=d_in[2]; a.p[3]=d_in[3]; a.p[4]=d_in[4]; a.p[5]=d_in[5]; a.p[6]=d_in[6];
  a.p[7]=d_in[7]; a.p[8]=d_in[8]; a.p[9]=d_in[10]; a.p[10]=d_in[11]; a.p[11]=d_in[12]; a.p[12]=d_out; a.p[13]=d_ws;
  fused_main<<<256,256,0,stream>>>(a);
}
```
